```python
import torch

def _softmax(x, axis=-1):
    return torch.softmax(x, dim=axis)
def _gelu(x):
    return torch.nn.functional.gelu(x, approximate="tanh")
def _silu(x):
    return torch.nn.functional.silu(x)
def _sigmoid(x):
    return torch.sigmoid(x)
def _layernorm(x, axis=-1, eps=1e-5):
    m = x.mean(dim=axis, keepdim=True)
    v = x.var(dim=axis, keepdim=True, unbiased=False)
    return (x - m) / torch.sqrt(v + eps)
def _scatter_add(tgt, idx, src, axis=0):
    return tgt.clone().index_add_(axis, idx, src)
def _segment_sum(src, seg, n):
    out = torch.zeros((n,) + src.shape[1:], dtype=src.dtype)
    return out.index_add_(0, seg, src)
def _pad(x, pw):
    flat = tuple(v for pair in reversed(pw) for v in pair)
    return torch.nn.functional.pad(x, flat)

kernel_name = 'recurrent_scan__sliding_block_cached__glu_gated_glu__prompt'

def setup_inputs():

    _w = (lambda a: (lambda t: t.to(torch.bfloat16) if t.is_floating_point() else t)(torch.as_tensor(a)))
    def _A_setup():
        B, Sq, D = 1, 2048, 512
        Hq, Hkv, Sk, Dh = 32, 32, 2048, 128
        assert Hq % Hkv == 0, (Hq, Hkv)
        return {
            "x": _w((lambda *s: torch.randn(*s, dtype=torch.bfloat16) * 0.05)(B, Sq, D)),
            "k_cache": _w((lambda *s: torch.randn(*s, dtype=torch.bfloat16) * 0.8)(B, Hkv, Sk, Dh)),
            "v_cache": _w((lambda *s: torch.randn(*s, dtype=torch.bfloat16) * 0.2)(B, Hkv, Sk, Dh)),
            "wq": _w((lambda *s: torch.randn(*s, dtype=torch.bfloat16) * 0.8)(D, Hq * Dh) * (D ** -0.5)),
            "wo": _w((lambda *s: torch.randn(*s, dtype=torch.bfloat16) * 0.2)(Hq * Dh, D) * ((Hq * Dh) ** -0.5)),
        }
    _A_in = _A_setup()

    def _B_setup():
        B, S, D, F = 1, 2048, 512, 1376
        return {
            "x": _w((lambda *s: torch.randn(*s, dtype=torch.bfloat16) * 0.05)(B, S, D)),
            "w_gate": _w((lambda *s: torch.randn(*s, dtype=torch.bfloat16) * 0.2)(D, F)),
            "w_up": _w((lambda *s: torch.randn(*s, dtype=torch.bfloat16) * 0.1)(D, F)),
            "w_down": _w((lambda *s: torch.randn(*s, dtype=torch.bfloat16) * 0.1)(F, D)),
        }
    _B_in = _B_setup()

    return {
        "A_x": _A_in["x"],
        "A_k_cache": _A_in["k_cache"],
        "A_v_cache": _A_in["v_cache"],
        "A_wq": _A_in["wq"],
        "A_wo": _A_in["wo"],
        "B_x": _B_in["x"],
        "B_w_gate": _B_in["w_gate"],
        "B_w_up": _B_in["w_up"],
        "B_w_down": _B_in["w_down"],
    }

def reference(A_x, A_k_cache, A_v_cache, A_wq, A_wo, B_x, B_w_gate, B_w_up, B_w_down):
    def _A_ref(x, k_cache, v_cache, wq, wo):
        B, Sq, D = x.shape
        Hq, Dh = 32, 128
        Hkv, Sk = k_cache.shape[1], k_cache.shape[2]
        W = min(4096, max(256, Sk // 4))
        rep = Hq // Hkv
        q = (lambda x, *d: x.permute(*d))((x @ wq).reshape(B, Sq, Hq, Dh), 0, 2, 1, 3)
        k = (lambda x, n, axis: torch.repeat_interleave(x, n, dim=axis))(k_cache[:, :, Sk - W:, :], rep, axis=1)
        v = (lambda x, n, axis: torch.repeat_interleave(x, n, dim=axis))(v_cache[:, :, Sk - W:, :], rep, axis=1)
        scores = torch.einsum("bhqd,bhkd->bhqk", q, k) * (Dh ** -0.5)
        p = _softmax(scores, axis=-1)
        o = torch.einsum("bhqk,bhkd->bhqd", p, v)
        return (lambda x, *d: x.permute(*d))(o, 0, 2, 1, 3).reshape(B, Sq, Hq * Dh) @ wo

    def _B_ref(x, w_gate, w_up, w_down):
        g = torch.einsum("bsd,df->bsf", x, w_gate)
        u = torch.einsum("bsd,df->bsf", x, w_up)
        h = _sigmoid(g) * u
        return torch.einsum("bsf,fd->bsd", h, w_down)

    x = A_x
    xn = _layernorm(x, axis=-1)
    s = _A_ref(xn, A_k_cache, A_v_cache, A_wq, A_wo)
    g = _silu(_B_ref(xn, B_w_gate, B_w_up, B_w_down))
    return x + s * g
```

```cpp
#include <hip/hip_runtime.h>
#include <hip/hip_bf16.h>
#include <stdint.h>
typedef unsigned short u16;
__device__ __forceinline__ float bf2f(u16 v){ return __uint_as_float(((unsigned)v)<<16); }
__device__ __forceinline__ u16 f2bf(float f){ unsigned u=__float_as_uint(f); u += 0x7fffu + ((u>>16)&1u); return (u16)(u>>16); }

__global__ void ln_k(const u16* x, float* xn){
  int row=blockIdx.x; int t=threadIdx.x;
  float v[8]; float s=0;
  for(int i=0;i<8;i++){ v[i]=bf2f(x[row*512+t*8+i]); s+=v[i]; }
  for(int o=32;o>0;o>>=1) s+=__shfl_xor(s,o);
  float m=s/512.f; float q=0;
  for(int i=0;i<8;i++){ float d=v[i]-m; q+=d*d; }
  for(int o=32;o>0;o>>=1) q+=__shfl_xor(q,o);
  float r=rsqrtf(q/512.f+1e-5f);
  for(int i=0;i<8;i++) xn[row*512+t*8+i]=(v[i]-m)*r;
}
__global__ void gemm_k(const float* A, const u16* B, float* C, int M,int N,int K){
  __shared__ float As[16][17]; __shared__ float Bs[16][17];
  int tx=threadIdx.x&15, ty=threadIdx.x>>4;
  int r=blockIdx.y*16+ty, c=blockIdx.x*16+tx; float acc=0;
  for(int k0=0;k0<K;k0+=16){
    As[ty][tx]=A[r*K+k0+tx]; Bs[ty][tx]=bf2f(B[(k0+ty)*N+c]);
    __syncthreads();
    for(int k=0;k<16;k++) acc+=As[ty][k]*Bs[k][tx];
    __syncthreads();
  }
  C[r*N+c]=acc;
}
__global__ void attn_k(const float* q, const u16* kc, const u16* vc, float* o){
  __shared__ float qs[128]; __shared__ float p[512]; __shared__ float red[128];
  int row=blockIdx.x, h=blockIdx.y, t=threadIdx.x;
  qs[t]=q[row*4096+h*128+t]; __syncthreads();
  const u16* K=kc+((size_t)h*2048+1536)*128; const u16* V=vc+((size_t)h*2048+1536)*128;
  float mx=-1e30f;
  for(int i=0;i<4;i++){ int key=t+128*i; float s=0; for(int d=0;d<128;d++) s+=qs[d]*bf2f(K[key*128+d]); s*=0.08838834764831845f; p[key]=s; mx=fmaxf(mx,s);}
  red[t]=mx; __syncthreads();
  for(int o2=64;o2>0;o2>>=1){ if(t<o2) red[t]=fmaxf(red[t],red[t+o2]); __syncthreads(); }
  mx=red[0]; __syncthreads();
  float sm=0; for(int i=0;i<4;i++){ int key=t+128*i; float e=__expf(p[key]-mx); p[key]=e; sm+=e; }
  red[t]=sm; __syncthreads();
  for(int o2=64;o2>0;o2>>=1){ if(t<o2) red[t]+=red[t+o2]; __syncthreads(); }
  sm=red[0];
  float acc=0; for(int k=0;k<512;k++) acc+=p[k]*bf2f(V[k*128+t]);
  o[row*4096+h*128+t]=acc/sm;
}
__global__ void glu_k(const float* g, const float* u, float* h, int n){ int i=blockIdx.x*256+threadIdx.x; if(i<n) h[i]=u[i]/(1.f+__expf(-g[i])); }
__global__ void fin_k(const u16* x, const float* s, const float* d, u16* out, int n){ int i=blockIdx.x*256+threadIdx.x; if(i<n){ float z=d[i]; float g=z/(1.f+__expf(-z)); out[i]=f2bf(bf2f(x[i])+s[i]*g);} }

extern "C" void kernel_launch(void* const* d_in, const int* in_sizes, int n_in, void* d_out, int out_size, void* d_ws, size_t ws_size, hipStream_t stream){
  const u16* x=(const u16*)d_in[0]; const u16* kc=(const u16*)d_in[1]; const u16* vc=(const u16*)d_in[2];
  const u16* wq=(const u16*)d_in[3]; const u16* wo=(const u16*)d_in[4];
  const u16* wg=(const u16*)d_in[6]; const u16* wu=(const u16*)d_in[7]; const u16* wd=(const u16*)d_in[8];
  float* ws=(float*)d_ws;
  float* xn=ws; float* q=xn+2048*512; float* o=q+2048*4096; float* g=o+2048*4096; float* u=g+2048*1376; float* h=u+2048*1376; float* s=h+2048*1376; float* dn=s+2048*512;
  ln_k<<<2048,64,0,stream>>>(x,xn);
  gemm_k<<<dim3(4096/16,2048/16),256,0,stream>>>(xn,wq,q,2048,4096,512);
  gemm_k<<<dim3(1376/16,2048/16),256,0,stream>>>(xn,wg,g,2048,1376,512);
  gemm_k<<<dim3(1376/16,2048/16),256,0,stream>>>(xn,wu,u,2048,1376,512);
  attn_k<<<dim3(2048,32),128,0,stream>>>(q,kc,vc,o);
  glu_k<<<(2048*1376+255)/256,256,0,stream>>>(g,u,h,2048*1376);
  gemm_k<<<dim3(512/16,2048/16),256,0,stream>>>(o,wo,s,2048,512,4096);
  gemm_k<<<dim3(512/16,2048/16),256,0,stream>>>(h,wd,dn,2048,512,1376);
  fin_k<<<(2048*512+255)/256,256,0,stream>>>(x,s,dn,(u16*)d_out,2048*512);
}
```

```cpp
#include <hip/hip_runtime.h>
#include <stdint.h>
#define WS_STAT_OFF 135168
#define WS_CS_OFF 151552
#define WS_XN_OFF 413696
#define WS_O_OFF 413696
#define WS_H_OFF 17190912
#define WS_PART_OFF 22827008
#define WS_DBG_OFF 41701376
extern "C" __global__ __launch_bounds__(256, 1) void fused_kernel(const void* x, const void* kc, const void* vc, const void* wq, const void* wo,
    const void* wg, const void* wu, const void* wd, void* out, void* ws) {
  const void __attribute__((address_space(4)))* ka = (const void __attribute__((address_space(4)))*)__builtin_amdgcn_kernarg_segment_ptr();
  unsigned bid = blockIdx.x;
  unsigned w = __builtin_amdgcn_readfirstlane(threadIdx.x >> 6);
  asm volatile(
    "s_load_dwordx16 s[8:23], %0, 0x0\n"
    "s_load_dwordx4 s[24:27], %0, 0x40\n"
    "s_mov_b32 s60, %2\n"
    "s_and_b32 s63, %1, 7\n"
    "s_lshr_b32 s64, %1, 3\n"
    "s_lshr_b32 s81, s63, 1\n"
    "s_and_b32 s79, s63, 1\n"
    "s_and_b32 s61, s64, 7\n"
    "s_lshl_b32 s65, s81, 3\n"
    "s_add_u32 s61, s61, s65\n"
    "s_lshr_b32 s62, s64, 3\n"
    "s_lshl_b32 s73, s62, 15\n"
    "s_lshl_b32 s65, s79, 2\n"
    "s_add_u32 s62, s62, s65\n"
    "s_lshr_b32 s81, s61, 2\n"
    "s_lshr_b32 s79, s61, 3\n"
    "s_and_b32 s80, s61, 3\n"
    "v_mbcnt_lo_u32_b32 v238, -1, 0\n"
    "v_mbcnt_hi_u32_b32 v238, -1, v238\n"
    "s_getpc_b64 s[30:31]\n"
    "LPC:\n"
    "s_lshl_b32 s65, s64, 2\n"
    "s_add_u32 s65, s65, s60\n"
    "s_lshl_b32 s65, s65, 9\n"
    "v_min_u32 v254, 3, v238\n"
    "v_lshlrev_b32 v254, 7, v254\n"
    "v_add_u32 v254, s65, v254\n"
    "v_min_u32 v254, LEND-LPC-4, v254\n"
    "global_load_dword a255, v254, s[30:31]\n"
    "v_and_b32 v254, 7, v238\n"
    "v_lshrrev_b32 v239, 3, v238\n"
    "v_lshrrev_b32 v128, 4, v238\n"
    "v_bfe_u32 v129, v238, 2, 3\n"
    "v_and_b32 v130, 3, v238\n"
    "v_lshlrev_b32 v130, 4, v130\n"
    "v_lshrrev_b32 v131, 5, v238\n"
    "v_lshl_add_u32 v130, v131, 6, v130\n"
    "v_lshl_add_u32 v131, v129, 13, v130\n"
    "v_add_u32 v226, 4096, v131\n"
    "v_add_u32 v227, 3200, v131\n"
    "v_xor_b32 v129, 0, v128\n"
    "v_xor_b32 v129, v129, v254\n"
    "v_lshlrev_b32 v129, 4, v129\n"
    "v_lshl_add_u32 v224, v239, 10, v129\n"
    "v_xor_b32 v129, 4, v128\n"
    "v_xor_b32 v129, v129, v254\n"
    "v_lshlrev_b32 v129, 4, v129\n"
    "v_lshl_add_u32 v225, v239, 10, v129\n"
    "s_mov_b32 s97, s62\n"
    "s_mov_b32 s99, s97\n"
    "s_lshl_b32 s66, s97, 7\n"
    "s_lshl_b32 s67, s97, 19\n"
    "s_mul_i32 s76, s97, 176128\n"
    "s_lshl_b32 s71, s60, 13\n"
    "s_mul_i32 s72, s60, 4112\n"
    "s_mul_i32 s77, s60, 3072\n"
    "s_lshl_b32 s63, s62, 18\n"
    "s_lshl_b32 s64, s61, 13\n"
    "s_add_u32 s65, s63, s64\n"
    "s_lshl_b32 s64, s60, 8\n"
    "s_add_u32 s65, s65, s64\n"
    "v_lshlrev_b32 v158, 10, v254\n"
    "v_lshl_add_u32 v158, v239, 4, v158\n"
    "v_add_u32 v158, s65, v158\n"
    "v_mov_b32 v132, 0\n"
    "s_lshl_b32 s64, s60, 16\n"
    "s_add_u32 s63, s63, s64\n"
    "s_lshl_b32 s64, s61, 8\n"
    "s_lshl_b32 s65, s60, 17\n"
    "s_add_u32 s64, s64, s65\n"
    "s_waitcnt lgkmcnt(0)\n"
    "global_load_dwordx4 v[136:139], v158, s[8:9]\n"
    "global_load_dwordx4 v[140:143], v158, s[8:9] offset:128\n"
    "global_load_dwordx2 v[134:135], v132, s[26:27] sc1\n"
    "s_add_u32 s40, s14, s64\n"
    "s_addc_u32 s41, s15, 0\n"
    "s_sub_u32 s40, s40, 4096\n"
    "s_subb_u32 s41, s41, 0\n"
    "s_and_b32 s41, s41, 0xffff\n"
    "s_sub_u32 s42, 4198400, s64\n"
    "s_mov_b32 s43, 0x20000\n"
    "s_add_u32 s36, s8, s63\n"
    "s_addc_u32 s37, s9, 0\n"
    "s_and_b32 s37, s37, 0xffff\n"
    "s_sub_u32 s38, 2097152, s63\n"
    "s_mov_b32 s39, 0x20000\n"
    "s_add_u32 m0, s72, 73216\n"
    "s_mul_i32 s65, s61, 86\n"
    "s_add_u32 s63, s65, 86\n"
    "s_lshr_b32 s65, s65, 5\n"
    "s_lshr_b32 s63, s63, 5\n"
    "s_sub_u32 s70, s63, s65\n"
    "buffer_load_dwordx4 v226, s[40:43], s67 offen lds\n"
    "v_mov_b32 v131, 0x40000000\n"
    "v_add_u32 v129, 0, v128\n"
    "v_mul_u32_u24 v130, 43, v129\n"
    "v_lshrrev_b32 v130, 7, v130\n"
    "v_mul_u32_u24 v251, 3, v130\n"
    "buffer_load_dwordx4 v227, s[40:43], s67 offen offset:1024 lds\n"
    "v_sub_u32 v129, v129, v251\n"
    "v_cmp_le_u32 vcc, s70, v129\n"
    "v_and_b32 v251, 1, v238\n"
    "v_lshl_add_u32 v129, v129, 1, v251\n"
    "v_lshlrev_b32 v129, 4, v129\n"
    "s_add_u32 s90, s67, 63480\n"
    "v_add_u32 v129, 2048, v129\n"
    "v_and_b32 v251, 1, v130\n"
    "v_lshlrev_b32 v251, 2, v251\n"
    "v_lshl_add_u32 v251, v130, 3, v251\n"
    "v_bfe_u32 v130, v238, 1, 3\n"
    "buffer_load_dwordx4 v226, s[40:43], s90 offen offset:2056 lds\n"
    "v_xor_b32 v251, v251, v130\n"
    "v_mul_u32_u24 v251, 2752, v251\n"
    "v_add_u32 v251, v251, v129\n"
    "v_cndmask_b32 v251, v251, v131, vcc\n"
    "v_add_u32 v129, 4, v128\n"
    "buffer_load_dwordx4 v227, s[40:43], s90 offen offset:3080 lds\n"
    "v_mul_u32_u24 v130, 43, v129\n"
    "v_lshrrev_b32 v130, 7, v130\n"
    "v_mul_u32_u24 v252, 3, v130\n"
    "v_sub_u32 v129, v129, v252\n"
    "v_cmp_le_u32 vcc, s70, v129\n"
    "s_add_u32 m0, s71, 40448\n"
    "v_and_b32 v252, 1, v238\n"
    "v_lshl_add_u32 v129, v129, 1, v252\n"
    "v_lshlrev_b32 v129, 4, v129\n"
    "v_add_u32 v129, 1024, v129\n"
    "v_and_b32 v252, 1, v130\n"
    "buffer_load_dwordx4 v224, s[36:39], s66 offen lds\n"
    "v_lshlrev_b32 v252, 2, v252\n"
    "v_lshl_add_u32 v252, v130, 3, v252\n"
    "v_bfe_u32 v130, v238, 1, 3\n"
    "v_xor_b32 v252, v252, v130\n"
    "v_mul_u32_u24 v252, 2752, v252\n"
    "s_add_u32 s91, s66, 7168\n"
    "v_add_u32 v252, v252, v129\n"
    "v_cndmask_b32 v252, v252, v131, vcc\n"
    "v_add_u32 v129, 8, v128\n"
    "v_mul_u32_u24 v130, 43, v129\n"
    "v_lshrrev_b32 v130, 7, v130\n"
    "buffer_load_dwordx4 v225, s[36:39], s91 offen offset:1024 lds\n"
    "v_mul_u32_u24 v253, 3, v130\n"
    "v_sub_u32 v129, v129, v253\n"
    "v_cmp_le_u32 vcc, s70, v129\n"
    "v_and_b32 v253, 1, v238\n"
    "v_lshl_add_u32 v129, v129, 1, v253\n"
    "s_add_u32 s90, s66, 14336\n"
    "v_lshlrev_b32 v129, 4, v129\n"
    "v_add_u32 v129, 0, v129\n"
    "v_and_b32 v253, 1, v130\n"
    "v_lshlrev_b32 v253, 2, v253\n"
    "v_lshl_add_u32 v253, v130, 3, v253\n"
    "buffer_load_dwordx4 v224, s[36:39], s90 offen offset:2048 lds\n"
    "v_bfe_u32 v130, v238, 1, 3\n"
    "v_xor_b32 v253, v253, v130\n"
    "v_mul_u32_u24 v253, 2752, v253\n"
    "v_add_u32 v253, v253, v129\n"
    "v_cndmask_b32 v253, v253, v131, vcc\n"
    "s_add_u32 s91, s66, 21504\n"
    "s_mul_i32 s65, s61, 86\n"
    "s_lshr_b32 s65, s65, 5\n"
    "s_lshl_b32 s65, s65, 5\n"
    "s_and_b32 s63, s60, 1\n"
    "s_mul_i32 s63, s63, 88064\n"
    "buffer_load_dwordx4 v225, s[36:39], s91 offen offset:3072 lds\n"
    "s_add_u32 s65, s65, s63\n"
    "s_cmp_lt_u32 s60, 2\n"
    "s_cselect_b32 s18, s18, s20\n"
    "s_cselect_b32 s19, s19, s21\n"
    "s_add_u32 s52, s18, s65\n"
    "s_addc_u32 s53, s19, 0\n"
    "s_sub_u32 s52, s52, 2048\n"
    "s_subb_u32 s53, s53, 0\n"
    "s_and_b32 s53, s53, 0xffff\n"
    "s_sub_u32 s54, 1411072, s65\n"
    "s_add_u32 m0, s71, 44544\n"
    "s_mov_b32 s55, 0x20000\n"
    "v_and_b32 v132, 15, v238\n"
    "v_lshrrev_b32 v133, 1, v132\n"
    "v_xor_b32 v133, v133, v128\n"
    "v_xor_b32 v129, 0, v133\n"
    "s_add_u32 s90, s66, 32768\n"
    "v_lshlrev_b32 v129, 4, v129\n"
    "v_lshl_add_u32 v129, v132, 7, v129\n"
    "v_add_u32 v129, s71, v129\n"
    "v_add_u32 v228, 40448, v129\n"
    "v_add_u32 v230, 102144, v129\n"
    "buffer_load_dwordx4 v224, s[36:39], s90 offen lds\n"
    "v_xor_b32 v129, 4, v133\n"
    "v_lshlrev_b32 v129, 4, v129\n"
    "v_lshl_add_u32 v129, v132, 7, v129\n"
    "v_add_u32 v129, s71, v129\n"
    "v_add_u32 v229, 40448, v129\n"
    "s_add_u32 s91, s66, 39936\n"
    "v_add_u32 v231, 102144, v129\n"
    "v_bfe_u32 v129, v238, 2, 2\n"
    "v_and_b32 v130, 3, v238\n"
    "v_lshlrev_b32 v131, 4, v130\n"
    "v_lshl_add_u32 v131, v129, 6, v131\n"
    "buffer_load_dwordx4 v225, s[36:39], s91 offen offset:1024 lds\n"
    "v_mul_u32_u24 v232, 2056, v128\n"
    "v_add_u32 v131, v131, v232\n"
    "v_add_u32 v232, 73216, v131\n"
    "v_add_u32 v233, 134912, v131\n"
    "v_lshlrev_b32 v130, 3, v130\n"
    "s_add_u32 s90, s66, 47104\n"
    "v_lshl_add_u32 v130, v129, 5, v130\n"
    "v_and_b32 v129, 1, v128\n"
    "v_lshl_add_u32 v244, v129, 7, v130\n"
    "v_mul_u32_u24 v131, 768, v128\n"
    "v_add_u32 v131, v131, v244\n"
    "buffer_load_dwordx4 v224, s[36:39], s90 offen offset:2048 lds\n"
    "v_add_u32 v234, 89856, v131\n"
    "v_xor_b32 v235, 0x80, v234\n"
    "v_add_u32 v236, 151552, v131\n"
    "v_xor_b32 v237, 0x80, v236\n"
    "v_lshlrev_b32 v156, 4, v254\n"
    "s_add_u32 s91, s66, 54272\n"
    "v_add_u32 v156, 32768, v156\n"
    "s_lshl_b32 s63, s60, 2\n"
    "v_add_u32 v157, s63, v156\n"
    "v_lshlrev_b32 v158, 3, v254\n"
    "buffer_load_dwordx4 v225, s[36:39], s91 offen offset:3072 lds\n"
    "s_waitcnt vmcnt(12)\n"
    "v_readfirstlane_b32 s68, v134\n"
    "v_readfirstlane_b32 s69, v135\n"
    "s_add_u32 s68, s68, 1\n"
    "s_addc_u32 s69, s69, 0\n"
    "v_and_b32 v151, 0xffff0000, v143\n"
    "v_lshlrev_b32 v150, 16, v143\n"
    "s_add_u32 m0, s77, 89856\n"
    "v_and_b32 v149, 0xffff0000, v142\n"
    "v_lshlrev_b32 v148, 16, v142\n"
    "v_and_b32 v147, 0xffff0000, v141\n"
    "v_lshlrev_b32 v146, 16, v141\n"
    "buffer_load_dwordx4 v251, s[52:55], s76 offen lds\n"
    "v_and_b32 v145, 0xffff0000, v140\n"
    "v_lshlrev_b32 v144, 16, v140\n"
    "v_and_b32 v143, 0xffff0000, v139\n"
    "v_lshlrev_b32 v142, 16, v139\n"
    "buffer_load_dwordx4 v252, s[52:55], s76 offen offset:1024 lds\n"
    "v_and_b32 v141, 0xffff0000, v138\n"
    "v_lshlrev_b32 v140, 16, v138\n"
    "v_and_b32 v139, 0xffff0000, v137\n"
    "v_lshlrev_b32 v138, 16, v137\n"
    "buffer_load_dwordx4 v253, s[52:55], s76 offen offset:2048 lds\n"
    "v_and_b32 v137, 0xffff0000, v136\n"
    "v_lshlrev_b32 v136, 16, v136\n"
    "v_add_f32 v254, v136, v138\n"
    "v_add_f32 v239, v137, v139\n"
    "s_add_u32 s97, s97, 1\n"
    "v_add_f32 v254, v254, v140\n"
    "v_add_f32 v239, v239, v141\n"
    "v_add_f32 v254, v254, v142\n"
    "v_add_f32 v239, v239, v143\n"
    "s_and_b32 s97, s97, 7\n"
    "v_add_f32 v254, v254, v144\n"
    "v_add_f32 v239, v239, v145\n"
    "v_add_f32 v254, v254, v146\n"
    "v_add_f32 v239, v239, v147\n"
    "s_lshl_b32 s66, s97, 7\n"
    "v_add_f32 v254, v254, v148\n"
    "v_add_f32 v239, v239, v149\n"
    "v_add_f32 v254, v254, v150\n"
    "v_add_f32 v239, v239, v151\n"
    "s_lshl_b32 s67, s97, 19\n"
    "v_add_f32 v254, v254, v239\n"
    "s_nop 1\n"
    "v_add_f32_dpp v254, v254, v254 row_ror:8 row_mask:0xf bank_mask:0xf\n"
    "v_mov_b32 v239, v254\n"
    "s_nop 1\n"
    "v_permlane16_swap_b32 v239, v254\n"
    "s_mul_i32 s76, s97, 176128\n"
    "v_add_f32 v254, v254, v239\n"
    "v_mov_b32 v239, v254\n"
    "s_nop 1\n"
    "v_permlane32_swap_b32 v239, v254\n"
    "v_add_f32 v254, v254, v239\n"
    "ds_write_b32 v157, v254\n"
    "s_add_u32 m0, s72, 134912\n"
    "s_nop 0\n"
    "buffer_load_dwordx4 v226, s[40:43], s67 offen lds\n"
    "buffer_load_dwordx4 v227, s[40:43], s67 offen offset:1024 lds\n"
    "s_add_u32 s90, s67, 63480\n"
    "buffer_load_dwordx4 v226, s[40:43], s90 offen offset:2056 lds\n"
    "buffer_load_dwordx4 v227, s[40:43], s90 offen offset:3080 lds\n"
    "s_waitcnt vmcnt(7)\n"
    "s_waitcnt lgkmcnt(0)\n"
    "s_barrier\n"
    "ds_read_b128 v[160:163], v156\n"
    "ds_read_b128 a[112:115], v228 offset:0\n"
    "ds_read_b128 a[128:131], v229 offset:0\n"
    "ds_read_b64_tr_b16 a[0:1], v232 offset:0\n"
    "ds_read_b64_tr_b16 a[2:3], v232 offset:256\n"
    "ds_read_b64_tr_b16 a[4:5], v232 offset:8224\n"
    "ds_read_b64_tr_b16 a[6:7], v232 offset:8480\n"
    "ds_read_b128 a[116:119], v228 offset:2048\n"
    "ds_read_b128 a[132:135], v229 offset:2048\n"
    "ds_read_b128 a[120:123], v228 offset:4096\n"
    "ds_read_b128 a[136:139], v229 offset:4096\n"
    "ds_read_b128 a[124:127], v228 offset:6144\n"
    "ds_read_b128 a[140:143], v229 offset:6144\n"
    "ds_read_b64_tr_b16 a[8:9], v232 offset:8\n"
    "ds_read_b64_tr_b16 a[10:11], v232 offset:264\n"
    "ds_read_b64_tr_b16 a[12:13], v232 offset:8232\n"
    "ds_read_b64_tr_b16 a[14:15], v232 offset:8488\n"
    "ds_read_b64_tr_b16 a[16:17], v232 offset:512\n"
    "ds_read_b64_tr_b16 a[18:19], v232 offset:768\n"
    "ds_read_b64_tr_b16 a[20:21], v232 offset:8736\n"
    "ds_read_b64_tr_b16 a[22:23], v232 offset:8992\n"
    "ds_read_b64_tr_b16 a[24:25], v232 offset:520\n"
    "ds_read_b64_tr_b16 a[26:27], v232 offset:776\n"
    "ds_read_b64_tr_b16 a[28:29], v232 offset:8744\n"
    "ds_read_b64_tr_b16 a[30:31], v232 offset:9000\n"
    "ds_read_b64_tr_b16 a[32:33], v232 offset:1024\n"
    "ds_read_b64_tr_b16 a[34:35], v232 offset:1280\n"
    "ds_read_b64_tr_b16 a[36:37], v232 offset:9248\n"
    "ds_read_b64_tr_b16 a[38:39], v232 offset:9504\n"
    "ds_read_b64_tr_b16 a[40:41], v232 offset:1032\n"
    "ds_read_b64_tr_b16 a[42:43], v232 offset:1288\n"
    "ds_read_b64_tr_b16 a[44:45], v232 offset:9256\n"
    "ds_read_b64_tr_b16 a[46:47], v232 offset:9512\n"
    "ds_read_b64_tr_b16 a[48:49], v232 offset:1536\n"
    "ds_read_b64_tr_b16 a[50:51], v232 offset:1792\n"
    "ds_read_b64_tr_b16 a[52:53], v232 offset:9760\n"
    "ds_read_b64_tr_b16 a[54:55], v232 offset:10016\n"
    "s_waitcnt lgkmcnt(15)\n"
    "v_mfma_f32_16x16x32_bf16 v[0:3], a[0:3], a[112:115], 0\n"
    "s_add_u32 m0, s71, 102144\n"
    "v_add_f32 v160, v160, v161\n"
    "v_mov_b32 v152, 0x3f803f80\n"
    "v_mfma_f32_16x16x32_bf16 v[0:3], a[4:7], a[128:131], v[0:3]\n"
    "v_cmp_eq_u32 vcc, 0, v132\n"
    "v_add_f32 v162, v162, v163\n"
    "s_nop 0\n"
    "v_mfma_f32_16x16x32_bf16 v[32:35], a[0:3], a[116:119], 0\n"
    "buffer_load_dwordx4 v224, s[36:39], s66 offen lds\n"
    "v_add_f32 v160, v160, v162\n"
    "v_mfma_f32_16x16x32_bf16 v[32:35], a[4:7], a[132:135], v[32:35]\n"
    "s_nop 0\n"
    "v_mul_f32 v164, 0x3b000000, v160\n"
    "v_cndmask_b32 v153, 0, v152, vcc\n"
    "v_mfma_f32_16x16x32_bf16 v[64:67], a[0:3], a[120:123], 0\n"
    "s_add_u32 s91, s66, 7168\n"
    "v_sub_f32 v136, v136, v164\n"
    "v_accvgpr_write_b32 a144, v153\n"
    "v_mfma_f32_16x16x32_bf16 v[64:67], a[4:7], a[136:139], v[64:67]\n"
    "buffer_load_dwordx4 v225, s[36:39], s91 offen offset:1024 lds\n"
    "v_sub_f32 v137, v137, v164\n"
    "v_mfma_f32_16x16x32_bf16 v[96:99], a[0:3], a[124:127], 0\n"
    "v_accvgpr_write_b32 a145, v153\n"
    "v_sub_f32 v138, v138, v164\n"
    "v_accvgpr_write_b32 a146, v153\n"
    "v_mfma_f32_16x16x32_bf16 v[96:99], a[4:7], a[140:143], v[96:99]\n"
    "s_add_u32 s90, s66, 14336\n"
    "v_sub_f32 v139, v139, v164\n"
    "v_cmp_eq_u32 vcc, 1, v132\n"
    "v_mfma_f32_16x16x32_bf16 v[4:7], a[8:11], a[112:115], 0\n"
    "v_sub_f32 v140, v140, v164\n"
    "v_accvgpr_write_b32 a147, v153\n"
    "v_sub_f32 v141, v141, v164\n"
    "s_nop 0\n"
    "v_mfma_f32_16x16x32_bf16 v[4:7], a[12:15], a[128:131], v[4:7]\n"
    "buffer_load_dwordx4 v224, s[36:39], s90 offen offset:2048 lds\n"
    "v_sub_f32 v142, v142, v164\n"
    "v_mfma_f32_16x16x32_bf16 v[36:39], a[8:11], a[116:119], 0\n"
    "v_cndmask_b32 v154, 0, v152, vcc\n"
    "v_sub_f32 v143, v143, v164\n"
    "v_accvgpr_write_b32 a148, v154\n"
    "v_mfma_f32_16x16x32_bf16 v[36:39], a[12:15], a[132:135], v[36:39]\n"
    "s_add_u32 s91, s66, 21504\n"
    "v_sub_f32 v144, v144, v164\n"
    "v_accvgpr_write_b32 a149, v154\n"
    "v_mfma_f32_16x16x32_bf16 v[68:71], a[8:11], a[120:123], 0\n"
    "buffer_load_dwordx4 v225, s[36:39], s91 offen offset:3072 lds\n"
    "v_sub_f32 v145, v145, v164\n"
    "v_mfma_f32_16x16x32_bf16 v[68:71], a[12:15], a[136:139], v[68:71]\n"
    "v_accvgpr_write_b32 a150, v154\n"
    "v_sub_f32 v146, v146, v164\n"
    "v_cmp_eq_u32 vcc, 2, v132\n"
    "v_mfma_f32_16x16x32_bf16 v[100:103], a[8:11], a[124:127], 0\n"
    "s_add_u32 m0, s71, 106240\n"
    "v_sub_f32 v147, v147, v164\n"
    "v_accvgpr_write_b32 a151, v154\n"
    "v_mfma_f32_16x16x32_bf16 v[100:103], a[12:15], a[140:143], v[100:103]\n"
    "s_nop 0\n"
    "s_add_u32 s90, s66, 32768\n"
    "v_sub_f32 v148, v148, v164\n"
    "v_mfma_f32_16x16x32_bf16 v[8:11], a[16:19], a[112:115], 0\n"
    "v_sub_f32 v149, v149, v164\n"
    "v_cndmask_b32 v153, 0, v152, vcc\n"
    "v_sub_f32 v150, v150, v164\n"
    "v_accvgpr_write_b32 a152, v153\n"
    "v_mfma_f32_16x16x32_bf16 v[8:11], a[20:23], a[128:131], v[8:11]\n"
    "buffer_load_dwordx4 v224, s[36:39], s90 offen lds\n"
    "v_sub_f32 v151, v151, v164\n"
    "v_mfma_f32_16x16x32_bf16 v[40:43], a[16:19], a[116:119], 0\n"
    "v_accvgpr_write_b32 a153, v153\n"
    "v_mul_f32 v254, v136, v136\n"
    "v_accvgpr_write_b32 a154, v153\n"
    "v_mfma_f32_16x16x32_bf16 v[40:43], a[20:23], a[132:135], v[40:43]\n"
    "s_add_u32 s91, s66, 39936\n"
    "v_mul_f32 v239, v137, v137\n"
    "v_cmp_eq_u32 vcc, 3, v132\n"
    "v_mfma_f32_16x16x32_bf16 v[72:75], a[16:19], a[120:123], 0\n"
    "buffer_load_dwordx4 v225, s[36:39], s91 offen offset:1024 lds\n"
    "v_fmac_f32 v254, v138, v138\n"
    "v_mfma_f32_16x16x32_bf16 v[72:75], a[20:23], a[136:139], v[72:75]\n"
    "v_accvgpr_write_b32 a155, v153\n"
    "v_fmac_f32 v239, v139, v139\n"
    "s_nop 0\n"
    "v_mfma_f32_16x16x32_bf16 v[104:107], a[16:19], a[124:127], 0\n"
    "s_add_u32 s90, s66, 47104\n"
    "v_fmac_f32 v254, v140, v140\n"
    "v_cndmask_b32 v154, 0, v152, vcc\n"
    "v_mfma_f32_16x16x32_bf16 v[104:107], a[20:23], a[140:143], v[104:107]\n"
    "v_fmac_f32 v239, v141, v141\n"
    "v_accvgpr_write_b32 a156, v154\n"
    "v_accvgpr_write_b32 a157, v154\n"
    "s_waitcnt lgkmcnt(14)\n"
    "v_mfma_f32_16x16x32_bf16 v[12:15], a[24:27], a[112:115], 0\n"
    "buffer_load_dwordx4 v224, s[36:39], s90 offen offset:2048 lds\n"
    "v_fmac_f32 v254, v142, v142\n"
    "s_waitcnt lgkmcnt(12)\n"
    "v_mfma_f32_16x16x32_bf16 v[12:15], a[28:31], a[128:131], v[12:15]\n"
    "v_accvgpr_write_b32 a158, v154\n"
    "v_fmac_f32 v239, v143, v143\n"
    "v_cmp_eq_u32 vcc, 4, v132\n"
    "v_fmac_f32 v254, v144, v144\n"
    "v_mfma_f32_16x16x32_bf16 v[44:47], a[24:27], a[116:119], 0\n"
    "s_add_u32 s91, s66, 54272\n"
    "v_accvgpr_write_b32 a159, v154\n"
    "v_fmac_f32 v239, v145, v145\n"
    "v_mfma_f32_16x16x32_bf16 v[44:47], a[28:31], a[132:135], v[44:47]\n"
    "s_nop 0\n"
    "buffer_load_dwordx4 v225, s[36:39], s91 offen offset:3072 lds\n"
    "v_mfma_f32_16x16x32_bf16 v[76:79], a[24:27], a[120:123], 0\n"
    "v_fmac_f32 v254, v146, v146\n"
    "v_cndmask_b32 v153, 0, v152, vcc\n"
    "v_fmac_f32 v239, v147, v147\n"
    "v_mfma_f32_16x16x32_bf16 v[76:79], a[28:31], a[136:139], v[76:79]\n"
    "s_add_u32 m0, s77, 151552\n"
    "v_accvgpr_write_b32 a160, v153\n"
    "v_fmac_f32 v254, v148, v148\n"
    "v_mfma_f32_16x16x32_bf16 v[108:111], a[24:27], a[124:127], 0\n"
    "v_accvgpr_write_b32 a161, v153\n"
    "v_fmac_f32 v239, v149, v149\n"
    "v_accvgpr_write_b32 a162, v153\n"
    "v_mfma_f32_16x16x32_bf16 v[108:111], a[28:31], a[140:143], v[108:111]\n"
    "buffer_load_dwordx4 v251, s[52:55], s76 offen lds\n"
    "v_fmac_f32 v254, v150, v150\n"
    "s_waitcnt lgkmcnt(10)\n"
    "v_mfma_f32_16x16x32_bf16 v[16:19], a[32:35], a[112:115], 0\n"
    "v_cmp_eq_u32 vcc, 5, v132\n"
    "v_fmac_f32 v239, v151, v151\n"
    "v_accvgpr_write_b32 a163, v153\n"
    "s_waitcnt lgkmcnt(8)\n"
    "v_mfma_f32_16x16x32_bf16 v[16:19], a[36:39], a[128:131], v[16:19]\n"
    "buffer_load_dwordx4 v252, s[52:55], s76 offen offset:1024 lds\n"
    "s_nop 0\n"
    "v_add_f32 v254, v254, v239\n"
    "v_mfma_f32_16x16x32_bf16 v[48:51], a[32:35], a[116:119], 0\n"
    "s_nop 0\n"
    "v_add_f32_dpp v254, v254, v254 row_ror:8 row_mask:0xf bank_mask:0xf\n"
    "v_cndmask_b32 v154, 0, v152, vcc\n"
    "v_mov_b32 v239, v254\n"
    "v_mfma_f32_16x16x32_bf16 v[48:51], a[36:39], a[132:135], v[48:51]\n"
    "buffer_load_dwordx4 v253, s[52:55], s76 offen offset:2048 lds\n"
    "v_accvgpr_write_b32 a164, v154\n"
    "v_mfma_f32_16x16x32_bf16 v[80:83], a[32:35], a[120:123], 0\n"
    "v_permlane16_swap_b32 v239, v254\n"
    "v_accvgpr_write_b32 a165, v154\n"
    "v_mfma_f32_16x16x32_bf16 v[80:83], a[36:39], a[136:139], v[80:83]\n"
    "v_accvgpr_write_b32 a166, v154\n"
    "v_add_f32 v254, v254, v239\n"
    "v_cmp_eq_u32 vcc, 6, v132\n"
    "v_mfma_f32_16x16x32_bf16 v[112:115], a[32:35], a[124:127], 0\n"
    "s_add_u32 s97, s97, 1\n"
    "v_mov_b32 v239, v254\n"
    "v_accvgpr_write_b32 a167, v154\n"
    "v_mfma_f32_16x16x32_bf16 v[112:115], a[36:39], a[140:143], v[112:115]\n"
    "s_and_b32 s97, s97, 7\n"
    "v_permlane32_swap_b32 v239, v254\n"
    "s_waitcnt lgkmcnt(6)\n"
    "v_mfma_f32_16x16x32_bf16 v[20:23], a[40:43], a[112:115], 0\n"
    "s_nop 0\n"
    "s_lshl_b32 s66, s97, 7\n"
    "v_cndmask_b32 v153, 0, v152, vcc\n"
    "s_waitcnt lgkmcnt(4)\n"
    "v_mfma_f32_16x16x32_bf16 v[20:23], a[44:47], a[128:131], v[20:23]\n"
    "s_lshl_b32 s67, s97, 19\n"
    "v_add_f32 v254, v254, v239\n"
    "v_accvgpr_write_b32 a168, v153\n"
    "v_accvgpr_write_b32 a169, v153\n"
    "v_mfma_f32_16x16x32_bf16 v[52:55], a[40:43], a[116:119], 0\n"
    "ds_write_b32 v157, v254 offset:128\n"
    "s_mul_i32 s76, s97, 176128\n"
    "v_accvgpr_write_b32 a170, v153\n"
    "v_mfma_f32_16x16x32_bf16 v[52:55], a[44:47], a[132:135], v[52:55]\n"
    "s_waitcnt vmcnt(15)\n"
    "s_waitcnt lgkmcnt(0)\n"
    "s_barrier\n"
    "v_mfma_f32_16x16x32_bf16 v[84:87], a[40:43], a[120:123], 0\n"
    "ds_read_b128 v[160:163], v156 offset:128\n"
    "ds_read_b64_tr_b16 a[56:57], v232 offset:1544\n"
    "v_cmp_eq_u32 vcc, 7, v132\n"
    "v_mfma_f32_16x16x32_bf16 v[84:87], a[44:47], a[136:139], v[84:87]\n"
    "ds_read_b64_tr_b16 a[58:59], v232 offset:1800\n"
    "ds_read_b64_tr_b16 a[60:61], v232 offset:9768\n"
    "v_accvgpr_write_b32 a171, v153\n"
    "s_nop 0\n"
    "v_mfma_f32_16x16x32_bf16 v[116:119], a[40:43], a[124:127], 0\n"
    "ds_read_b64_tr_b16 a[62:63], v232 offset:10024\n"
    "v_cndmask_b32 v154, 0, v152, vcc\n"
    "v_accvgpr_write_b32 a172, v154\n"
    "v_mfma_f32_16x16x32_bf16 v[116:119], a[44:47], a[140:143], v[116:119]\n"
    "ds_read_b64_tr_b16 a[64:65], v234 offset:0\n"
    "v_accvgpr_write_b32 a204, v164\n"
    "v_accvgpr_write_b32 a173, v154\n"
    "v_accvgpr_write_b32 a174, v154\n"
    "v_mfma_f32_16x16x32_bf16 v[24:27], a[48:51], a[112:115], 0\n"
    "ds_read_b64_tr_b16 a[66:67], v235 offset:0\n"
    "v_cmp_eq_u32 vcc, 8, v132\n"
    "v_accvgpr_write_b32 a175, v154\n"
    "v_mfma_f32_16x16x32_bf16 v[24:27], a[52:55], a[128:131], v[24:27]\n"
    "ds_read_b64_tr_b16 a[68:69], v234 offset:3072\n"
    "s_nop 0\n"
    "ds_read_b64_tr_b16 a[70:71], v235 offset:3072\n"
    "v_accvgpr_write_b32 a206, v158\n"
    "v_mfma_f32_16x16x32_bf16 v[56:59], a[48:51], a[116:119], 0\n"
    "ds_read_b64_tr_b16 a[72:73], v234 offset:6144\n"
    "v_cndmask_b32 v153, 0, v152, vcc\n"
    "v_accvgpr_write_b32 a176, v153\n"
    "v_mfma_f32_16x16x32_bf16 v[56:59], a[52:55], a[132:135], v[56:59]\n"
    "ds_read_b64_tr_b16 a[74:75], v235 offset:6144\n"
    "v_accvgpr_write_b32 a177, v153\n"
    "v_mov_b32 v165, 0x3727c5ac\n"
    "v_accvgpr_write_b32 a178, v153\n"
    "v_mfma_f32_16x16x32_bf16 v[88:91], a[48:51], a[120:123], 0\n"
    "ds_read_b64_tr_b16 a[76:77], v234 offset:9216\n"
    "v_cmp_eq_u32 vcc, 9, v132\n"
    "v_accvgpr_write_b32 a179, v153\n"
    "v_mfma_f32_16x16x32_bf16 v[88:91], a[52:55], a[136:139], v[88:91]\n"
    "ds_read_b64_tr_b16 a[78:79], v235 offset:9216\n"
    "s_nop 0\n"
    "ds_read_b64_tr_b16 a[80:81], v234 offset:256\n"
    "s_waitcnt lgkmcnt(13)\n"
    "v_add_f32 v160, v160, v161\n"
    "v_mfma_f32_16x16x32_bf16 v[120:123], a[48:51], a[124:127], 0\n"
    "ds_read_b64_tr_b16 a[82:83], v235 offset:256\n"
    "v_cndmask_b32 v154, 0, v152, vcc\n"
    "v_accvgpr_write_b32 a180, v154\n"
    "v_accvgpr_write_b32 a181, v154\n"
    "v_mfma_f32_16x16x32_bf16 v[120:123], a[52:55], a[140:143], v[120:123]\n"
    "ds_read_b64_tr_b16 a[84:85], v234 offset:3328\n"
    "ds_read_b64_tr_b16 a[86:87], v235 offset:3328\n"
    "v_accvgpr_write_b32 a182, v154\n"
    "v_mfma_f32_16x16x32_bf16 a[200:203], a[144:147], a[0:3], 0\n"
    "ds_read_b64_tr_b16 a[88:89], v234 offset:6400\n"
    "v_add_f32 v162, v162, v163\n"
    "v_cmp_eq_u32 vcc, 10, v132\n"
    "v_accvgpr_write_b32 a183, v154\n"
    "v_mfma_f32_16x16x32_bf16 a[200:203], a[144:147], a[4:7], a[200:203]\n"
    "s_nop 0\n"
    "ds_read_b64_tr_b16 a[90:91], v235 offset:6400\n"
    "v_cndmask_b32 v153, 0, v152, vcc\n"
    "v_mfma_f32_16x16x32_bf16 a[200:203], a[148:151], a[8:11], a[200:203]\n"
    "ds_read_b64_tr_b16 a[92:93], v234 offset:9472\n"
    "v_add_f32 v160, v160, v162\n"
    "v_accvgpr_write_b32 a184, v153\n"
    "v_accvgpr_write_b32 a185, v153\n"
    "v_mfma_f32_16x16x32_bf16 a[200:203], a[148:151], a[12:15], a[200:203]\n"
    "ds_read_b64_tr_b16 a[94:95], v235 offset:9472\n"
    "ds_read_b64_tr_b16 a[96:97], v234 offset:512\n"
    "v_accvgpr_write_b32 a186, v153\n"
    "v_mfma_f32_16x16x32_bf16 a[200:203], a[152:155], a[16:19], a[200:203]\n"
    "ds_read_b64_tr_b16 a[98:99], v235 offset:512\n"
    "v_cmp_eq_u32 vcc, 11, v132\n"
    "v_accvgpr_write_b32 a187, v153\n"
    "v_fmac_f32 v165, 0x3b000000, v160\n"
    "v_mfma_f32_16x16x32_bf16 a[200:203], a[152:155], a[20:23], a[200:203]\n"
    "s_nop 0\n"
    "ds_read_b64_tr_b16 a[100:101], v234 offset:3584\n"
    "v_cndmask_b32 v154, 0, v152, vcc\n"
    "v_mfma_f32_16x16x32_bf16 a[200:203], a[156:159], a[24:27], a[200:203]\n"
    "ds_read_b64_tr_b16 a[102:103], v235 offset:3584\n"
    "ds_read_b64_tr_b16 a[104:105], v234 offset:6656\n"
    "v_accvgpr_write_b32 a188, v154\n"
    "v_accvgpr_write_b32 a189, v154\n"
    "v_mfma_f32_16x16x32_bf16 a[200:203], a[156:159], a[28:31], a[200:203]\n"
    "ds_read_b64_tr_b16 a[106:107], v235 offset:6656\n"
    "ds_read_b64_tr_b16 a[108:109], v234 offset:9728\n"
    "ds_read_b64_tr_b16 a[110:111], v235 offset:9728\n"
    "v_mfma_f32_16x16x32_bf16 a[200:203], a[160:163], a[32:35], a[200:203]\n"
    "v_rsq_f32 v165, v165\n"
    "v_accvgpr_write_b32 a190, v154\n"
    "v_cmp_eq_u32 vcc, 12, v132\n"
    "v_mfma_f32_16x16x32_bf16 a[200:203], a[160:163], a[36:39], a[200:203]\n"
    "v_accvgpr_write_b32 a191, v154\n"
    "s_nop 0\n"
    "v_cndmask_b32 v153, 0, v152, vcc\n"
    "v_accvgpr_write_b32 a192, v153\n"
    "v_mfma_f32_16x16x32_bf16 a[200:203], a[164:167], a[40:43], a[200:203]\n"
    "s_nop 0\n"
    "v_accvgpr_write_b32 a193, v153\n"
    "v_accvgpr_write_b32 a194, v153\n"
    "v_mfma_f32_16x16x32_bf16 a[200:203], a[164:167], a[44:47], a[200:203]\n"
    "v_cmp_eq_u32 vcc, 13, v132\n"
    "v_accvgpr_write_b32 a195, v153\n"
    "s_nop 0\n"
    "v_cndmask_b32 v154, 0, v152, vcc\n"
    "v_mfma_f32_16x16x32_bf16 a[200:203], a[168:171], a[48:51], a[200:203]\n"
    "v_accvgpr_write_b32 a205, v165\n"
    "v_accvgpr_write_b32 a196, v154\n"
    "v_accvgpr_write_b32 a197, v154\n"
    "v_mfma_f32_16x16x32_bf16 a[200:203], a[168:171], a[52:55], a[200:203]\n"
    "v_accvgpr_write_b32 a198, v154\n"
    "v_accvgpr_write_b32 a199, v154\n"
    "s_waitcnt vmcnt(0)\n"
    "s_waitcnt lgkmcnt(0)\n"
    "s_barrier\n"
    "v_mfma_f32_16x16x32_bf16 v[28:31], a[56:59], a[112:115], 0\n"
    "s_mov_b32 s56, s26\n"
    "ds_read_b128 a[212:215], v230 offset:0\n"
    "v_mfma_f32_16x16x32_bf16 v[28:31], a[60:63], a[128:131], v[28:31]\n"
    "s_add_u32 m0, s72, 73216\n"
    "s_and_b32 s57, s27, 0xffff\n"
    "v_mfma_f32_16x16x32_bf16 v[60:63], a[56:59], a[116:119], 0\n"
    "ds_read_b128 a[228:231], v231 offset:0\n"
    "s_mov_b32 s58, 41832448\n"
    "v_mfma_f32_16x16x32_bf16 v[60:63], a[60:63], a[132:135], v[60:63]\n"
    "buffer_load_dwordx4 v226, s[40:43], s67 offen lds\n"
    "v_mfma_f32_16x16x32_bf16 v[92:95], a[56:59], a[120:123], 0\n"
    "s_mov_b32 s59, 0x20000\n"
    "ds_read_b64_tr_b16 a[0:1], v233 offset:0\n"
    "v_mfma_f32_16x16x32_bf16 v[92:95], a[60:63], a[136:139], v[92:95]\n"
    "s_lshl_b32 s78, s62, 11\n"
    "s_lshl_b32 s65, s61, 6\n"
    "v_mfma_f32_16x16x32_bf16 v[124:127], a[56:59], a[124:127], 0\n"
    "ds_read_b64_tr_b16 a[2:3], v233 offset:256\n"
    "s_add_u32 s78, s78, s65\n"
    "v_mfma_f32_16x16x32_bf16 v[124:127], a[60:63], a[140:143], v[124:127]\n"
    "buffer_load_dwordx4 v227, s[40:43], s67 offen offset:1024 lds\n"
    "v_mfma_f32_16x16x32_bf16 v[128:131], a[64:67], a[112:115], 0\n"
    "ds_read_b64_tr_b16 a[4:5], v233 offset:8224\n"
    "s_add_u32 s78, s78, 135168\n"
    "v_mfma_f32_16x16x32_bf16 v[128:131], a[68:71], a[128:131], v[128:131]\n"
    "ds_read_b64_tr_b16 a[6:7], v233 offset:8480\n"
    "v_lshrrev_b32 v254, 5, v238\n"
    "v_mfma_f32_16x16x32_bf16 v[152:155], a[64:67], a[116:119], 0\n"
    "v_lshl_add_u32 v244, v254, 11, v244\n"
    "v_add_u32 v244, 32768, v244\n"
    "v_mfma_f32_16x16x32_bf16 v[152:155], a[68:71], a[132:135], v[152:155]\n"
    "ds_read_b128 a[216:219], v230 offset:2048\n"
    "v_lshrrev_b32 v249, 4, v238\n"
    "v_mfma_f32_16x16x32_bf16 v[176:179], a[64:67], a[120:123], 0\n"
    "s_add_u32 s90, s67, 63480\n"
    "v_and_b32 v250, 15, v238\n"
    "v_mfma_f32_16x16x32_bf16 v[176:179], a[68:71], a[136:139], v[176:179]\n"
    "ds_read_b128 a[232:235], v231 offset:2048\n"
    "v_xor_b32 v239, v249, v250\n"
    "v_mfma_f32_16x16x32_bf16 v[200:203], a[64:67], a[124:127], 0\n"
    "buffer_load_dwordx4 v226, s[40:43], s90 offen offset:2056 lds\n"
    "v_mfma_f32_16x16x32_bf16 v[200:203], a[68:71], a[140:143], v[200:203]\n"
    "ds_read_b128 a[220:223], v230 offset:4096\n"
    "v_xor_b32 v254, 0, v239\n"
    "v_mfma_f32_16x16x32_bf16 v[132:135], a[72:75], a[112:115], 0\n"
    "ds_read_b128 a[236:239], v231 offset:4096\n"
    "v_lshlrev_b32 v254, 4, v254\n"
    "v_mfma_f32_16x16x32_bf16 v[132:135], a[76:79], a[128:131], v[132:135]\n"
    "v_lshl_add_u32 v240, v250, 8, v254\n"
    "v_lshl_add_u32 v245, v249, 8, v254\n"
    "v_mfma_f32_16x16x32_bf16 v[156:159], a[72:75], a[116:119], 0\n"
    "ds_read_b128 a[224:227], v230 offset:6144\n"
    "v_xor_b32 v254, 4, v239\n"
    "v_mfma_f32_16x16x32_bf16 v[156:159], a[76:79], a[132:135], v[156:159]\n"
    "buffer_load_dwordx4 v227, s[40:43], s90 offen offset:3080 lds\n"
    "v_mfma_f32_16x16x32_bf16 v[180:183], a[72:75], a[120:123], 0\n"
    "ds_read_b128 a[240:243], v231 offset:6144\n"
    "v_lshlrev_b32 v254, 4, v254\n"
    "v_mfma_f32_16x16x32_bf16 v[180:183], a[76:79], a[136:139], v[180:183]\n"
    "ds_read_b64_tr_b16 a[8:9], v233 offset:8\n"
    "v_lshl_add_u32 v241, v250, 8, v254\n"
    "v_mfma_f32_16x16x32_bf16 v[204:207], a[72:75], a[124:127], 0\n"
    "v_lshl_add_u32 v246, v249, 8, v254\n"
    "v_xor_b32 v254, 8, v239\n"
    "v_mfma_f32_16x16x32_bf16 v[204:207], a[76:79], a[140:143], v[204:207]\n"
    "ds_read_b64_tr_b16 a[10:11], v233 offset:264\n"
    "s_add_u32 m0, s71, 40448\n"
    "v_mfma_f32_16x16x32_bf16 v[136:139], a[80:83], a[112:115], 0\n"
    "v_lshlrev_b32 v254, 4, v254\n"
    "v_lshl_add_u32 v242, v250, 8, v254\n"
    "v_mfma_f32_16x16x32_bf16 v[136:139], a[84:87], a[128:131], v[136:139]\n"
    "ds_read_b64_tr_b16 a[12:13], v233 offset:8232\n"
    "v_lshl_add_u32 v247, v249, 8, v254\n"
    "v_mfma_f32_16x16x32_bf16 v[160:163], a[80:83], a[116:119], 0\n"
    "buffer_load_dwordx4 v224, s[36:39], s66 offen lds\n"
    "v_mfma_f32_16x16x32_bf16 v[160:163], a[84:87], a[132:135], v[160:163]\n"
    "ds_read_b64_tr_b16 a[14:15], v233 offset:8488\n"
    "v_xor_b32 v254, 12, v239\n"
    "v_mfma_f32_16x16x32_bf16 v[184:187], a[80:83], a[120:123], 0\n"
    "ds_read_b64_tr_b16 a[16:17], v233 offset:512\n"
    "v_lshlrev_b32 v254, 4, v254\n"
    "v_mfma_f32_16x16x32_bf16 v[184:187], a[84:87], a[136:139], v[184:187]\n"
    "v_lshl_add_u32 v243, v250, 8, v254\n"
    "v_lshl_add_u32 v248, v249, 8, v254\n"
    "v_mfma_f32_16x16x32_bf16 v[208:211], a[80:83], a[124:127], 0\n"
    "ds_read_b64_tr_b16 a[18:19], v233 offset:768\n"
    "s_add_u32 s91, s66, 7168\n"
    "v_mfma_f32_16x16x32_bf16 v[208:211], a[84:87], a[140:143], v[208:211]\n"
    "v_bfe_u32 v254, v238, 1, 3\n"
    "v_and_b32 v239, 1, v238\n"
    "v_mfma_f32_16x16x32_bf16 v[140:143], a[88:91], a[112:115], 0\n"
    "ds_read_b64_tr_b16 a[20:21], v233 offset:8736\n"
    "v_lshlrev_b32 v239, 4, v239\n"
    "v_mfma_f32_16x16x32_bf16 v[140:143], a[92:95], a[128:131], v[140:143]\n"
    "buffer_load_dwordx4 v225, s[36:39], s91 offen offset:1024 lds\n"
    "v_mfma_f32_16x16x32_bf16 v[164:167], a[88:91], a[116:119], 0\n"
    "ds_read_b64_tr_b16 a[22:23], v233 offset:8992\n"
    "v_lshl_add_u32 v239, v249, 5, v239\n"
    "v_mfma_f32_16x16x32_bf16 v[164:167], a[92:95], a[132:135], v[164:167]\n"
    "ds_read_b64_tr_b16 a[24:25], v233 offset:520\n"
    "v_lshl_add_u32 v250, v254, 8, v239\n"
    "v_mfma_f32_16x16x32_bf16 v[188:191], a[88:91], a[120:123], 0\n"
    "v_add_u32 v249, 1024, v250\n"
    "v_add_u32 v250, 128, v250\n"
    "v_mfma_f32_16x16x32_bf16 v[188:191], a[92:95], a[136:139], v[188:191]\n"
    "ds_read_b64_tr_b16 a[26:27], v233 offset:776\n"
    "s_add_u32 s90, s66, 14336\n"
    "v_mfma_f32_16x16x32_bf16 v[212:215], a[88:91], a[124:127], 0\n"
    "ds_read_b64_tr_b16 a[28:29], v233 offset:8744\n"
    "v_and_b32 v254, 3, v238\n"
    "v_mfma_f32_16x16x32_bf16 v[212:215], a[92:95], a[140:143], v[212:215]\n"
    "v_cmp_eq_u32 vcc, 0, v254\n"
    "v_mov_b32 v239, 0x3f803f80\n"
    "v_mfma_f32_16x16x32_bf16 v[144:147], a[96:99], a[112:115], 0\n"
    "buffer_load_dwordx4 v224, s[36:39], s90 offen offset:2048 lds\n"
    "v_mfma_f32_16x16x32_bf16 v[144:147], a[100:103], a[128:131], v[144:147]\n"
    "ds_read_b64_tr_b16 a[30:31], v233 offset:9000\n"
    "s_nop 0\n"
    "v_mfma_f32_16x16x32_bf16 v[168:171], a[96:99], a[116:119], 0\n"
    "ds_read_b64_tr_b16 a[32:33], v233 offset:1024\n"
    "v_cndmask_b32 v239, 0, v239, vcc\n"
    "v_mfma_f32_16x16x32_bf16 v[168:171], a[100:103], a[132:135], v[168:171]\n"
    "v_accvgpr_write_b32 a208, v239\n"
    "v_accvgpr_write_b32 a209, v239\n"
    "v_mfma_f32_16x16x32_bf16 v[192:195], a[96:99], a[120:123], 0\n"
    "s_add_u32 s91, s66, 21504\n"
    "ds_read_b64_tr_b16 a[34:35], v233 offset:1280\n"
    "v_mfma_f32_16x16x32_bf16 v[192:195], a[100:103], a[136:139], v[192:195]\n"
    "ds_read_b64_tr_b16 a[36:37], v233 offset:9248\n"
    "v_accvgpr_write_b32 a210, v239\n"
    "v_mfma_f32_16x16x32_bf16 v[216:219], a[96:99], a[124:127], 0\n"
    "v_accvgpr_write_b32 a211, v239\n"
    "s_lshl_b32 s63, s61, 19\n"
    "v_mfma_f32_16x16x32_bf16 v[216:219], a[100:103], a[140:143], v[216:219]\n"
    "buffer_load_dwordx4 v225, s[36:39], s91 offen offset:3072 lds\n"
    "v_mfma_f32_16x16x32_bf16 v[148:151], a[104:107], a[112:115], 0\n"
    "ds_read_b64_tr_b16 a[38:39], v233 offset:9504\n"
    "s_add_u32 s63, s63, 393216\n"
    "v_mfma_f32_16x16x32_bf16 v[148:151], a[108:111], a[128:131], v[148:151]\n"
    "s_add_u32 s44, s10, s63\n"
    "s_addc_u32 s45, s11, 0\n"
    "v_mfma_f32_16x16x32_bf16 v[172:175], a[104:107], a[116:119], 0\n"
    "ds_read_b64_tr_b16 a[40:41], v233 offset:1032\n"
    "s_add_u32 m0, s71, 44544\n"
    "v_mfma_f32_16x16x32_bf16 v[172:175], a[108:111], a[132:135], v[172:175]\n"
    "s_and_b32 s45, s45, 0xffff\n"
    "ds_read_b64_tr_b16 a[42:43], v233 offset:1288\n"
    "v_mfma_f32_16x16x32_bf16 v[196:199], a[104:107], a[120:123], 0\n"
    "s_sub_u32 s46, 0, s63\n"
    "ds_read_b64_tr_b16 a[44:45], v233 offset:9256\n"
    "v_mfma_f32_16x16x32_bf16 v[196:199], a[108:111], a[136:139], v[196:199]\n"
    "s_mov_b32 s47, 0x20000\n"
    "s_add_u32 s90, s66, 32768\n"
    "v_mfma_f32_16x16x32_bf16 v[220:223], a[104:107], a[124:127], 0\n"
    "s_mov_b32 s46, 131072\n"
    "ds_read_b64_tr_b16 a[46:47], v233 offset:9512\n"
    "v_mfma_f32_16x16x32_bf16 v[220:223], a[108:111], a[140:143], v[220:223]\n"
    "s_lshl_b32 s64, s60, 12\n"
    "s_add_u32 s63, s63, s64\n"
    "v_mfma_f32_16x16x32_bf16 a[200:203], a[172:175], a[56:59], a[200:203]\n"
    "buffer_load_dwordx4 v224, s[36:39], s90 offen lds\n"
    "v_mfma_f32_16x16x32_bf16 a[200:203], a[172:175], a[60:63], a[200:203]\n"
    "ds_read_b64_tr_b16 a[48:49], v233 offset:1536\n"
    "ds_read_b64_tr_b16 a[50:51], v233 offset:1792\n"
    "v_mfma_f32_16x16x32_bf16 a[200:203], a[176:179], a[64:67], a[200:203]\n"
    "s_add_u32 s48, s12, s63\n"
    "s_addc_u32 s49, s13, 0\n"
    "s_sub_u32 s48, s48, 1024\n"
    "s_subb_u32 s49, s49, 0\n"
    "v_mfma_f32_16x16x32_bf16 a[200:203], a[176:179], a[68:71], a[200:203]\n"
    "ds_read_b64_tr_b16 a[52:53], v233 offset:9760\n"
    "s_add_u32 s91, s66, 39936\n"
    "v_mfma_f32_16x16x32_bf16 a[200:203], a[180:183], a[72:75], a[200:203]\n"
    "s_and_b32 s49, s49, 0xffff\n"
    "ds_read_b64_tr_b16 a[54:55], v233 offset:10016\n"
    "v_mfma_f32_16x16x32_bf16 a[200:203], a[180:183], a[76:79], a[200:203]\n"
    "s_sub_u32 s50, 1024, s63\n"
    "s_mov_b32 s51, 0x20000\n"
    "v_mfma_f32_16x16x32_bf16 a[200:203], a[184:187], a[80:83], a[200:203]\n"
    "buffer_load_dwordx4 v225, s[36:39], s91 offen offset:1024 lds\n"
    "v_mfma_f32_16x16x32_bf16 a[200:203], a[184:187], a[84:87], a[200:203]\n"
    "s_sub_u32 s50, 132096, s64\n"
    "s_add_u32 s74, s64, 0\n"
    "v_mfma_f32_16x16x32_bf16 a[200:203], a[188:191], a[88:91], a[200:203]\n"
    "s_add_u32 s75, s64, 32768\n"
    "v_mfma_f32_16x16x32_bf16 a[200:203], a[188:191], a[92:95], a[200:203]\n"
    "s_add_u32 s90, s66, 47104\n"
    "s_and_b32 s65, s64, 4096\n"
    "v_mfma_f32_16x16x32_bf16 a[200:203], a[192:195], a[96:99], a[200:203]\n"
    "s_add_u32 s98, s73, s65\n"
    "v_mfma_f32_16x16x32_bf16 a[200:203], a[192:195], a[100:103], a[200:203]\n"
    "v_mfma_f32_16x16x32_bf16 a[200:203], a[196:199], a[104:107], a[200:203]\n"
    "v_mfma_f32_16x16x32_bf16 a[200:203], a[196:199], a[108:111], a[200:203]\n"
    "s_waitcnt lgkmcnt(15)\n"
    "v_mfma_f32_16x16x32_bf16 v[0:3], a[0:3], a[212:215], v[0:3]\n"
    "ds_read_b64_tr_b16 a[56:57], v233 offset:1544\n"
    "ds_read_b64_tr_b16 a[58:59], v233 offset:1800\n"
    "v_mfma_f32_16x16x32_bf16 v[0:3], a[4:7], a[228:231], v[0:3]\n"
    "buffer_load_dwordx4 v224, s[36:39], s90 offen offset:2048 lds\n"
    "v_mfma_f32_16x16x32_bf16 v[32:35], a[0:3], a[216:219], v[32:35]\n"
    "s_add_u32 s91, s66, 54272\n"
    "ds_read_b64_tr_b16 a[60:61], v233 offset:9768\n"
    "v_mfma_f32_16x16x32_bf16 v[32:35], a[4:7], a[232:235], v[32:35]\n"
    "buffer_load_dwordx4 v225, s[36:39], s91 offen offset:3072 lds\n"
    "v_mfma_f32_16x16x32_bf16 v[64:67], a[0:3], a[220:223], v[64:67]\n"
    "ds_read_b64_tr_b16 a[62:63], v233 offset:10024\n"
    "s_add_u32 m0, s77, 89856\n"
    "v_mfma_f32_16x16x32_bf16 v[64:67], a[4:7], a[236:239], v[64:67]\n"
    "ds_read_b64_tr_b16 a[64:65], v236 offset:0\n"
    "v_mfma_f32_16x16x32_bf16 v[96:99], a[0:3], a[224:227], v[96:99]\n"
    "buffer_load_dwordx4 v251, s[52:55], s76 offen lds\n"
    "v_mfma_f32_16x16x32_bf16 v[96:99], a[4:7], a[240:243], v[96:99]\n"
    "ds_read_b64_tr_b16 a[66:67], v237 offset:0\n"
    "v_mfma_f32_16x16x32_bf16 v[4:7], a[8:11], a[212:215], v[4:7]\n"
    "buffer_load_dwordx4 v252, s[52:55], s76 offen offset:1024 lds\n"
    "v_mfma_f32_16x16x32_bf16 v[4:7], a[12:15], a[228:231], v[4:7]\n"
    "ds_read_b64_tr_b16 a[68:69], v236 offset:3072\n"
    "v_mfma_f32_16x16x32_bf16 v[36:39], a[8:11], a[216:219], v[36:39]\n"
    "ds_read_b64_tr_b16 a[70:71], v237 offset:3072\n"
    "v_mfma_f32_16x16x32_bf16 v[36:39], a[12:15], a[232:235], v[36:39]\n"
    "buffer_load_dwordx4 v253, s[52:55], s76 offen offset:2048 lds\n"
    "v_mfma_f32_16x16x32_bf16 v[68:71], a[8:11], a[220:223], v[68:71]\n"
    "ds_read_b64_tr_b16 a[72:73], v236 offset:6144\n"
    "s_add_u32 s97, s97, 1\n"
    "v_mfma_f32_16x16x32_bf16 v[68:71], a[12:15], a[236:239], v[68:71]\n"
    "v_mfma_f32_16x16x32_bf16 v[100:103], a[8:11], a[224:227], v[100:103]\n"
    "ds_read_b64_tr_b16 a[74:75], v237 offset:6144\n"
    "s_and_b32 s97, s97, 7\n"
    "v_mfma_f32_16x16x32_bf16 v[100:103], a[12:15], a[240:243], v[100:103]\n"
    "s_lshl_b32 s66, s97, 7\n"
    "ds_read_b64_tr_b16 a[76:77], v236 offset:9216\n"
    "v_mfma_f32_16x16x32_bf16 v[8:11], a[16:19], a[212:215], v[8:11]\n"
    "s_lshl_b32 s67, s97, 19\n"
    "v_mfma_f32_16x16x32_bf16 v[8:11], a[20:23], a[228:231], v[8:11]\n"
    "ds_read_b64_tr_b16 a[78:79], v237 offset:9216\n"
    "s_mul_i32 s76, s97, 176128\n"
    "v_mfma_f32_16x16x32_bf16 v[40:43], a[16:19], a[216:219], v[40:43]\n"
    "v_mfma_f32_16x16x32_bf16 v[40:43], a[20:23], a[232:235], v[40:43]\n"
    "ds_read_b64_tr_b16 a[80:81], v236 offset:256\n"
    "v_min_u32 v254, 7, v238\n"
    "v_mfma_f32_16x16x32_bf16 v[72:75], a[16:19], a[220:223], v[72:75]\n"
    "s_and_b32 s65, s61, 7\n"
    "v_mfma_f32_16x16x32_bf16 v[72:75], a[20:23], a[236:239], v[72:75]\n"
    "ds_read_b64_tr_b16 a[82:83], v237 offset:256\n"
    "s_lshl_b32 s65, s65, 13\n"
    "v_mfma_f32_16x16x32_bf16 v[104:107], a[16:19], a[224:227], v[104:107]\n"
    "ds_read_b64_tr_b16 a[84:85], v236 offset:3328\n"
    "v_lshlrev_b32 v254, 10, v254\n"
    "v_mfma_f32_16x16x32_bf16 v[104:107], a[20:23], a[240:243], v[104:107]\n"
    "s_waitcnt lgkmcnt(15)\n"
    "v_mfma_f32_16x16x32_bf16 v[12:15], a[24:27], a[212:215], v[12:15]\n"
    "s_add_u32 s65, s65, s66\n"
    "ds_read_b64_tr_b16 a[86:87], v237 offset:3328\n"
    "v_mfma_f32_16x16x32_bf16 v[12:15], a[28:31], a[228:231], v[12:15]\n"
    "buffer_load_dword a207, v254, s[36:39], s65 offen\n"
    "v_mfma_f32_16x16x32_bf16 v[44:47], a[24:27], a[216:219], v[44:47]\n"
    "ds_read_b64_tr_b16 a[88:89], v236 offset:6400\n"
    "v_mfma_f32_16x16x32_bf16 v[44:47], a[28:31], a[232:235], v[44:47]\n"
    "v_mfma_f32_16x16x32_bf16 v[76:79], a[24:27], a[220:223], v[76:79]\n"
    "ds_read_b64_tr_b16 a[90:91], v237 offset:6400\n"
    "v_mfma_f32_16x16x32_bf16 v[76:79], a[28:31], a[236:239], v[76:79]\n"
    "v_mfma_f32_16x16x32_bf16 v[108:111], a[24:27], a[224:227], v[108:111]\n"
    "ds_read_b64_tr_b16 a[92:93], v236 offset:9472\n"
    "v_accvgpr_read_b32 v239, a206\n"
    "v_mfma_f32_16x16x32_bf16 v[108:111], a[28:31], a[240:243], v[108:111]\n"
    "ds_read_b64_tr_b16 a[94:95], v237 offset:9472\n"
    "v_lshlrev_b32 v254, 4, v238\n"
    "v_mfma_f32_16x16x32_bf16 v[16:19], a[32:35], a[212:215], v[16:19]\n"
    "buffer_store_dwordx2 a[204:205], v239, s[56:59], s78 offen sc1\n"
    "v_mfma_f32_16x16x32_bf16 v[16:19], a[36:39], a[228:231], v[16:19]\n"
    "ds_read_b64_tr_b16 a[96:97], v236 offset:512\n"
    "v_mfma_f32_16x16x32_bf16 v[48:51], a[32:35], a[216:219], v[48:51]\n"
    "v_mfma_f32_16x16x32_bf16 v[48:51], a[36:39], a[232:235], v[48:51]\n"
    "ds_read_b64_tr_b16 a[98:99], v237 offset:512\n"
    "v_mfma_f32_16x16x32_bf16 v[80:83], a[32:35], a[220:223], v[80:83]\n"
    "s_lshl_b32 s63, s61, 13\n"
    "v_mfma_f32_16x16x32_bf16 v[80:83], a[36:39], a[236:239], v[80:83]\n"
    "ds_read_b64_tr_b16 a[100:101], v236 offset:3584\n"
    "v_mfma_f32_16x16x32_bf16 v[112:115], a[32:35], a[224:227], v[112:115]\n"
    "s_lshl_b32 s64, s99, 10\n"
    "v_mfma_f32_16x16x32_bf16 v[112:115], a[36:39], a[240:243], v[112:115]\n"
    "ds_read_b64_tr_b16 a[102:103], v237 offset:3584\n"
    "v_mfma_f32_16x16x32_bf16 v[20:23], a[40:43], a[212:215], v[20:23]\n"
    "ds_read_b64_tr_b16 a[104:105], v236 offset:6656\n"
    "s_add_u32 s63, s63, s64\n"
    "v_mfma_f32_16x16x32_bf16 v[20:23], a[44:47], a[228:231], v[20:23]\n"
    "v_mfma_f32_16x16x32_bf16 v[52:55], a[40:43], a[216:219], v[52:55]\n"
    "ds_read_b64_tr_b16 a[106:107], v237 offset:6656\n"
    "v_mfma_f32_16x16x32_bf16 v[52:55], a[44:47], a[232:235], v[52:55]\n"
    "s_add_u32 s63, s63, 151552\n"
    "v_mfma_f32_16x16x32_bf16 v[84:87], a[40:43], a[220:223], v[84:87]\n"
    "ds_read_b64_tr_b16 a[108:109], v236 offset:9728\n"
    "v_mfma_f32_16x16x32_bf16 v[84:87], a[44:47], a[236:239], v[84:87]\n"
    "s_cmp_eq_u32 s60, 0\n"
    "s_cbranch_scc0 LCS\n"
    "buffer_store_dwordx4 a[200:203], v254, s[56:59], s63 offen sc1\n"
    "LCS:\n"
    "v_mfma_f32_16x16x32_bf16 v[116:119], a[40:43], a[224:227], v[116:119]\n"
    "ds_read_b64_tr_b16 a[110:111], v237 offset:9728\n"
    "v_mfma_f32_16x16x32_bf16 v[116:119], a[44:47], a[240:243], v[116:119]\n"
    "v_mfma_f32_16x16x32_bf16 v[24:27], a[48:51], a[212:215], v[24:27]\n"
    "v_mfma_f32_16x16x32_bf16 v[24:27], a[52:55], a[228:231], v[24:27]\n"
    "v_mfma_f32_16x16x32_bf16 v[56:59], a[48:51], a[216:219], v[56:59]\n"
    "v_mfma_f32_16x16x32_bf16 v[56:59], a[52:55], a[232:235], v[56:59]\n"
    "v_mfma_f32_16x16x32_bf16 v[88:91], a[48:51], a[220:223], v[88:91]\n"
    "v_mfma_f32_16x16x32_bf16 v[88:91], a[52:55], a[236:239], v[88:91]\n"
    "v_mfma_f32_16x16x32_bf16 v[120:123], a[48:51], a[224:227], v[120:123]\n"
    "v_mfma_f32_16x16x32_bf16 v[120:123], a[52:55], a[240:243], v[120:123]\n"
    "s_waitcnt vmcnt(2)\n"
    "s_waitcnt lgkmcnt(0)\n"
    "s_barrier\n"
    "v_mfma_f32_16x16x32_bf16 v[28:31], a[56:59], a[212:215], v[28:31]\n"
    "ds_read_b128 a[112:115], v228 offset:0\n"
    "s_add_u32 m0, s72, 134912\n"
    "v_mfma_f32_16x16x32_bf16 v[28:31], a[60:63], a[228:231], v[28:31]\n"
    "s_mov_b32 s12, s16\n"
    "ds_read_b128 a[128:131], v229 offset:0\n"
    "v_mfma_f32_16x16x32_bf16 v[60:63], a[56:59], a[216:219], v[60:63]\n"
    "buffer_load_dwordx4 v226, s[40:43], s67 offen lds\n"
    "v_mfma_f32_16x16x32_bf16 v[60:63], a[60:63], a[232:235], v[60:63]\n"
    "ds_read_b64_tr_b16 a[0:1], v232 offset:0\n"
    "ds_read_b64_tr_b16 a[2:3], v232 offset:256\n"
    "v_mfma_f32_16x16x32_bf16 v[92:95], a[56:59], a[220:223], v[92:95]\n"
    "s_and_b32 s13, s17, 0xffff\n"
    "ds_read_b64_tr_b16 a[4:5], v232 offset:8224\n"
    "v_mfma_f32_16x16x32_bf16 v[92:95], a[60:63], a[236:239], v[92:95]\n"
    "buffer_load_dwordx4 v227, s[40:43], s67 offen offset:1024 lds\n"
    "v_mfma_f32_16x16x32_bf16 v[124:127], a[56:59], a[224:227], v[124:127]\n"
    "ds_read_b64_tr_b16 a[6:7], v232 offset:8480\n"
    "ds_read_b128 a[116:119], v228 offset:2048\n"
    "v_mfma_f32_16x16x32_bf16 v[124:127], a[60:63], a[240:243], v[124:127]\n"
    "s_mov_b32 s14, 4194304\n"
    "s_add_u32 s90, s67, 63480\n"
    "v_mfma_f32_16x16x32_bf16 v[128:131], a[64:67], a[212:215], v[128:131]\n"
    "ds_read_b128 a[132:135], v229 offset:2048\n"
    "v_mfma_f32_16x16x32_bf16 v[128:131], a[68:71], a[228:231], v[128:131]\n"
    "ds_read_b128 a[120:123], v228 offset:4096\n"
    "v_mfma_f32_16x16x32_bf16 v[152:155], a[64:67], a[216:219], v[152:155]\n"
    "buffer_load_dwordx4 v226, s[40:43], s90 offen offset:2056 lds\n"
    "v_mfma_f32_16x16x32_bf16 v[152:155], a[68:71], a[232:235], v[152:155]\n"
    "ds_read_b128 a[136:139], v229 offset:4096\n"
    "s_mov_b32 s15, 0x20000\n"
    "v_mfma_f32_16x16x32_bf16 v[176:179], a[64:67], a[220:223], v[176:179]\n"
    "ds_read_b128 a[124:127], v228 offset:6144\n"
    "v_mfma_f32_16x16x32_bf16 v[176:179], a[68:71], a[236:239], v[176:179]\n"
    "buffer_load_dwordx4 v227, s[40:43], s90 offen offset:3080 lds\n"
    "v_mfma_f32_16x16x32_bf16 v[200:203], a[64:67], a[224:227], v[200:203]\n"
    "ds_read_b128 a[140:143], v229 offset:6144\n"
    "ds_read_b64_tr_b16 a[8:9], v232 offset:8\n"
    "v_mfma_f32_16x16x32_bf16 v[200:203], a[68:71], a[240:243], v[200:203]\n"
    "s_lshl_b32 s94, %1, 2\n"
    "v_mfma_f32_16x16x32_bf16 v[132:135], a[72:75], a[212:215], v[132:135]\n"
    "ds_read_b64_tr_b16 a[10:11], v232 offset:264\n"
    "s_add_u32 m0, s71, 102144\n"
    "v_mfma_f32_16x16x32_bf16 v[132:135], a[76:79], a[228:231], v[132:135]\n"
    "ds_read_b64_tr_b16 a[12:13], v232 offset:8232\n"
    "v_mfma_f32_16x16x32_bf16 v[156:159], a[72:75], a[216:219], v[156:159]\n"
    "buffer_load_dwordx4 v224, s[36:39], s66 offen lds\n"
    "v_mfma_f32_16x16x32_bf16 v[156:159], a[76:79], a[232:235], v[156:159]\n"
    "ds_read_b64_tr_b16 a[14:15], v232 offset:8488\n"
    "s_add_u32 s94, s94, s60\n"
    "v_mfma_f32_16x16x32_bf16 v[180:183], a[72:75], a[220:223], v[180:183]\n"
    "ds_read_b64_tr_b16 a[16:17], v232 offset:512\n"
    "v_mfma_f32_16x16x32_bf16 v[180:183], a[76:79], a[236:239], v[180:183]\n"
    "ds_read_b64_tr_b16 a[18:19], v232 offset:768\n"
    "v_mfma_f32_16x16x32_bf16 v[204:207], a[72:75], a[224:227], v[204:207]\n"
    "s_add_u32 s91, s66, 7168\n"
    "ds_read_b64_tr_b16 a[20:21], v232 offset:8736\n"
    "v_mfma_f32_16x16x32_bf16 v[204:207], a[76:79], a[240:243], v[204:207]\n"
    "s_lshl_b32 s93, s94, 12\n"
    "v_mfma_f32_16x16x32_bf16 v[136:139], a[80:83], a[212:215], v[136:139]\n"
    "buffer_load_dwordx4 v225, s[36:39], s91 offen offset:1024 lds\n"
    "v_mfma_f32_16x16x32_bf16 v[136:139], a[84:87], a[228:231], v[136:139]\n"
    "ds_read_b64_tr_b16 a[22:23], v232 offset:8992\n"
    "ds_read_b64_tr_b16 a[24:25], v232 offset:520\n"
    "v_mfma_f32_16x16x32_bf16 v[160:163], a[80:83], a[216:219], v[160:163]\n"
    "v_mfma_f32_16x16x32_bf16 v[160:163], a[84:87], a[232:235], v[160:163]\n"
    "ds_read_b64_tr_b16 a[26:27], v232 offset:776\n"
    "s_add_u32 s90, s66, 14336\n"
    "v_mfma_f32_16x16x32_bf16 v[184:187], a[80:83], a[220:223], v[184:187]\n"
    "s_mul_i32 s94, s94, 640\n"
    "ds_read_b64_tr_b16 a[28:29], v232 offset:8744\n"
    "v_mfma_f32_16x16x32_bf16 v[184:187], a[84:87], a[236:239], v[184:187]\n"
    "buffer_load_dwordx4 v224, s[36:39], s90 offen offset:2048 lds\n"
    "v_mfma_f32_16x16x32_bf16 v[208:211], a[80:83], a[224:227], v[208:211]\n"
    "ds_read_b64_tr_b16 a[30:31], v232 offset:9000\n"
    "v_mfma_f32_16x16x32_bf16 v[208:211], a[84:87], a[240:243], v[208:211]\n"
    "ds_read_b64_tr_b16 a[32:33], v232 offset:1024\n"
    "v_mfma_f32_16x16x32_bf16 v[140:143], a[88:91], a[212:215], v[140:143]\n"
    "s_add_u32 s94, s94, 786432\n"
    "ds_read_b64_tr_b16 a[34:35], v232 offset:1280\n"
    "v_mfma_f32_16x16x32_bf16 v[140:143], a[92:95], a[228:231], v[140:143]\n"
    "s_add_u32 s91, s66, 21504\n"
    "v_mfma_f32_16x16x32_bf16 v[164:167], a[88:91], a[216:219], v[164:167]\n"
    "ds_read_b64_tr_b16 a[36:37], v232 offset:9248\n"
    "v_mfma_f32_16x16x32_bf16 v[164:167], a[92:95], a[232:235], v[164:167]\n"
    "buffer_load_dwordx4 v225, s[36:39], s91 offen offset:3072 lds\n"
    "v_mfma_f32_16x16x32_bf16 v[188:191], a[88:91], a[220:223], v[188:191]\n"
    "ds_read_b64_tr_b16 a[38:39], v232 offset:9504\n"
    "s_mov_b32 s20, s22\n"
    "v_mfma_f32_16x16x32_bf16 v[188:191], a[92:95], a[236:239], v[188:191]\n"
    "ds_read_b64_tr_b16 a[40:41], v232 offset:1032\n"
    "v_mfma_f32_16x16x32_bf16 v[212:215], a[88:91], a[224:227], v[212:215]\n"
    "ds_read_b64_tr_b16 a[42:43], v232 offset:1288\n"
    "v_mfma_f32_16x16x32_bf16 v[212:215], a[92:95], a[240:243], v[212:215]\n"
    "s_add_u32 m0, s71, 106240\n"
    "ds_read_b64_tr_b16 a[44:45], v232 offset:9256\n"
    "v_mfma_f32_16x16x32_bf16 v[144:147], a[96:99], a[212:215], v[144:147]\n"
    "s_add_u32 s90, s66, 32768\n"
    "v_mfma_f32_16x16x32_bf16 v[144:147], a[100:103], a[228:231], v[144:147]\n"
    "ds_read_b64_tr_b16 a[46:47], v232 offset:9512\n"
    "s_and_b32 s21, s23, 0xffff\n"
    "v_mfma_f32_16x16x32_bf16 v[168:171], a[96:99], a[216:219], v[168:171]\n"
    "ds_read_b64_tr_b16 a[48:49], v232 offset:1536\n"
    "v_mfma_f32_16x16x32_bf16 v[168:171], a[100:103], a[232:235], v[168:171]\n"
    "buffer_load_dwordx4 v224, s[36:39], s90 offen lds\n"
    "v_mfma_f32_16x16x32_bf16 v[192:195], a[96:99], a[220:223], v[192:195]\n"
    "ds_read_b64_tr_b16 a[50:51], v232 offset:1792\n"
    "v_mfma_f32_16x16x32_bf16 v[192:195], a[100:103], a[236:239], v[192:195]\n"
    "ds_read_b64_tr_b16 a[52:53], v232 offset:9760\n"
    "s_mov_b32 s22, 1409024\n"
    "v_mfma_f32_16x16x32_bf16 v[216:219], a[96:99], a[224:227], v[216:219]\n"
    "s_add_u32 s91, s66, 39936\n"
    "ds_read_b64_tr_b16 a[54:55], v232 offset:10016\n"
    "v_mfma_f32_16x16x32_bf16 v[216:219], a[100:103], a[240:243], v[216:219]\n"
    "v_mfma_f32_16x16x32_bf16 v[148:151], a[104:107], a[212:215], v[148:151]\n"
    "buffer_load_dwordx4 v225, s[36:39], s91 offen offset:1024 lds\n"
    "v_mfma_f32_16x16x32_bf16 v[148:151], a[108:111], a[228:231], v[148:151]\n"
    "s_mov_b32 s23, 0x20000\n"
    "v_mfma_f32_16x16x32_bf16 v[172:175], a[104:107], a[216:219], v[172:175]\n"
    "v_mfma_f32_16x16x32_bf16 v[172:175], a[108:111], a[232:235], v[172:175]\n"
    "v_mfma_f32_16x16x32_bf16 v[196:199], a[104:107], a[220:223], v[196:199]\n"
    "s_add_u32 s90, s66, 47104\n"
    "v_mfma_f32_16x16x32_bf16 v[196:199], a[108:111], a[236:239], v[196:199]\n"
    "v_mfma_f32_16x16x32_bf16 v[220:223], a[104:107], a[224:227], v[220:223]\n"
    "v_mfma_f32_16x16x32_bf16 v[220:223], a[108:111], a[240:243], v[220:223]\n"
    "s_waitcnt lgkmcnt(15)\n"
    "v_mfma_f32_16x16x32_bf16 v[0:3], a[0:3], a[112:115], v[0:3]\n"
    "ds_read_b64_tr_b16 a[56:57], v232 offset:1544\n"
    "ds_read_b64_tr_b16 a[58:59], v232 offset:1800\n"
    "v_mfma_f32_16x16x32_bf16 v[0:3], a[4:7], a[128:131], v[0:3]\n"
    "buffer_load_dwordx4 v224, s[36:39], s90 offen offset:2048 lds\n"
    "v_mfma_f32_16x16x32_bf16 v[32:35], a[0:3], a[116:119], v[32:35]\n"
    "s_add_u32 s91, s66, 54272\n"
    "ds_read_b64_tr_b16 a[60:61], v232 offset:9768\n"
    "v_mfma_f32_16x16x32_bf16 v[32:35], a[4:7], a[132:135], v[32:35]\n"
    "buffer_load_dwordx4 v225, s[36:39], s91 offen offset:3072 lds\n"
    "v_mfma_f32_16x16x32_bf16 v[64:67], a[0:3], a[120:123], v[64:67]\n"
    "ds_read_b64_tr_b16 a[62:63], v232 offset:10024\n"
    "s_add_u32 m0, s77, 151552\n"
    "v_mfma_f32_16x16x32_bf16 v[64:67], a[4:7], a[136:139], v[64:67]\n"
    "ds_read_b64_tr_b16 a[64:65], v234 offset:0\n"
    "v_mfma_f32_16x16x32_bf16 v[96:99], a[0:3], a[124:127], v[96:99]\n"
    "buffer_load_dwordx4 v251, s[52:55], s76 offen lds\n"
    "v_mfma_f32_16x16x32_bf16 v[96:99], a[4:7], a[140:143], v[96:99]\n"
    "ds_read_b64_tr_b16 a[66:67], v235 offset:0\n"
    "v_mfma_f32_16x16x32_bf16 v[4:7], a[8:11], a[112:115], v[4:7]\n"
    "buffer_load_dwordx4 v252, s[52:55], s76 offen offset:1024 lds\n"
    "v_mfma_f32_16x16x32_bf16 v[4:7], a[12:15], a[128:131], v[4:7]\n"
    "ds_read_b64_tr_b16 a[68:69], v234 offset:3072\n"
    "v_mfma_f32_16x16x32_bf16 v[36:39], a[8:11], a[116:119], v[36:39]\n"
    "ds_read_b64_tr_b16 a[70:71], v235 offset:3072\n"
    "v_mfma_f32_16x16x32_bf16 v[36:39], a[12:15], a[132:135], v[36:39]\n"
    "buffer_load_dwordx4 v253, s[52:55], s76 offen offset:2048 lds\n"
    "v_mfma_f32_16x16x32_bf16 v[68:71], a[8:11], a[120:123], v[68:71]\n"
    "ds_read_b64_tr_b16 a[72:73], v234 offset:6144\n"
    "s_add_u32 s97, s97, 1\n"
    "v_mfma_f32_16x16x32_bf16 v[68:71], a[12:15], a[136:139], v[68:71]\n"
    "v_mfma_f32_16x16x32_bf16 v[100:103], a[8:11], a[124:127], v[100:103]\n"
    "ds_read_b64_tr_b16 a[74:75], v235 offset:6144\n"
    "s_and_b32 s97, s97, 7\n"
    "v_mfma_f32_16x16x32_bf16 v[100:103], a[12:15], a[140:143], v[100:103]\n"
    "s_lshl_b32 s66, s97, 7\n"
    "ds_read_b64_tr_b16 a[76:77], v234 offset:9216\n"
    "v_mfma_f32_16x16x32_bf16 v[8:11], a[16:19], a[112:115], v[8:11]\n"
    "s_lshl_b32 s67, s97, 19\n"
    "v_mfma_f32_16x16x32_bf16 v[8:11], a[20:23], a[128:131], v[8:11]\n"
    "ds_read_b64_tr_b16 a[78:79], v235 offset:9216\n"
    "s_mul_i32 s76, s97, 176128\n"
    "v_mfma_f32_16x16x32_bf16 v[40:43], a[16:19], a[116:119], v[40:43]\n"
    "v_mfma_f32_16x16x32_bf16 v[40:43], a[20:23], a[132:135], v[40:43]\n"
    "ds_read_b64_tr_b16 a[80:81], v234 offset:256\n"
    "v_min_u32 v254, 7, v238\n"
    "v_mfma_f32_16x16x32_bf16 v[72:75], a[16:19], a[120:123], v[72:75]\n"
    "s_and_b32 s65, s61, 7\n"
    "v_mfma_f32_16x16x32_bf16 v[72:75], a[20:23], a[136:139], v[72:75]\n"
    "ds_read_b64_tr_b16 a[82:83], v235 offset:256\n"
    "s_lshl_b32 s65, s65, 13\n"
    "v_mfma_f32_16x16x32_bf16 v[104:107], a[16:19], a[124:127], v[104:107]\n"
    "ds_read_b64_tr_b16 a[84:85], v234 offset:3328\n"
    "v_lshlrev_b32 v254, 10, v254\n"
    "v_mfma_f32_16x16x32_bf16 v[104:107], a[20:23], a[140:143], v[104:107]\n"
    "s_waitcnt lgkmcnt(15)\n"
    "v_mfma_f32_16x16x32_bf16 v[12:15], a[24:27], a[112:115], v[12:15]\n"
    "s_add_u32 s65, s65, s66\n"
    "ds_read_b64_tr_b16 a[86:87], v235 offset:3328\n"
    "v_mfma_f32_16x16x32_bf16 v[12:15], a[28:31], a[128:131], v[12:15]\n"
    "s_waitcnt vmcnt(16)\n"
    "buffer_load_dword a207, v254, s[36:39], s65 offen\n"
    "v_mfma_f32_16x16x32_bf16 v[44:47], a[24:27], a[116:119], v[44:47]\n"
    "ds_read_b64_tr_b16 a[88:89], v234 offset:6400\n"
    "v_mfma_f32_16x16x32_bf16 v[44:47], a[28:31], a[132:135], v[44:47]\n"
    "v_mfma_f32_16x16x32_bf16 v[76:79], a[24:27], a[120:123], v[76:79]\n"
    "ds_read_b64_tr_b16 a[90:91], v235 offset:6400\n"
    "v_mfma_f32_16x16x32_bf16 v[76:79], a[28:31], a[136:139], v[76:79]\n"
    "v_mfma_f32_16x16x32_bf16 v[108:111], a[24:27], a[124:127], v[108:111]\n"
    "s_and_b32 s63, s98, 4096\n"
    "s_add_u32 m0, s63, 24576\n"
    "v_mfma_f32_16x16x32_bf16 v[108:111], a[28:31], a[140:143], v[108:111]\n"
    "ds_read_b64_tr_b16 a[92:93], v234 offset:9472\n"
    "ds_read_b64_tr_b16 a[94:95], v235 offset:9472\n"
    "v_mfma_f32_16x16x32_bf16 v[16:19], a[32:35], a[112:115], v[16:19]\n"
    "buffer_load_dwordx4 v245, s[44:47], s98 offen lds\n"
    "v_mfma_f32_16x16x32_bf16 v[16:19], a[36:39], a[128:131], v[16:19]\n"
    "ds_read_b64_tr_b16 a[96:97], v234 offset:512\n"
    "v_mfma_f32_16x16x32_bf16 v[48:51], a[32:35], a[116:119], v[48:51]\n"
    "v_mfma_f32_16x16x32_bf16 v[48:51], a[36:39], a[132:135], v[48:51]\n"
    "buffer_load_dwordx4 v246, s[44:47], s98 offen offset:1024 lds\n"
    "v_mfma_f32_16x16x32_bf16 v[80:83], a[32:35], a[120:123], v[80:83]\n"
    "ds_read_b64_tr_b16 a[98:99], v235 offset:512\n"
    "v_mfma_f32_16x16x32_bf16 v[80:83], a[36:39], a[136:139], v[80:83]\n"
    "ds_read_b64_tr_b16 a[100:101], v234 offset:3584\n"
    "v_mfma_f32_16x16x32_bf16 v[112:115], a[32:35], a[124:127], v[112:115]\n"
    "v_mfma_f32_16x16x32_bf16 v[112:115], a[36:39], a[140:143], v[112:115]\n"
    "ds_read_b64_tr_b16 a[102:103], v235 offset:3584\n"
    "v_mfma_f32_16x16x32_bf16 v[20:23], a[40:43], a[112:115], v[20:23]\n"
    "buffer_load_dwordx4 v247, s[44:47], s98 offen offset:2048 lds\n"
    "v_mfma_f32_16x16x32_bf16 v[20:23], a[44:47], a[128:131], v[20:23]\n"
    "ds_read_b64_tr_b16 a[104:105], v234 offset:6656\n"
    "v_mfma_f32_16x16x32_bf16 v[52:55], a[40:43], a[116:119], v[52:55]\n"
    "ds_read_b64_tr_b16 a[106:107], v235 offset:6656\n"
    "v_mfma_f32_16x16x32_bf16 v[52:55], a[44:47], a[132:135], v[52:55]\n"
    "buffer_load_dwordx4 v248, s[44:47], s98 offen offset:3072 lds\n"
    "v_mfma_f32_16x16x32_bf16 v[84:87], a[40:43], a[120:123], v[84:87]\n"
    "ds_read_b64_tr_b16 a[108:109], v234 offset:9728\n"
    "v_mfma_f32_16x16x32_bf16 v[84:87], a[44:47], a[136:139], v[84:87]\n"
    "v_mfma_f32_16x16x32_bf16 v[116:119], a[40:43], a[124:127], v[116:119]\n"
    "ds_read_b64_tr_b16 a[110:111], v235 offset:9728\n"
    "s_lshl_b32 s64, s60, 12\n"
    "v_mfma_f32_16x16x32_bf16 v[116:119], a[44:47], a[140:143], v[116:119]\n"
    "v_mfma_f32_16x16x32_bf16 v[24:27], a[48:51], a[112:115], v[24:27]\n"
    "s_add_u32 s64, s64, 8192\n"
    "v_mfma_f32_16x16x32_bf16 v[24:27], a[52:55], a[128:131], v[24:27]\n"
    "v_mfma_f32_16x16x32_bf16 v[56:59], a[48:51], a[116:119], v[56:59]\n"
    "s_add_u32 s98, s73, s64\n"
    "v_mfma_f32_16x16x32_bf16 v[56:59], a[52:55], a[132:135], v[56:59]\n"
    "v_mfma_f32_16x16x32_bf16 v[88:91], a[48:51], a[120:123], v[88:91]\n"
    "s_and_b32 s98, s98, 0x1ffff\n"
    "v_mfma_f32_16x16x32_bf16 v[88:91], a[52:55], a[136:139], v[88:91]\n"
    "v_mfma_f32_16x16x32_bf16 v[120:123], a[48:51], a[124:127], v[120:123]\n"
    "v_mfma_f32_16x16x32_bf16 v[120:123], a[52:55], a[140:143], v[120:123]\n"
    "s_waitcnt vmcnt(5)\n"
    "s_waitcnt lgkmcnt(0)\n"
    "s_barrier\n"
    "v_mfma_f32_16x16x32_bf16 v[28:31], a[56:59], a[112:115], v[28:31]\n"
    "ds_read_b128 a[212:215], v230 offset:0\n"
    "s_add_u32 m0, s72, 73216\n"
    "v_mfma_f32_16x16x32_bf16 v[28:31], a[60:63], a[128:131], v[28:31]\n"
    "s_lshl_b32 s63, s62, 12\n"
    "s_lshl_b32 s64, s61, 7\n"
    "v_mfma_f32_16x16x32_bf16 v[60:63], a[56:59], a[116:119], v[60:63]\n"
    "s_add_u32 s63, s63, s64\n"
    "s_add_u32 s63, s63, 4096\n"
    "v_mfma_f32_16x16x32_bf16 v[60:63], a[60:63], a[132:135], v[60:63]\n"
    "v_mov_b32 v254, s68\n"
    "v_accvgpr_write_b32 a204, v254\n"
    "v_mfma_f32_16x16x32_bf16 v[92:95], a[56:59], a[120:123], v[92:95]\n"
    "v_mov_b32 v254, s69\n"
    "v_accvgpr_write_b32 a205, v254\n"
    "v_mfma_f32_16x16x32_bf16 v[92:95], a[60:63], a[136:139], v[92:95]\n"
    "ds_read_b128 a[228:231], v231 offset:0\n"
    "v_mov_b32 v254, 0\n"
    "v_mfma_f32_16x16x32_bf16 v[124:127], a[56:59], a[124:127], v[124:127]\n"
    "s_cmp_eq_u32 s60, 0\n"
    "s_cbranch_scc0 LPA\n"
    "s_mov_b64 exec, 1\n"
    "s_nop 0\n"
    "buffer_store_dwordx2 a[204:205], v254, s[56:59], s63 offen sc1\n"
    "s_mov_b64 exec, -1\n"
    "LPA:\n"
    "s_nop 0\n"
    "v_mfma_f32_16x16x32_bf16 v[124:127], a[60:63], a[140:143], v[124:127]\n"
    "buffer_load_dwordx4 v226, s[40:43], s67 offen lds\n"
    "v_mfma_f32_16x16x32_bf16 v[128:131], a[64:67], a[112:115], v[128:131]\n"
    "ds_read_b64_tr_b16 a[0:1], v233 offset:0\n"
    "ds_read_b64_tr_b16 a[2:3], v233 offset:256\n"
    "v_mfma_f32_16x16x32_bf16 v[128:131], a[68:71], a[128:131], v[128:131]\n"
    "ds_read_b64_tr_b16 a[4:5], v233 offset:8224\n"
    "ds_read_b64_tr_b16 a[6:7], v233 offset:8480\n"
    "v_mfma_f32_16x16x32_bf16 v[152:155], a[64:67], a[116:119], v[152:155]\n"
    "buffer_load_dwordx4 v227, s[40:43], s67 offen offset:1024 lds\n"
    "v_mfma_f32_16x16x32_bf16 v[152:155], a[68:71], a[132:135], v[152:155]\n"
    "ds_read_b128 a[216:219], v230 offset:2048\n"
    "s_add_u32 s90, s67, 63480\n"
    "v_mfma_f32_16x16x32_bf16 v[176:179], a[64:67], a[120:123], v[176:179]\n"
    "ds_read_b128 a[232:235], v231 offset:2048\n"
    "ds_read_b128 a[220:223], v230 offset:4096\n"
    "v_mfma_f32_16x16x32_bf16 v[176:179], a[68:71], a[136:139], v[176:179]\n"
    "buffer_load_dwordx4 v226, s[40:43], s90 offen offset:2056 lds\n"
    "v_mfma_f32_16x16x32_bf16 v[200:203], a[64:67], a[124:127], v[200:203]\n"
    "ds_read_b128 a[236:239], v231 offset:4096\n"
    "ds_read_b128 a[224:227], v230 offset:6144\n"
    "v_mfma_f32_16x16x32_bf16 v[200:203], a[68:71], a[140:143], v[200:203]\n"
    "buffer_load_dwordx4 v227, s[40:43], s90 offen offset:3080 lds\n"
    "v_mfma_f32_16x16x32_bf16 v[132:135], a[72:75], a[112:115], v[132:135]\n"
    "ds_read_b128 a[240:243], v231 offset:6144\n"
    "ds_read_b64_tr_b16 a[8:9], v233 offset:8\n"
    "v_mfma_f32_16x16x32_bf16 v[132:135], a[76:79], a[128:131], v[132:135]\n"
    "ds_read_b64_tr_b16 a[10:11], v233 offset:264\n"
    "s_add_u32 m0, s71, 40448\n"
    "v_mfma_f32_16x16x32_bf16 v[156:159], a[72:75], a[116:119], v[156:159]\n"
    "ds_read_b64_tr_b16 a[12:13], v233 offset:8232\n"
    "ds_read_b64_tr_b16 a[14:15], v233 offset:8488\n"
    "v_mfma_f32_16x16x32_bf16 v[156:159], a[76:79], a[132:135], v[156:159]\n"
    "buffer_load_dwordx4 v224, s[36:39], s66 offen lds\n"
    "v_mfma_f32_16x16x32_bf16 v[180:183], a[72:75], a[120:123], v[180:183]\n"
    "ds_read_b64_tr_b16 a[16:17], v233 offset:512\n"
    "v_mfma_f32_16x16x32_bf16 v[180:183], a[76:79], a[136:139], v[180:183]\n"
    "ds_read_b64_tr_b16 a[18:19], v233 offset:768\n"
    "v_mfma_f32_16x16x32_bf16 v[204:207], a[72:75], a[124:127], v[204:207]\n"
    "s_add_u32 s91, s66, 7168\n"
    "ds_read_b64_tr_b16 a[20:21], v233 offset:8736\n"
    "v_mfma_f32_16x16x32_bf16 v[204:207], a[76:79], a[140:143], v[204:207]\n"
    "v_mfma_f32_16x16x32_bf16 v[136:139], a[80:83], a[112:115], v[136:139]\n"
    "buffer_load_dwordx4 v225, s[36:39], s91 offen offset:1024 lds\n"
    "v_mfma_f32_16x16x32_bf16 v[136:139], a[84:87], a[128:131], v[136:139]\n"
    "ds_read_b64_tr_b16 a[22:23], v233 offset:8992\n"
    "ds_read_b64_tr_b16 a[24:25], v233 offset:520\n"
    "v_mfma_f32_16x16x32_bf16 v[160:163], a[80:83], a[116:119], v[160:163]\n"
    "v_mfma_f32_16x16x32_bf16 v[160:163], a[84:87], a[132:135], v[160:163]\n"
    "ds_read_b64_tr_b16 a[26:27], v233 offset:776\n"
    "s_add_u32 s90, s66, 14336\n"
    "v_mfma_f32_16x16x32_bf16 v[184:187], a[80:83], a[120:123], v[184:187]\n"
    "ds_read_b64_tr_b16 a[28:29], v233 offset:8744\n"
    "v_mfma_f32_16x16x32_bf16 v[184:187], a[84:87], a[136:139], v[184:187]\n"
    "buffer_load_dwordx4 v224, s[36:39], s90 offen offset:2048 lds\n"
    "v_mfma_f32_16x16x32_bf16 v[208:211], a[80:83], a[124:127], v[208:211]\n"
    "ds_read_b64_tr_b16 a[30:31], v233 offset:9000\n"
    "v_mfma_f32_16x16x32_bf16 v[208:211], a[84:87], a[140:143], v[208:211]\n"
    "ds_read_b64_tr_b16 a[32:33], v233 offset:1024\n"
    "v_mfma_f32_16x16x32_bf16 v[140:143], a[88:91], a[112:115], v[140:143]\n"
    "ds_read_b64_tr_b16 a[34:35], v233 offset:1280\n"
    "v_mfma_f32_16x16x32_bf16 v[140:143], a[92:95], a[128:131], v[140:143]\n"
    "s_add_u32 s91, s66, 21504\n"
    "v_mfma_f32_16x16x32_bf16 v[164:167], a[88:91], a[116:119], v[164:167]\n"
    "ds_read_b64_tr_b16 a[36:37], v233 offset:9248\n"
    "v_mfma_f32_16x16x32_bf16 v[164:167], a[92:95], a[132:135], v[164:167]\n"
    "buffer_load_dwordx4 v225, s[36:39], s91 offen offset:3072 lds\n"
    "v_mfma_f32_16x16x32_bf16 v[188:191], a[88:91], a[120:123], v[188:191]\n"
    "ds_read_b64_tr_b16 a[38:39], v233 offset:9504\n"
    "ds_read_b64_tr_b16 a[40:41], v233 offset:1032\n"
    "v_mfma_f32_16x16x32_bf16 v[188:191], a[92:95], a[136:139], v[188:191]\n"
    "v_mfma_f32_16x16x32_bf16 v[212:215], a[88:91], a[124:127], v[212:215]\n"
    "ds_read_b64_tr_b16 a[42:43], v233 offset:1288\n"
    "v_mfma_f32_16x16x32_bf16 v[212:215], a[92:95], a[140:143], v[212:215]\n"
    "s_add_u32 m0, s71, 44544\n"
    "ds_read_b64_tr_b16 a[44:45], v233 offset:9256\n"
    "v_mfma_f32_16x16x32_bf16 v[144:147], a[96:99], a[112:115], v[144:147]\n"
    "s_add_u32 s90, s66, 32768\n"
    "v_mfma_f32_16x16x32_bf16 v[144:147], a[100:103], a[128:131], v[144:147]\n"
    "ds_read_b64_tr_b16 a[46:47], v233 offset:9512\n"
    "v_mfma_f32_16x16x32_bf16 v[168:171], a[96:99], a[116:119], v[168:171]\n"
    "ds_read_b64_tr_b16 a[48:49], v233 offset:1536\n"
    "v_mfma_f32_16x16x32_bf16 v[168:171], a[100:103], a[132:135], v[168:171]\n"
    "buffer_load_dwordx4 v224, s[36:39], s90 offen lds\n"
    "v_mfma_f32_16x16x32_bf16 v[192:195], a[96:99], a[120:123], v[192:195]\n"
    "ds_read_b64_tr_b16 a[50:51], v233 offset:1792\n"
    "v_mfma_f32_16x16x32_bf16 v[192:195], a[100:103], a[136:139], v[192:195]\n"
    "ds_read_b64_tr_b16 a[52:53], v233 offset:9760\n"
    "v_mfma_f32_16x16x32_bf16 v[216:219], a[96:99], a[124:127], v[216:219]\n"
    "s_add_u32 s91, s66, 39936\n"
    "ds_read_b64_tr_b16 a[54:55], v233 offset:10016\n"
    "v_mfma_f32_16x16x32_bf16 v[216:219], a[100:103], a[140:143], v[216:219]\n"
    "v_mfma_f32_16x16x32_bf16 v[148:151], a[104:107], a[112:115], v[148:151]\n"
    "buffer_load_dwordx4 v225, s[36:39], s91 offen offset:1024 lds\n"
    "v_mfma_f32_16x16x32_bf16 v[148:151], a[108:111], a[128:131], v[148:151]\n"
    "v_mfma_f32_16x16x32_bf16 v[172:175], a[104:107], a[116:119], v[172:175]\n"
    "v_mfma_f32_16x16x32_bf16 v[172:175], a[108:111], a[132:135], v[172:175]\n"
    "v_mfma_f32_16x16x32_bf16 v[196:199], a[104:107], a[120:123], v[196:199]\n"
    "s_add_u32 s90, s66, 47104\n"
    "v_mfma_f32_16x16x32_bf16 v[196:199], a[108:111], a[136:139], v[196:199]\n"
    "v_mfma_f32_16x16x32_bf16 v[220:223], a[104:107], a[124:127], v[220:223]\n"
    "v_mfma_f32_16x16x32_bf16 v[220:223], a[108:111], a[140:143], v[220:223]\n"
    "s_waitcnt lgkmcnt(15)\n"
    "v_mfma_f32_16x16x32_bf16 v[0:3], a[0:3], a[212:215], v[0:3]\n"
    "ds_read_b64_tr_b16 a[56:57], v233 offset:1544\n"
    "ds_read_b64_tr_b16 a[58:59], v233 offset:1800\n"
    "v_mfma_f32_16x16x32_bf16 v[0:3], a[4:7], a[228:231], v[0:3]\n"
    "buffer_load_dwordx4 v224, s[36:39], s90 offen offset:2048 lds\n"
    "v_mfma_f32_16x16x32_bf16 v[32:35], a[0:3], a[216:219], v[32:35]\n"
    "s_add_u32 s91, s66, 54272\n"
    "ds_read_b64_tr_b16 a[60:61], v233 offset:9768\n"
    "v_mfma_f32_16x16x32_bf16 v[32:35], a[4:7], a[232:235], v[32:35]\n"
    "buffer_load_dwordx4 v225, s[36:39], s91 offen offset:3072 lds\n"
    "v_mfma_f32_16x16x32_bf16 v[64:67], a[0:3], a[220:223], v[64:67]\n"
    "ds_read_b64_tr_b16 a[62:63], v233 offset:10024\n"
    "v_mfma_f32_16x16x32_bf16 v[64:67], a[4:7], a[236:239], v[64:67]\n"
    "ds_read_b64_tr_b16 a[64:65], v236 offset:0\n"
    "s_add_u32 m0, s77, 89856\n"
    "v_mfma_f32_16x16x32_bf16 v[96:99], a[0:3], a[224:227], v[96:99]\n"
    "ds_read_b64_tr_b16 a[66:67], v237 offset:0\n"
    "v_mfma_f32_16x16x32_bf16 v[96:99], a[4:7], a[240:243], v[96:99]\n"
    "buffer_load_dwordx4 v251, s[52:55], s76 offen lds\n"
    "v_mfma_f32_16x16x32_bf16 v[4:7], a[8:11], a[212:215], v[4:7]\n"
    "ds_read_b64_tr_b16 a[68:69], v236 offset:3072\n"
    "v_mfma_f32_16x16x32_bf16 v[4:7], a[12:15], a[228:231], v[4:7]\n"
    "v_mfma_f32_16x16x32_bf16 v[36:39], a[8:11], a[216:219], v[36:39]\n"
    "ds_read_b64_tr_b16 a[70:71], v237 offset:3072\n"
    "v_mfma_f32_16x16x32_bf16 v[36:39], a[12:15], a[232:235], v[36:39]\n"
    "buffer_load_dwordx4 v252, s[52:55], s76 offen offset:1024 lds\n"
    "v_mfma_f32_16x16x32_bf16 v[68:71], a[8:11], a[220:223], v[68:71]\n"
    "ds_read_b64_tr_b16 a[72:73], v236 offset:6144\n"
    "v_mfma_f32_16x16x32_bf16 v[68:71], a[12:15], a[236:239], v[68:71]\n"
    "buffer_load_dwordx4 v253, s[52:55], s76 offen offset:2048 lds\n"
    "v_mfma_f32_16x16x32_bf16 v[100:103], a[8:11], a[224:227], v[100:103]\n"
    "ds_read_b64_tr_b16 a[74:75], v237 offset:6144\n"
    "v_mfma_f32_16x16x32_bf16 v[100:103], a[12:15], a[240:243], v[100:103]\n"
    "ds_read_b64_tr_b16 a[76:77], v236 offset:9216\n"
    "v_mfma_f32_16x16x32_bf16 v[8:11], a[16:19], a[212:215], v[8:11]\n"
    "s_add_u32 s97, s97, 1\n"
    "v_mfma_f32_16x16x32_bf16 v[8:11], a[20:23], a[228:231], v[8:11]\n"
    "ds_read_b64_tr_b16 a[78:79], v237 offset:9216\n"
    "v_mfma_f32_16x16x32_bf16 v[40:43], a[16:19], a[216:219], v[40:43]\n"
    "s_and_b32 s97, s97, 7\n"
    "v_mfma_f32_16x16x32_bf16 v[40:43], a[20:23], a[232:235], v[40:43]\n"
    "ds_read_b64_tr_b16 a[80:81], v236 offset:256\n"
    "v_mfma_f32_16x16x32_bf16 v[72:75], a[16:19], a[220:223], v[72:75]\n"
    "s_lshl_b32 s66, s97, 7\n"
    "v_mfma_f32_16x16x32_bf16 v[72:75], a[20:23], a[236:239], v[72:75]\n"
    "ds_read_b64_tr_b16 a[82:83], v237 offset:256\n"
    "s_lshl_b32 s67, s97, 19\n"
    "v_mfma_f32_16x16x32_bf16 v[104:107], a[16:19], a[224:227], v[104:107]\n"
    "ds_read_b64_tr_b16 a[84:85], v236 offset:3328\n"
    "v_mfma_f32_16x16x32_bf16 v[104:107], a[20:23], a[240:243], v[104:107]\n"
    "s_mul_i32 s76, s97, 176128\n"
    "s_waitcnt lgkmcnt(15)\n"
    "v_mfma_f32_16x16x32_bf16 v[12:15], a[24:27], a[212:215], v[12:15]\n"
    "ds_read_b64_tr_b16 a[86:87], v237 offset:3328\n"
    "v_min_u32 v254, 7, v238\n"
    "v_mfma_f32_16x16x32_bf16 v[12:15], a[28:31], a[228:231], v[12:15]\n"
    "v_mfma_f32_16x16x32_bf16 v[44:47], a[24:27], a[216:219], v[44:47]\n"
    "s_and_b32 s65, s61, 7\n"
    "ds_read_b64_tr_b16 a[88:89], v236 offset:6400\n"
    "v_mfma_f32_16x16x32_bf16 v[44:47], a[28:31], a[232:235], v[44:47]\n"
    "v_mfma_f32_16x16x32_bf16 v[76:79], a[24:27], a[220:223], v[76:79]\n"
    "s_lshl_b32 s65, s65, 13\n"
    "ds_read_b64_tr_b16 a[90:91], v237 offset:6400\n"
    "v_mfma_f32_16x16x32_bf16 v[76:79], a[28:31], a[236:239], v[76:79]\n"
    "v_lshlrev_b32 v254, 10, v254\n"
    "v_mfma_f32_16x16x32_bf16 v[108:111], a[24:27], a[224:227], v[108:111]\n"
    "ds_read_b64_tr_b16 a[92:93], v236 offset:9472\n"
    "v_mfma_f32_16x16x32_bf16 v[108:111], a[28:31], a[240:243], v[108:111]\n"
    "s_add_u32 s65, s65, s66\n"
    "ds_read_b64_tr_b16 a[94:95], v237 offset:9472\n"
    "v_mfma_f32_16x16x32_bf16 v[16:19], a[32:35], a[212:215], v[16:19]\n"
    "s_waitcnt vmcnt(19)\n"
    "buffer_load_dword a207, v254, s[36:39], s65 offen\n"
    "v_mfma_f32_16x16x32_bf16 v[16:19], a[36:39], a[228:231], v[16:19]\n"
    "ds_read_b64_tr_b16 a[96:97], v236 offset:512\n"
    "v_mfma_f32_16x16x32_bf16 v[48:51], a[32:35], a[216:219], v[48:51]\n"
    "v_mfma_f32_16x16x32_bf16 v[48:51], a[36:39], a[232:235], v[48:51]\n"
    "ds_read_b64_tr_b16 a[98:99], v237 offset:512\n"
    "v_mfma_f32_16x16x32_bf16 v[80:83], a[32:35], a[220:223], v[80:83]\n"
    "v_mfma_f32_16x16x32_bf16 v[80:83], a[36:39], a[236:239], v[80:83]\n"
    "ds_read_b64_tr_b16 a[100:101], v236 offset:3584\n"
    "v_mfma_f32_16x16x32_bf16 v[112:115], a[32:35], a[224:227], v[112:115]\n"
    "v_mfma_f32_16x16x32_bf16 v[112:115], a[36:39], a[240:243], v[112:115]\n"
    "ds_read_b64_tr_b16 a[102:103], v237 offset:3584\n"
    "v_mfma_f32_16x16x32_bf16 v[20:23], a[40:43], a[212:215], v[20:23]\n"
    "ds_read_b64_tr_b16 a[104:105], v236 offset:6656\n"
    "v_mfma_f32_16x16x32_bf16 v[20:23], a[44:47], a[228:231], v[20:23]\n"
    "v_mfma_f32_16x16x32_bf16 v[52:55], a[40:43], a[216:219], v[52:55]\n"
    "ds_read_b64_tr_b16 a[106:107], v237 offset:6656\n"
    "v_mfma_f32_16x16x32_bf16 v[52:55], a[44:47], a[232:235], v[52:55]\n"
    "v_mfma_f32_16x16x32_bf16 v[84:87], a[40:43], a[220:223], v[84:87]\n"
    "ds_read_b64_tr_b16 a[108:109], v236 offset:9728\n"
    "v_mfma_f32_16x16x32_bf16 v[84:87], a[44:47], a[236:239], v[84:87]\n"
    "v_mfma_f32_16x16x32_bf16 v[116:119], a[40:43], a[224:227], v[116:119]\n"
    "ds_read_b64_tr_b16 a[110:111], v237 offset:9728\n"
    "v_mfma_f32_16x16x32_bf16 v[116:119], a[44:47], a[240:243], v[116:119]\n"
    "v_mfma_f32_16x16x32_bf16 v[24:27], a[48:51], a[212:215], v[24:27]\n"
    "v_mfma_f32_16x16x32_bf16 v[24:27], a[52:55], a[228:231], v[24:27]\n"
    "v_mfma_f32_16x16x32_bf16 v[56:59], a[48:51], a[216:219], v[56:59]\n"
    "v_mfma_f32_16x16x32_bf16 v[56:59], a[52:55], a[232:235], v[56:59]\n"
    "v_mfma_f32_16x16x32_bf16 v[88:91], a[48:51], a[220:223], v[88:91]\n"
    "v_mfma_f32_16x16x32_bf16 v[88:91], a[52:55], a[236:239], v[88:91]\n"
    "v_mfma_f32_16x16x32_bf16 v[120:123], a[48:51], a[224:227], v[120:123]\n"
    "v_mfma_f32_16x16x32_bf16 v[120:123], a[52:55], a[240:243], v[120:123]\n"
    "s_waitcnt vmcnt(1)\n"
    "s_waitcnt lgkmcnt(0)\n"
    "s_barrier\n"
    "v_mfma_f32_16x16x32_bf16 v[28:31], a[56:59], a[212:215], v[28:31]\n"
    "ds_read_b128 a[112:115], v228 offset:0\n"
    "s_add_u32 m0, s72, 134912\n"
    "v_mfma_f32_16x16x32_bf16 v[28:31], a[60:63], a[228:231], v[28:31]\n"
    "ds_read_b128 a[128:131], v229 offset:0\n"
    "ds_read_b64_tr_b16 a[0:1], v232 offset:0\n"
    "v_mfma_f32_16x16x32_bf16 v[60:63], a[56:59], a[216:219], v[60:63]\n"
    "buffer_load_dwordx4 v226, s[40:43], s67 offen lds\n"
    "v_mfma_f32_16x16x32_bf16 v[60:63], a[60:63], a[232:235], v[60:63]\n"
    "ds_read_b64_tr_b16 a[2:3], v232 offset:256\n"
    "ds_read_b64_tr_b16 a[4:5], v232 offset:8224\n"
    "v_mfma_f32_16x16x32_bf16 v[92:95], a[56:59], a[220:223], v[92:95]\n"
    "buffer_load_dwordx4 v227, s[40:43], s67 offen offset:1024 lds\n"
    "v_mfma_f32_16x16x32_bf16 v[92:95], a[60:63], a[236:239], v[92:95]\n"
    "ds_read_b64_tr_b16 a[6:7], v232 offset:8480\n"
    "v_mfma_f32_16x16x32_bf16 v[124:127], a[56:59], a[224:227], v[124:127]\n"
    "ds_read_b128 a[116:119], v228 offset:2048\n"
    "v_mfma_f32_16x16x32_bf16 v[124:127], a[60:63], a[240:243], v[124:127]\n"
    "s_add_u32 s90, s67, 63480\n"
    "ds_read_b128 a[132:135], v229 offset:2048\n"
    "v_mfma_f32_16x16x32_bf16 v[128:131], a[64:67], a[212:215], v[128:131]\n"
    "v_mfma_f32_16x16x32_bf16 v[128:131], a[68:71], a[228:231], v[128:131]\n"
    "ds_read_b128 a[120:123], v228 offset:4096\n"
    "v_mfma_f32_16x16x32_bf16 v[152:155], a[64:67], a[216:219], v[152:155]\n"
    "buffer_load_dwordx4 v226, s[40:43], s90 offen offset:2056 lds\n"
    "v_mfma_f32_16x16x32_bf16 v[152:155], a[68:71], a[232:235], v[152:155]\n"
    "ds_read_b128 a[136:139], v229 offset:4096\n"
    "ds_read_b128 a[124:127], v228 offset:6144\n"
    "v_mfma_f32_16x16x32_bf16 v[176:179], a[64:67], a[220:223], v[176:179]\n"
    "buffer_load_dwordx4 v227, s[40:43], s90 offen offset:3080 lds\n"
    "v_mfma_f32_16x16x32_bf16 v[176:179], a[68:71], a[236:239], v[176:179]\n"
    "ds_read_b128 a[140:143], v229 offset:6144\n"
    "v_mfma_f32_16x16x32_bf16 v[200:203], a[64:67], a[224:227], v[200:203]\n"
    "ds_read_b64_tr_b16 a[8:9], v232 offset:8\n"
    "v_mfma_f32_16x16x32_bf16 v[200:203], a[68:71], a[240:243], v[200:203]\n"
    "v_mfma_f32_16x16x32_bf16 v[132:135], a[72:75], a[212:215], v[132:135]\n"
    "ds_read_b64_tr_b16 a[10:11], v232 offset:264\n"
    "s_add_u32 m0, s71, 102144\n"
    "v_mfma_f32_16x16x32_bf16 v[132:135], a[76:79], a[228:231], v[132:135]\n"
    "ds_read_b64_tr_b16 a[12:13], v232 offset:8232\n"
    "v_mfma_f32_16x16x32_bf16 v[156:159], a[72:75], a[216:219], v[156:159]\n"
    "buffer_load_dwordx4 v224, s[36:39], s66 offen lds\n"
    "v_mfma_f32_16x16x32_bf16 v[156:159], a[76:79], a[232:235], v[156:159]\n"
    "ds_read_b64_tr_b16 a[14:15], v232 offset:8488\n"
    "v_mfma_f32_16x16x32_bf16 v[180:183], a[72:75], a[220:223], v[180:183]\n"
    "ds_read_b64_tr_b16 a[16:17], v232 offset:512\n"
    "v_mfma_f32_16x16x32_bf16 v[180:183], a[76:79], a[236:239], v[180:183]\n"
    "ds_read_b64_tr_b16 a[18:19], v232 offset:768\n"
    "v_mfma_f32_16x16x32_bf16 v[204:207], a[72:75], a[224:227], v[204:207]\n"
    "s_add_u32 s91, s66, 7168\n"
    "ds_read_b64_tr_b16 a[20:21], v232 offset:8736\n"
    "v_mfma_f32_16x16x32_bf16 v[204:207], a[76:79], a[240:243], v[204:207]\n"
    "v_mfma_f32_16x16x32_bf16 v[136:139], a[80:83], a[212:215], v[136:139]\n"
    "buffer_load_dwordx4 v225, s[36:39], s91 offen offset:1024 lds\n"
    "v_mfma_f32_16x16x32_bf16 v[136:139], a[84:87], a[228:231], v[136:139]\n"
    "ds_read_b64_tr_b16 a[22:23], v232 offset:8992\n"
    "ds_read_b64_tr_b16 a[24:25], v232 offset:520\n"
    "v_mfma_f32_16x16x32_bf16 v[160:163], a[80:83], a[216:219], v[160:163]\n"
    "v_mfma_f32_16x16x32_bf16 v[160:163], a[84:87], a[232:235], v[160:163]\n"
    "ds_read_b64_tr_b16 a[26:27], v232 offset:776\n"
    "s_add_u32 s90, s66, 14336\n"
    "v_mfma_f32_16x16x32_bf16 v[184:187], a[80:83], a[220:223], v[184:187]\n"
    "ds_read_b64_tr_b16 a[28:29], v232 offset:8744\n"
    "v_mfma_f32_16x16x32_bf16 v[184:187], a[84:87], a[236:239], v[184:187]\n"
    "buffer_load_dwordx4 v224, s[36:39], s90 offen offset:2048 lds\n"
    "v_mfma_f32_16x16x32_bf16 v[208:211], a[80:83], a[224:227], v[208:211]\n"
    "ds_read_b64_tr_b16 a[30:31], v232 offset:9000\n"
    "v_mfma_f32_16x16x32_bf16 v[208:211], a[84:87], a[240:243], v[208:211]\n"
    "ds_read_b64_tr_b16 a[32:33], v232 offset:1024\n"
    "v_mfma_f32_16x16x32_bf16 v[140:143], a[88:91], a[212:215], v[140:143]\n"
    "ds_read_b64_tr_b16 a[34:35], v232 offset:1280\n"
    "v_mfma_f32_16x16x32_bf16 v[140:143], a[92:95], a[228:231], v[140:143]\n"
    "s_add_u32 s91, s66, 21504\n"
    "v_mfma_f32_16x16x32_bf16 v[164:167], a[88:91], a[216:219], v[164:167]\n"
    "ds_read_b64_tr_b16 a[36:37], v232 offset:9248\n"
    "v_mfma_f32_16x16x32_bf16 v[164:167], a[92:95], a[232:235], v[164:167]\n"
    "buffer_load_dwordx4 v225, s[36:39], s91 offen offset:3072 lds\n"
    "v_mfma_f32_16x16x32_bf16 v[188:191], a[88:91], a[220:223], v[188:191]\n"
    "ds_read_b64_tr_b16 a[38:39], v232 offset:9504\n"
    "ds_read_b64_tr_b16 a[40:41], v232 offset:1032\n"
    "v_mfma_f32_16x16x32_bf16 v[188:191], a[92:95], a[236:239], v[188:191]\n"
    "v_mfma_f32_16x16x32_bf16 v[212:215], a[88:91], a[224:227], v[212:215]\n"
    "ds_read_b64_tr_b16 a[42:43], v232 offset:1288\n"
    "v_mfma_f32_16x16x32_bf16 v[212:215], a[92:95], a[240:243], v[212:215]\n"
    "s_add_u32 m0, s71, 106240\n"
    "ds_read_b64_tr_b16 a[44:45], v232 offset:9256\n"
    "v_mfma_f32_16x16x32_bf16 v[144:147], a[96:99], a[212:215], v[144:147]\n"
    "s_add_u32 s90, s66, 32768\n"
    "v_mfma_f32_16x16x32_bf16 v[144:147], a[100:103], a[228:231], v[144:147]\n"
    "ds_read_b64_tr_b16 a[46:47], v232 offset:9512\n"
    "v_mfma_f32_16x16x32_bf16 v[168:171], a[96:99], a[216:219], v[168:171]\n"
    "ds_read_b64_tr_b16 a[48:49], v232 offset:1536\n"
    "v_mfma_f32_16x16x32_bf16 v[168:171], a[100:103], a[232:235], v[168:171]\n"
    "buffer_load_dwordx4 v224, s[36:39], s90 offen lds\n"
    "v_mfma_f32_16x16x32_bf16 v[192:195], a[96:99], a[220:223], v[192:195]\n"
    "ds_read_b64_tr_b16 a[50:51], v232 offset:1792\n"
    "v_mfma_f32_16x16x32_bf16 v[192:195], a[100:103], a[236:239], v[192:195]\n"
    "ds_read_b64_tr_b16 a[52:53], v232 offset:9760\n"
    "v_mfma_f32_16x16x32_bf16 v[216:219], a[96:99], a[224:227], v[216:219]\n"
    "s_add_u32 s91, s66, 39936\n"
    "ds_read_b64_tr_b16 a[54:55], v232 offset:10016\n"
    "v_mfma_f32_16x16x32_bf16 v[216:219], a[100:103], a[240:243], v[216:219]\n"
    "v_mfma_f32_16x16x32_bf16 v[148:151], a[104:107], a[212:215], v[148:151]\n"
    "buffer_load_dwordx4 v225, s[36:39], s91 offen offset:1024 lds\n"
    "v_mfma_f32_16x16x32_bf16 v[148:151], a[108:111], a[228:231], v[148:151]\n"
    "v_mfma_f32_16x16x32_bf16 v[172:175], a[104:107], a[216:219], v[172:175]\n"
    "v_mfma_f32_16x16x32_bf16 v[172:175], a[108:111], a[232:235], v[172:175]\n"
    "v_mfma_f32_16x16x32_bf16 v[196:199], a[104:107], a[220:223], v[196:199]\n"
    "s_add_u32 s90, s66, 47104\n"
    "v_mfma_f32_16x16x32_bf16 v[196:199], a[108:111], a[236:239], v[196:199]\n"
    "v_mfma_f32_16x16x32_bf16 v[220:223], a[104:107], a[224:227], v[220:223]\n"
    "v_mfma_f32_16x16x32_bf16 v[220:223], a[108:111], a[240:243], v[220:223]\n"
    "s_waitcnt lgkmcnt(15)\n"
    "v_mfma_f32_16x16x32_bf16 v[0:3], a[0:3], a[112:115], v[0:3]\n"
    "ds_read_b64_tr_b16 a[56:57], v232 offset:1544\n"
    "ds_read_b64_tr_b16 a[58:59], v232 offset:1800\n"
    "v_mfma_f32_16x16x32_bf16 v[0:3], a[4:7], a[128:131], v[0:3]\n"
    "buffer_load_dwordx4 v224, s[36:39], s90 offen offset:2048 lds\n"
    "v_mfma_f32_16x16x32_bf16 v[32:35], a[0:3], a[116:119], v[32:35]\n"
    "s_add_u32 s91, s66, 54272\n"
    "ds_read_b64_tr_b16 a[60:61], v232 offset:9768\n"
    "v_mfma_f32_16x16x32_bf16 v[32:35], a[4:7], a[132:135], v[32:35]\n"
    "buffer_load_dwordx4 v225, s[36:39], s91 offen offset:3072 lds\n"
    "v_mfma_f32_16x16x32_bf16 v[64:67], a[0:3], a[120:123], v[64:67]\n"
    "ds_read_b64_tr_b16 a[62:63], v232 offset:10024\n"
    "s_add_u32 m0, s77, 151552\n"
    "v_mfma_f32_16x16x32_bf16 v[64:67], a[4:7], a[136:139], v[64:67]\n"
    "ds_read_b64_tr_b16 a[64:65], v234 offset:0\n"
    "v_mfma_f32_16x16x32_bf16 v[96:99], a[0:3], a[124:127], v[96:99]\n"
    "buffer_load_dwordx4 v251, s[52:55], s76 offen lds\n"
    "v_mfma_f32_16x16x32_bf16 v[96:99], a[4:7], a[140:143], v[96:99]\n"
    "ds_read_b64_tr_b16 a[66:67], v235 offset:0\n"
    "v_mfma_f32_16x16x32_bf16 v[4:7], a[8:11], a[112:115], v[4:7]\n"
    "buffer_load_dwordx4 v252, s[52:55], s76 offen offset:1024 lds\n"
    "v_mfma_f32_16x16x32_bf16 v[4:7], a[12:15], a[128:131], v[4:7]\n"
    "ds_read_b64_tr_b16 a[68:69], v234 offset:3072\n"
    "v_mfma_f32_16x16x32_bf16 v[36:39], a[8:11], a[116:119], v[36:39]\n"
    "ds_read_b64_tr_b16 a[70:71], v235 offset:3072\n"
    "v_mfma_f32_16x16x32_bf16 v[36:39], a[12:15], a[132:135], v[36:39]\n"
    "buffer_load_dwordx4 v253, s[52:55], s76 offen offset:2048 lds\n"
    "v_mfma_f32_16x16x32_bf16 v[68:71], a[8:11], a[120:123], v[68:71]\n"
    "ds_read_b64_tr_b16 a[72:73], v234 offset:6144\n"
    "s_add_u32 s97, s97, 1\n"
    "v_mfma_f32_16x16x32_bf16 v[68:71], a[12:15], a[136:139], v[68:71]\n"
    "v_mfma_f32_16x16x32_bf16 v[100:103], a[8:11], a[124:127], v[100:103]\n"
    "ds_read_b64_tr_b16 a[74:75], v235 offset:6144\n"
    "s_and_b32 s97, s97, 7\n"
    "v_mfma_f32_16x16x32_bf16 v[100:103], a[12:15], a[140:143], v[100:103]\n"
    "s_lshl_b32 s66, s97, 7\n"
    "ds_read_b64_tr_b16 a[76:77], v234 offset:9216\n"
    "v_mfma_f32_16x16x32_bf16 v[8:11], a[16:19], a[112:115], v[8:11]\n"
    "s_lshl_b32 s67, s97, 19\n"
    "v_mfma_f32_16x16x32_bf16 v[8:11], a[20:23], a[128:131], v[8:11]\n"
    "ds_read_b64_tr_b16 a[78:79], v235 offset:9216\n"
    "s_mul_i32 s76, s97, 176128\n"
    "v_mfma_f32_16x16x32_bf16 v[40:43], a[16:19], a[116:119], v[40:43]\n"
    "v_mfma_f32_16x16x32_bf16 v[40:43], a[20:23], a[132:135], v[40:43]\n"
    "ds_read_b64_tr_b16 a[80:81], v234 offset:256\n"
    "v_min_u32 v254, 7, v238\n"
    "v_mfma_f32_16x16x32_bf16 v[72:75], a[16:19], a[120:123], v[72:75]\n"
    "s_and_b32 s65, s61, 7\n"
    "v_mfma_f32_16x16x32_bf16 v[72:75], a[20:23], a[136:139], v[72:75]\n"
    "ds_read_b64_tr_b16 a[82:83], v235 offset:256\n"
    "s_lshl_b32 s65, s65, 13\n"
    "v_mfma_f32_16x16x32_bf16 v[104:107], a[16:19], a[124:127], v[104:107]\n"
    "ds_read_b64_tr_b16 a[84:85], v234 offset:3328\n"
    "v_lshlrev_b32 v254, 10, v254\n"
    "v_mfma_f32_16x16x32_bf16 v[104:107], a[20:23], a[140:143], v[104:107]\n"
    "s_waitcnt lgkmcnt(15)\n"
    "v_mfma_f32_16x16x32_bf16 v[12:15], a[24:27], a[112:115], v[12:15]\n"
    "s_add_u32 s65, s65, s66\n"
    "ds_read_b64_tr_b16 a[86:87], v235 offset:3328\n"
    "v_mfma_f32_16x16x32_bf16 v[12:15], a[28:31], a[128:131], v[12:15]\n"
    "s_waitcnt vmcnt(15)\n"
    "buffer_load_dword a207, v254, s[36:39], s65 offen\n"
    "v_mfma_f32_16x16x32_bf16 v[44:47], a[24:27], a[116:119], v[44:47]\n"
    "ds_read_b64_tr_b16 a[88:89], v234 offset:6400\n"
    "v_mfma_f32_16x16x32_bf16 v[44:47], a[28:31], a[132:135], v[44:47]\n"
    "v_mfma_f32_16x16x32_bf16 v[76:79], a[24:27], a[120:123], v[76:79]\n"
    "ds_read_b64_tr_b16 a[90:91], v235 offset:6400\n"
    "v_mfma_f32_16x16x32_bf16 v[76:79], a[28:31], a[136:139], v[76:79]\n"
    "v_mfma_f32_16x16x32_bf16 v[108:111], a[24:27], a[124:127], v[108:111]\n"
    "s_add_u32 m0, s74, 0\n"
    "ds_read_b64_tr_b16 a[92:93], v234 offset:9472\n"
    "v_mfma_f32_16x16x32_bf16 v[108:111], a[28:31], a[140:143], v[108:111]\n"
    "buffer_load_dwordx4 v245, s[44:47], s98 offen lds\n"
    "v_mfma_f32_16x16x32_bf16 v[16:19], a[32:35], a[112:115], v[16:19]\n"
    "ds_read_b64_tr_b16 a[94:95], v235 offset:9472\n"
    "v_mfma_f32_16x16x32_bf16 v[16:19], a[36:39], a[128:131], v[16:19]\n"
    "ds_read_b64_tr_b16 a[96:97], v234 offset:512\n"
    "v_mfma_f32_16x16x32_bf16 v[48:51], a[32:35], a[116:119], v[48:51]\n"
    "v_mfma_f32_16x16x32_bf16 v[48:51], a[36:39], a[132:135], v[48:51]\n"
    "buffer_load_dwordx4 v246, s[44:47], s98 offen offset:1024 lds\n"
    "v_mfma_f32_16x16x32_bf16 v[80:83], a[32:35], a[120:123], v[80:83]\n"
    "ds_read_b64_tr_b16 a[98:99], v235 offset:512\n"
    "v_mfma_f32_16x16x32_bf16 v[80:83], a[36:39], a[136:139], v[80:83]\n"
    "ds_read_b64_tr_b16 a[100:101], v234 offset:3584\n"
    "v_mfma_f32_16x16x32_bf16 v[112:115], a[32:35], a[124:127], v[112:115]\n"
    "v_mfma_f32_16x16x32_bf16 v[112:115], a[36:39], a[140:143], v[112:115]\n"
    "ds_read_b64_tr_b16 a[102:103], v235 offset:3584\n"
    "v_mfma_f32_16x16x32_bf16 v[20:23], a[40:43], a[112:115], v[20:23]\n"
    "buffer_load_dwordx4 v247, s[44:47], s98 offen offset:2048 lds\n"
    "v_mfma_f32_16x16x32_bf16 v[20:23], a[44:47], a[128:131], v[20:23]\n"
    "ds_read_b64_tr_b16 a[104:105], v234 offset:6656\n"
    "v_mfma_f32_16x16x32_bf16 v[52:55], a[40:43], a[116:119], v[52:55]\n"
    "ds_read_b64_tr_b16 a[106:107], v235 offset:6656\n"
    "v_mfma_f32_16x16x32_bf16 v[52:55], a[44:47], a[132:135], v[52:55]\n"
    "v_mfma_f32_16x16x32_bf16 v[84:87], a[40:43], a[120:123], v[84:87]\n"
    "ds_read_b64_tr_b16 a[108:109], v234 offset:9728\n"
    "v_mfma_f32_16x16x32_bf16 v[84:87], a[44:47], a[136:139], v[84:87]\n"
    "buffer_load_dwordx4 v248, s[44:47], s98 offen offset:3072 lds\n"
    "v_mfma_f32_16x16x32_bf16 v[116:119], a[40:43], a[124:127], v[116:119]\n"
    "ds_read_b64_tr_b16 a[110:111], v235 offset:9728\n"
    "v_mfma_f32_16x16x32_bf16 v[116:119], a[44:47], a[140:143], v[116:119]\n"
    "v_mfma_f32_16x16x32_bf16 v[24:27], a[48:51], a[112:115], v[24:27]\n"
    "v_mfma_f32_16x16x32_bf16 v[24:27], a[52:55], a[128:131], v[24:27]\n"
    "s_add_u32 s98, s98, 16384\n"
    "v_mfma_f32_16x16x32_bf16 v[56:59], a[48:51], a[116:119], v[56:59]\n"
    "v_mfma_f32_16x16x32_bf16 v[56:59], a[52:55], a[132:135], v[56:59]\n"
    "s_and_b32 s98, s98, 0x1ffff\n"
    "v_mfma_f32_16x16x32_bf16 v[88:91], a[48:51], a[120:123], v[88:91]\n"
    "v_mfma_f32_16x16x32_bf16 v[88:91], a[52:55], a[136:139], v[88:91]\n"
    "v_mfma_f32_16x16x32_bf16 v[120:123], a[48:51], a[124:127], v[120:123]\n"
    "v_mfma_f32_16x16x32_bf16 v[120:123], a[52:55], a[140:143], v[120:123]\n"
    "s_waitcnt vmcnt(5)\n"
    "s_waitcnt lgkmcnt(0)\n"
    "s_barrier\n"
    "v_mfma_f32_16x16x32_bf16 v[28:31], a[56:59], a[112:115], v[28:31]\n"
    "ds_read_b128 a[212:215], v230 offset:0\n"
    "s_add_u32 m0, s72, 73216\n"
    "v_mfma_f32_16x16x32_bf16 v[28:31], a[60:63], a[128:131], v[28:31]\n"
    "ds_read_b128 a[228:231], v231 offset:0\n"
    "ds_read_b64_tr_b16 a[0:1], v233 offset:0\n"
    "v_mfma_f32_16x16x32_bf16 v[60:63], a[56:59], a[116:119], v[60:63]\n"
    "buffer_load_dwordx4 v226, s[40:43], s67 offen lds\n"
    "v_mfma_f32_16x16x32_bf16 v[60:63], a[60:63], a[132:135], v[60:63]\n"
    "ds_read_b64_tr_b16 a[2:3], v233 offset:256\n"
    "ds_read_b64_tr_b16 a[4:5], v233 offset:8224\n"
    "v_mfma_f32_16x16x32_bf16 v[92:95], a[56:59], a[120:123], v[92:95]\n"
    "buffer_load_dwordx4 v227, s[40:43], s67 offen offset:1024 lds\n"
    "v_mfma_f32_16x16x32_bf16 v[92:95], a[60:63], a[136:139], v[92:95]\n"
    "ds_read_b64_tr_b16 a[6:7], v233 offset:8480\n"
    "v_mfma_f32_16x16x32_bf16 v[124:127], a[56:59], a[124:127], v[124:127]\n"
    "ds_read_b128 a[216:219], v230 offset:2048\n"
    "v_mfma_f32_16x16x32_bf16 v[124:127], a[60:63], a[140:143], v[124:127]\n"
    "s_add_u32 s90, s67, 63480\n"
    "ds_read_b128 a[232:235], v231 offset:2048\n"
    "v_mfma_f32_16x16x32_bf16 v[128:131], a[64:67], a[112:115], v[128:131]\n"
    "v_mfma_f32_16x16x32_bf16 v[128:131], a[68:71], a[128:131], v[128:131]\n"
    "ds_read_b128 a[220:223], v230 offset:4096\n"
    "v_mfma_f32_16x16x32_bf16 v[152:155], a[64:67], a[116:119], v[152:155]\n"
    "buffer_load_dwordx4 v226, s[40:43], s90 offen offset:2056 lds\n"
    "v_mfma_f32_16x16x32_bf16 v[152:155], a[68:71], a[132:135], v[152:155]\n"
    "ds_read_b128 a[236:239], v231 offset:4096\n"
    "ds_read_b128 a[224:227], v230 offset:6144\n"
    "v_mfma_f32_16x16x32_bf16 v[176:179], a[64:67], a[120:123], v[176:179]\n"
    "buffer_load_dwordx4 v227, s[40:43], s90 offen offset:3080 lds\n"
    "v_mfma_f32_16x16x32_bf16 v[176:179], a[68:71], a[136:139], v[176:179]\n"
    "ds_read_b128 a[240:243], v231 offset:6144\n"
    "v_mfma_f32_16x16x32_bf16 v[200:203], a[64:67], a[124:127], v[200:203]\n"
    "ds_read_b64_tr_b16 a[8:9], v233 offset:8\n"
    "v_mfma_f32_16x16x32_bf16 v[200:203], a[68:71], a[140:143], v[200:203]\n"
    "v_mfma_f32_16x16x32_bf16 v[132:135], a[72:75], a[112:115], v[132:135]\n"
    "ds_read_b64_tr_b16 a[10:11], v233 offset:264\n"
    "s_add_u32 m0, s71, 40448\n"
    "v_mfma_f32_16x16x32_bf16 v[132:135], a[76:79], a[128:131], v[132:135]\n"
    "ds_read_b64_tr_b16 a[12:13], v233 offset:8232\n"
    "v_mfma_f32_16x16x32_bf16 v[156:159], a[72:75], a[116:119], v[156:159]\n"
    "buffer_load_dwordx4 v224, s[36:39], s66 offen lds\n"
    "v_mfma_f32_16x16x32_bf16 v[156:159], a[76:79], a[132:135], v[156:159]\n"
    "ds_read_b64_tr_b16 a[14:15], v233 offset:8488\n"
    "v_mfma_f32_16x16x32_bf16 v[180:183], a[72:75], a[120:123], v[180:183]\n"
    "ds_read_b64_tr_b16 a[16:17], v233 offset:512\n"
    "v_mfma_f32_16x16x32_bf16 v[180:183], a[76:79], a[136:139], v[180:183]\n"
    "ds_read_b64_tr_b16 a[18:19], v233 offset:768\n"
    "v_mfma_f32_16x16x32_bf16 v[204:207], a[72:75], a[124:127], v[204:207]\n"
    "s_add_u32 s91, s66, 7168\n"
    "ds_read_b64_tr_b16 a[20:21], v233 offset:8736\n"
    "v_mfma_f32_16x16x32_bf16 v[204:207], a[76:79], a[140:143], v[204:207]\n"
    "v_mfma_f32_16x16x32_bf16 v[136:139], a[80:83], a[112:115], v[136:139]\n"
    "buffer_load_dwordx4 v225, s[36:39], s91 offen offset:1024 lds\n"
    "v_mfma_f32_16x16x32_bf16 v[136:139], a[84:87], a[128:131], v[136:139]\n"
    "ds_read_b64_tr_b16 a[22:23], v233 offset:8992\n"
    "ds_read_b64_tr_b16 a[24:25], v233 offset:520\n"
    "v_mfma_f32_16x16x32_bf16 v[160:163], a[80:83], a[116:119], v[160:163]\n"
    "v_mfma_f32_16x16x32_bf16 v[160:163], a[84:87], a[132:135], v[160:163]\n"
    "ds_read_b64_tr_b16 a[26:27], v233 offset:776\n"
    "s_add_u32 s90, s66, 14336\n"
    "v_mfma_f32_16x16x32_bf16 v[184:187], a[80:83], a[120:123], v[184:187]\n"
    "ds_read_b64_tr_b16 a[28:29], v233 offset:8744\n"
    "v_mfma_f32_16x16x32_bf16 v[184:187], a[84:87], a[136:139], v[184:187]\n"
    "buffer_load_dwordx4 v224, s[36:39], s90 offen offset:2048 lds\n"
    "v_mfma_f32_16x16x32_bf16 v[208:211], a[80:83], a[124:127], v[208:211]\n"
    "ds_read_b64_tr_b16 a[30:31], v233 offset:9000\n"
    "v_mfma_f32_16x16x32_bf16 v[208:211], a[84:87], a[140:143], v[208:211]\n"
    "ds_read_b64_tr_b16 a[32:33], v233 offset:1024\n"
    "v_mfma_f32_16x16x32_bf16 v[140:143], a[88:91], a[112:115], v[140:143]\n"
    "ds_read_b64_tr_b16 a[34:35], v233 offset:1280\n"
    "v_mfma_f32_16x16x32_bf16 v[140:143], a[92:95], a[128:131], v[140:143]\n"
    "s_add_u32 s91, s66, 21504\n"
    "v_mfma_f32_16x16x32_bf16 v[164:167], a[88:91], a[116:119], v[164:167]\n"
    "ds_read_b64_tr_b16 a[36:37], v233 offset:9248\n"
    "v_mfma_f32_16x16x32_bf16 v[164:167], a[92:95], a[132:135], v[164:167]\n"
    "buffer_load_dwordx4 v225, s[36:39], s91 offen offset:3072 lds\n"
    "v_mfma_f32_16x16x32_bf16 v[188:191], a[88:91], a[120:123], v[188:191]\n"
    "ds_read_b64_tr_b16 a[38:39], v233 offset:9504\n"
    "ds_read_b64_tr_b16 a[40:41], v233 offset:1032\n"
    "v_mfma_f32_16x16x32_bf16 v[188:191], a[92:95], a[136:139], v[188:191]\n"
    "v_mfma_f32_16x16x32_bf16 v[212:215], a[88:91], a[124:127], v[212:215]\n"
    "ds_read_b64_tr_b16 a[42:43], v233 offset:1288\n"
    "v_mfma_f32_16x16x32_bf16 v[212:215], a[92:95], a[140:143], v[212:215]\n"
    "s_add_u32 m0, s71, 44544\n"
    "ds_read_b64_tr_b16 a[44:45], v233 offset:9256\n"
    "v_mfma_f32_16x16x32_bf16 v[144:147], a[96:99], a[112:115], v[144:147]\n"
    "s_add_u32 s90, s66, 32768\n"
    "v_mfma_f32_16x16x32_bf16 v[144:147], a[100:103], a[128:131], v[144:147]\n"
    "ds_read_b64_tr_b16 a[46:47], v233 offset:9512\n"
    "v_mfma_f32_16x16x32_bf16 v[168:171], a[96:99], a[116:119], v[168:171]\n"
    "ds_read_b64_tr_b16 a[48:49], v233 offset:1536\n"
    "v_mfma_f32_16x16x32_bf16 v[168:171], a[100:103], a[132:135], v[168:171]\n"
    "buffer_load_dwordx4 v224, s[36:39], s90 offen lds\n"
    "v_mfma_f32_16x16x32_bf16 v[192:195], a[96:99], a[120:123], v[192:195]\n"
    "ds_read_b64_tr_b16 a[50:51], v233 offset:1792\n"
    "v_mfma_f32_16x16x32_bf16 v[192:195], a[100:103], a[136:139], v[192:195]\n"
    "ds_read_b64_tr_b16 a[52:53], v233 offset:9760\n"
    "v_mfma_f32_16x16x32_bf16 v[216:219], a[96:99], a[124:127], v[216:219]\n"
    "s_add_u32 s91, s66, 39936\n"
    "ds_read_b64_tr_b16 a[54:55], v233 offset:10016\n"
    "v_mfma_f32_16x16x32_bf16 v[216:219], a[100:103], a[140:143], v[216:219]\n"
    "v_mfma_f32_16x16x32_bf16 v[148:151], a[104:107], a[112:115], v[148:151]\n"
    "buffer_load_dwordx4 v225, s[36:39], s91 offen offset:1024 lds\n"
    "v_mfma_f32_16x16x32_bf16 v[148:151], a[108:111], a[128:131], v[148:151]\n"
    "v_mfma_f32_16x16x32_bf16 v[172:175], a[104:107], a[116:119], v[172:175]\n"
    "v_mfma_f32_16x16x32_bf16 v[172:175], a[108:111], a[132:135], v[172:175]\n"
    "v_mfma_f32_16x16x32_bf16 v[196:199], a[104:107], a[120:123], v[196:199]\n"
    "s_add_u32 s90, s66, 47104\n"
    "v_mfma_f32_16x16x32_bf16 v[196:199], a[108:111], a[136:139], v[196:199]\n"
    "v_mfma_f32_16x16x32_bf16 v[220:223], a[104:107], a[124:127], v[220:223]\n"
    "v_mfma_f32_16x16x32_bf16 v[220:223], a[108:111], a[140:143], v[220:223]\n"
    "s_waitcnt lgkmcnt(15)\n"
    "v_mfma_f32_16x16x32_bf16 v[0:3], a[0:3], a[212:215], v[0:3]\n"
    "ds_read_b64_tr_b16 a[56:57], v233 offset:1544\n"
    "ds_read_b64_tr_b16 a[58:59], v233 offset:1800\n"
    "v_mfma_f32_16x16x32_bf16 v[0:3], a[4:7], a[228:231], v[0:3]\n"
    "buffer_load_dwordx4 v224, s[36:39], s90 offen offset:2048 lds\n"
    "v_mfma_f32_16x16x32_bf16 v[32:35], a[0:3], a[216:219], v[32:35]\n"
    "s_add_u32 s91, s66, 54272\n"
    "ds_read_b64_tr_b16 a[60:61], v233 offset:9768\n"
    "v_mfma_f32_16x16x32_bf16 v[32:35], a[4:7], a[232:235], v[32:35]\n"
    "buffer_load_dwordx4 v225, s[36:39], s91 offen offset:3072 lds\n"
    "v_mfma_f32_16x16x32_bf16 v[64:67], a[0:3], a[220:223], v[64:67]\n"
    "ds_read_b64_tr_b16 a[62:63], v233 offset:10024\n"
    "v_mfma_f32_16x16x32_bf16 v[64:67], a[4:7], a[236:239], v[64:67]\n"
    "ds_read_b64_tr_b16 a[64:65], v236 offset:0\n"
    "s_add_u32 m0, s77, 89856\n"
    "v_mfma_f32_16x16x32_bf16 v[96:99], a[0:3], a[224:227], v[96:99]\n"
    "ds_read_b64_tr_b16 a[66:67], v237 offset:0\n"
    "v_mfma_f32_16x16x32_bf16 v[96:99], a[4:7], a[240:243], v[96:99]\n"
    "buffer_load_dwordx4 v251, s[52:55], s76 offen lds\n"
    "v_mfma_f32_16x16x32_bf16 v[4:7], a[8:11], a[212:215], v[4:7]\n"
    "ds_read_b64_tr_b16 a[68:69], v236 offset:3072\n"
    "v_mfma_f32_16x16x32_bf16 v[4:7], a[12:15], a[228:231], v[4:7]\n"
    "v_mfma_f32_16x16x32_bf16 v[36:39], a[8:11], a[216:219], v[36:39]\n"
    "ds_read_b64_tr_b16 a[70:71], v237 offset:3072\n"
    "v_mfma_f32_16x16x32_bf16 v[36:39], a[12:15], a[232:235], v[36:39]\n"
    "buffer_load_dwordx4 v252, s[52:55], s76 offen offset:1024 lds\n"
    "v_mfma_f32_16x16x32_bf16 v[68:71], a[8:11], a[220:223], v[68:71]\n"
    "ds_read_b64_tr_b16 a[72:73], v236 offset:6144\n"
    "v_mfma_f32_16x16x32_bf16 v[68:71], a[12:15], a[236:239], v[68:71]\n"
    "buffer_load_dwordx4 v253, s[52:55], s76 offen offset:2048 lds\n"
    "v_mfma_f32_16x16x32_bf16 v[100:103], a[8:11], a[224:227], v[100:103]\n"
    "ds_read_b64_tr_b16 a[74:75], v237 offset:6144\n"
    "v_mfma_f32_16x16x32_bf16 v[100:103], a[12:15], a[240:243], v[100:103]\n"
    "ds_read_b64_tr_b16 a[76:77], v236 offset:9216\n"
    "v_mfma_f32_16x16x32_bf16 v[8:11], a[16:19], a[212:215], v[8:11]\n"
    "s_add_u32 s97, s97, 1\n"
    "v_mfma_f32_16x16x32_bf16 v[8:11], a[20:23], a[228:231], v[8:11]\n"
    "ds_read_b64_tr_b16 a[78:79], v237 offset:9216\n"
    "v_mfma_f32_16x16x32_bf16 v[40:43], a[16:19], a[216:219], v[40:43]\n"
    "s_and_b32 s97, s97, 7\n"
    "v_mfma_f32_16x16x32_bf16 v[40:43], a[20:23], a[232:235], v[40:43]\n"
    "ds_read_b64_tr_b16 a[80:81], v236 offset:256\n"
    "v_mfma_f32_16x16x32_bf16 v[72:75], a[16:19], a[220:223], v[72:75]\n"
    "s_lshl_b32 s66, s97, 7\n"
    "v_mfma_f32_16x16x32_bf16 v[72:75], a[20:23], a[236:239], v[72:75]\n"
    "ds_read_b64_tr_b16 a[82:83], v237 offset:256\n"
    "s_lshl_b32 s67, s97, 19\n"
    "v_mfma_f32_16x16x32_bf16 v[104:107], a[16:19], a[224:227], v[104:107]\n"
    "ds_read_b64_tr_b16 a[84:85], v236 offset:3328\n"
    "v_mfma_f32_16x16x32_bf16 v[104:107], a[20:23], a[240:243], v[104:107]\n"
    "s_mul_i32 s76, s97, 176128\n"
    "s_waitcnt lgkmcnt(15)\n"
    "v_mfma_f32_16x16x32_bf16 v[12:15], a[24:27], a[212:215], v[12:15]\n"
    "ds_read_b64_tr_b16 a[86:87], v237 offset:3328\n"
    "v_min_u32 v254, 7, v238\n"
    "v_mfma_f32_16x16x32_bf16 v[12:15], a[28:31], a[228:231], v[12:15]\n"
    "v_mfma_f32_16x16x32_bf16 v[44:47], a[24:27], a[216:219], v[44:47]\n"
    "s_and_b32 s65, s61, 7\n"
    "ds_read_b64_tr_b16 a[88:89], v236 offset:6400\n"
    "v_mfma_f32_16x16x32_bf16 v[44:47], a[28:31], a[232:235], v[44:47]\n"
    "v_mfma_f32_16x16x32_bf16 v[76:79], a[24:27], a[220:223], v[76:79]\n"
    "s_lshl_b32 s65, s65, 13\n"
    "ds_read_b64_tr_b16 a[90:91], v237 offset:6400\n"
    "v_mfma_f32_16x16x32_bf16 v[76:79], a[28:31], a[236:239], v[76:79]\n"
    "v_lshlrev_b32 v254, 10, v254\n"
    "v_mfma_f32_16x16x32_bf16 v[108:111], a[24:27], a[224:227], v[108:111]\n"
    "ds_read_b64_tr_b16 a[92:93], v236 offset:9472\n"
    "v_mfma_f32_16x16x32_bf16 v[108:111], a[28:31], a[240:243], v[108:111]\n"
    "s_add_u32 s65, s65, s66\n"
    "ds_read_b64_tr_b16 a[94:95], v237 offset:9472\n"
    "v_mfma_f32_16x16x32_bf16 v[16:19], a[32:35], a[212:215], v[16:19]\n"
    "s_waitcnt vmcnt(19)\n"
    "buffer_load_dword a207, v254, s[36:39], s65 offen\n"
    "v_mfma_f32_16x16x32_bf16 v[16:19], a[36:39], a[228:231], v[16:19]\n"
    "ds_read_b64_tr_b16 a[96:97], v236 offset:512\n"
    "v_mfma_f32_16x16x32_bf16 v[48:51], a[32:35], a[216:219], v[48:51]\n"
    "v_mfma_f32_16x16x32_bf16 v[48:51], a[36:39], a[232:235], v[48:51]\n"
    "ds_read_b64_tr_b16 a[98:99], v237 offset:512\n"
    "v_mfma_f32_16x16x32_bf16 v[80:83], a[32:35], a[220:223], v[80:83]\n"
    "v_mfma_f32_16x16x32_bf16 v[80:83], a[36:39], a[236:239], v[80:83]\n"
    "ds_read_b64_tr_b16 a[100:101], v236 offset:3584\n"
    "v_mfma_f32_16x16x32_bf16 v[112:115], a[32:35], a[224:227], v[112:115]\n"
    "v_mfma_f32_16x16x32_bf16 v[112:115], a[36:39], a[240:243], v[112:115]\n"
    "ds_read_b64_tr_b16 a[102:103], v237 offset:3584\n"
    "v_mfma_f32_16x16x32_bf16 v[20:23], a[40:43], a[212:215], v[20:23]\n"
    "ds_read_b64_tr_b16 a[104:105], v236 offset:6656\n"
    "v_mfma_f32_16x16x32_bf16 v[20:23], a[44:47], a[228:231], v[20:23]\n"
    "v_mfma_f32_16x16x32_bf16 v[52:55], a[40:43], a[216:219], v[52:55]\n"
    "ds_read_b64_tr_b16 a[106:107], v237 offset:6656\n"
    "v_mfma_f32_16x16x32_bf16 v[52:55], a[44:47], a[232:235], v[52:55]\n"
    "v_mfma_f32_16x16x32_bf16 v[84:87], a[40:43], a[220:223], v[84:87]\n"
    "ds_read_b64_tr_b16 a[108:109], v236 offset:9728\n"
    "v_mfma_f32_16x16x32_bf16 v[84:87], a[44:47], a[236:239], v[84:87]\n"
    "v_mfma_f32_16x16x32_bf16 v[116:119], a[40:43], a[224:227], v[116:119]\n"
    "ds_read_b64_tr_b16 a[110:111], v237 offset:9728\n"
    "v_mfma_f32_16x16x32_bf16 v[116:119], a[44:47], a[240:243], v[116:119]\n"
    "v_mfma_f32_16x16x32_bf16 v[24:27], a[48:51], a[212:215], v[24:27]\n"
    "v_mfma_f32_16x16x32_bf16 v[24:27], a[52:55], a[228:231], v[24:27]\n"
    "v_mfma_f32_16x16x32_bf16 v[56:59], a[48:51], a[216:219], v[56:59]\n"
    "v_mfma_f32_16x16x32_bf16 v[56:59], a[52:55], a[232:235], v[56:59]\n"
    "v_mfma_f32_16x16x32_bf16 v[88:91], a[48:51], a[220:223], v[88:91]\n"
    "v_mfma_f32_16x16x32_bf16 v[88:91], a[52:55], a[236:239], v[88:91]\n"
    "v_mfma_f32_16x16x32_bf16 v[120:123], a[48:51], a[224:227], v[120:123]\n"
    "v_mfma_f32_16x16x32_bf16 v[120:123], a[52:55], a[240:243], v[120:123]\n"
    "s_waitcnt vmcnt(1)\n"
    "s_waitcnt lgkmcnt(0)\n"
    "s_barrier\n"
    "v_mfma_f32_16x16x32_bf16 v[28:31], a[56:59], a[212:215], v[28:31]\n"
    "ds_read_b128 a[112:115], v228 offset:0\n"
    "s_add_u32 m0, s72, 134912\n"
    "v_mfma_f32_16x16x32_bf16 v[28:31], a[60:63], a[228:231], v[28:31]\n"
    "ds_read_b128 a[128:131], v229 offset:0\n"
    "v_and_b32 v254, 7, v238\n"
    "v_mfma_f32_16x16x32_bf16 v[60:63], a[56:59], a[216:219], v[60:63]\n"
    "buffer_load_dwordx4 v226, s[40:43], s67 offen lds\n"
    "v_mfma_f32_16x16x32_bf16 v[60:63], a[60:63], a[232:235], v[60:63]\n"
    "ds_read_b64_tr_b16 a[0:1], v232 offset:0\n"
    "v_lshlrev_b32 v254, 5, v254\n"
    "v_mfma_f32_16x16x32_bf16 v[92:95], a[56:59], a[220:223], v[92:95]\n"
    "ds_read_b64_tr_b16 a[2:3], v232 offset:256\n"
    "ds_read_b64_tr_b16 a[4:5], v232 offset:8224\n"
    "v_mfma_f32_16x16x32_bf16 v[92:95], a[60:63], a[236:239], v[92:95]\n"
    "buffer_load_dwordx4 v227, s[40:43], s67 offen offset:1024 lds\n"
    "v_mfma_f32_16x16x32_bf16 v[124:127], a[56:59], a[224:227], v[124:127]\n"
    "ds_read_b64_tr_b16 a[6:7], v232 offset:8480\n"
    "v_add_u32 v254, s61, v254\n"
    "v_mfma_f32_16x16x32_bf16 v[124:127], a[60:63], a[240:243], v[124:127]\n"
    "ds_read_b128 a[116:119], v228 offset:2048\n"
    "s_add_u32 s90, s67, 63480\n"
    "v_mfma_f32_16x16x32_bf16 v[128:131], a[64:67], a[212:215], v[128:131]\n"
    "ds_read_b128 a[132:135], v229 offset:2048\n"
    "s_lshl_b32 s64, s62, 5\n"
    "v_mfma_f32_16x16x32_bf16 v[128:131], a[68:71], a[228:231], v[128:131]\n"
    "ds_read_b128 a[120:123], v228 offset:4096\n"
    "v_mfma_f32_16x16x32_bf16 v[152:155], a[64:67], a[216:219], v[152:155]\n"
    "buffer_load_dwordx4 v226, s[40:43], s90 offen offset:2056 lds\n"
    "v_mfma_f32_16x16x32_bf16 v[152:155], a[68:71], a[232:235], v[152:155]\n"
    "ds_read_b128 a[136:139], v229 offset:4096\n"
    "v_add_u32 v239, s64, v238\n"
    "v_mfma_f32_16x16x32_bf16 v[176:179], a[64:67], a[220:223], v[176:179]\n"
    "ds_read_b128 a[124:127], v228 offset:6144\n"
    "v_mfma_f32_16x16x32_bf16 v[176:179], a[68:71], a[236:239], v[176:179]\n"
    "buffer_load_dwordx4 v227, s[40:43], s90 offen offset:3080 lds\n"
    "v_mfma_f32_16x16x32_bf16 v[200:203], a[64:67], a[224:227], v[200:203]\n"
    "ds_read_b128 a[140:143], v229 offset:6144\n"
    "v_cmp_gt_u32 vcc, 32, v238\n"
    "v_mfma_f32_16x16x32_bf16 v[200:203], a[68:71], a[240:243], v[200:203]\n"
    "ds_read_b64_tr_b16 a[8:9], v232 offset:8\n"
    "v_mfma_f32_16x16x32_bf16 v[132:135], a[72:75], a[212:215], v[132:135]\n"
    "ds_read_b64_tr_b16 a[10:11], v232 offset:264\n"
    "s_add_u32 m0, s71, 102144\n"
    "v_mfma_f32_16x16x32_bf16 v[132:135], a[76:79], a[228:231], v[132:135]\n"
    "ds_read_b64_tr_b16 a[12:13], v232 offset:8232\n"
    "v_mfma_f32_16x16x32_bf16 v[156:159], a[72:75], a[216:219], v[156:159]\n"
    "s_nop 1\n"
    "v_mfma_f32_16x16x32_bf16 v[156:159], a[76:79], a[232:235], v[156:159]\n"
    "buffer_load_dwordx4 v224, s[36:39], s66 offen lds\n"
    "v_mfma_f32_16x16x32_bf16 v[180:183], a[72:75], a[220:223], v[180:183]\n"
    "ds_read_b64_tr_b16 a[14:15], v232 offset:8488\n"
    "ds_read_b64_tr_b16 a[16:17], v232 offset:512\n"
    "v_mfma_f32_16x16x32_bf16 v[180:183], a[76:79], a[236:239], v[180:183]\n"
    "ds_read_b64_tr_b16 a[18:19], v232 offset:768\n"
    "v_mfma_f32_16x16x32_bf16 v[204:207], a[72:75], a[224:227], v[204:207]\n"
    "s_add_u32 s91, s66, 7168\n"
    "ds_read_b64_tr_b16 a[20:21], v232 offset:8736\n"
    "v_mfma_f32_16x16x32_bf16 v[204:207], a[76:79], a[240:243], v[204:207]\n"
    "v_mfma_f32_16x16x32_bf16 v[136:139], a[80:83], a[212:215], v[136:139]\n"
    "buffer_load_dwordx4 v225, s[36:39], s91 offen offset:1024 lds\n"
    "v_mfma_f32_16x16x32_bf16 v[136:139], a[84:87], a[228:231], v[136:139]\n"
    "ds_read_b64_tr_b16 a[22:23], v232 offset:8992\n"
    "v_cndmask_b32 v254, v254, v239, vcc\n"
    "v_mfma_f32_16x16x32_bf16 v[160:163], a[80:83], a[216:219], v[160:163]\n"
    "ds_read_b64_tr_b16 a[24:25], v232 offset:520\n"
    "v_mfma_f32_16x16x32_bf16 v[160:163], a[84:87], a[232:235], v[160:163]\n"
    "ds_read_b64_tr_b16 a[26:27], v232 offset:776\n"
    "v_lshlrev_b32 v254, 7, v254\n"
    "v_mfma_f32_16x16x32_bf16 v[184:187], a[80:83], a[220:223], v[184:187]\n"
    "s_add_u32 s90, s66, 14336\n"
    "ds_read_b64_tr_b16 a[28:29], v232 offset:8744\n"
    "v_mfma_f32_16x16x32_bf16 v[184:187], a[84:87], a[236:239], v[184:187]\n"
    "buffer_load_dwordx4 v224, s[36:39], s90 offen offset:2048 lds\n"
    "v_mfma_f32_16x16x32_bf16 v[208:211], a[80:83], a[224:227], v[208:211]\n"
    "ds_read_b64_tr_b16 a[30:31], v232 offset:9000\n"
    "v_add_u32 v254, 4096, v254\n"
    "v_mfma_f32_16x16x32_bf16 v[208:211], a[84:87], a[240:243], v[208:211]\n"
    "ds_read_b64_tr_b16 a[32:33], v232 offset:1024\n"
    "v_mfma_f32_16x16x32_bf16 v[140:143], a[88:91], a[212:215], v[140:143]\n"
    "ds_read_b64_tr_b16 a[34:35], v232 offset:1280\n"
    "v_mfma_f32_16x16x32_bf16 v[140:143], a[92:95], a[228:231], v[140:143]\n"
    "s_add_u32 s91, s66, 21504\n"
    "v_mfma_f32_16x16x32_bf16 v[164:167], a[88:91], a[216:219], v[164:167]\n"
    "ds_read_b64_tr_b16 a[36:37], v232 offset:9248\n"
    "s_lshl_b32 s63, s62, 11\n"
    "v_mfma_f32_16x16x32_bf16 v[164:167], a[92:95], a[232:235], v[164:167]\n"
    "buffer_load_dwordx4 v225, s[36:39], s91 offen offset:3072 lds\n"
    "v_mfma_f32_16x16x32_bf16 v[188:191], a[88:91], a[220:223], v[188:191]\n"
    "ds_read_b64_tr_b16 a[38:39], v232 offset:9504\n"
    "ds_read_b64_tr_b16 a[40:41], v232 offset:1032\n"
    "v_mfma_f32_16x16x32_bf16 v[188:191], a[92:95], a[236:239], v[188:191]\n"
    "s_lshl_b32 s64, s60, 9\n"
    "v_mfma_f32_16x16x32_bf16 v[212:215], a[88:91], a[224:227], v[212:215]\n"
    "ds_read_b64_tr_b16 a[42:43], v232 offset:1288\n"
    "v_mfma_f32_16x16x32_bf16 v[212:215], a[92:95], a[240:243], v[212:215]\n"
    "s_add_u32 m0, s71, 106240\n"
    "ds_read_b64_tr_b16 a[44:45], v232 offset:9256\n"
    "v_mfma_f32_16x16x32_bf16 v[144:147], a[96:99], a[212:215], v[144:147]\n"
    "s_add_u32 s63, s63, s64\n"
    "s_add_u32 s90, s66, 32768\n"
    "v_mfma_f32_16x16x32_bf16 v[144:147], a[100:103], a[228:231], v[144:147]\n"
    "ds_read_b64_tr_b16 a[46:47], v232 offset:9512\n"
    "v_mfma_f32_16x16x32_bf16 v[168:171], a[96:99], a[216:219], v[168:171]\n"
    "ds_read_b64_tr_b16 a[48:49], v232 offset:1536\n"
    "v_mfma_f32_16x16x32_bf16 v[168:171], a[100:103], a[232:235], v[168:171]\n"
    "buffer_load_dwordx4 v224, s[36:39], s90 offen lds\n"
    "v_mfma_f32_16x16x32_bf16 v[192:195], a[96:99], a[220:223], v[192:195]\n"
    "ds_read_b64_tr_b16 a[50:51], v232 offset:1792\n"
    "s_add_u32 s63, s63, 135168\n"
    "v_mfma_f32_16x16x32_bf16 v[192:195], a[100:103], a[236:239], v[192:195]\n"
    "ds_read_b64_tr_b16 a[52:53], v232 offset:9760\n"
    "v_mfma_f32_16x16x32_bf16 v[216:219], a[96:99], a[224:227], v[216:219]\n"
    "s_add_u32 s91, s66, 39936\n"
    "ds_read_b64_tr_b16 a[54:55], v232 offset:10016\n"
    "v_mfma_f32_16x16x32_bf16 v[216:219], a[100:103], a[240:243], v[216:219]\n"
    "s_lshl_b32 s64, s61, 13\n"
    "v_mfma_f32_16x16x32_bf16 v[148:151], a[104:107], a[212:215], v[148:151]\n"
    "buffer_load_dwordx4 v225, s[36:39], s91 offen offset:1024 lds\n"
    "v_mfma_f32_16x16x32_bf16 v[148:151], a[108:111], a[228:231], v[148:151]\n"
    "v_mfma_f32_16x16x32_bf16 v[172:175], a[104:107], a[216:219], v[172:175]\n"
    "s_add_u32 s64, s64, 151552\n"
    "v_mfma_f32_16x16x32_bf16 v[172:175], a[108:111], a[232:235], v[172:175]\n"
    "v_mfma_f32_16x16x32_bf16 v[196:199], a[104:107], a[220:223], v[196:199]\n"
    "s_add_u32 s90, s66, 47104\n"
    "v_mfma_f32_16x16x32_bf16 v[196:199], a[108:111], a[236:239], v[196:199]\n"
    "v_mfma_f32_16x16x32_bf16 v[220:223], a[104:107], a[224:227], v[220:223]\n"
    "v_mfma_f32_16x16x32_bf16 v[220:223], a[108:111], a[240:243], v[220:223]\n"
    "s_waitcnt lgkmcnt(15)\n"
    "v_mfma_f32_16x16x32_bf16 v[0:3], a[0:3], a[112:115], v[0:3]\n"
    "ds_read_b64_tr_b16 a[56:57], v232 offset:1544\n"
    "ds_read_b64_tr_b16 a[58:59], v232 offset:1800\n"
    "v_mfma_f32_16x16x32_bf16 v[0:3], a[4:7], a[128:131], v[0:3]\n"
    "buffer_load_dwordx4 v224, s[36:39], s90 offen offset:2048 lds\n"
    "v_mfma_f32_16x16x32_bf16 v[32:35], a[0:3], a[116:119], v[32:35]\n"
    "s_add_u32 s91, s66, 54272\n"
    "ds_read_b64_tr_b16 a[60:61], v232 offset:9768\n"
    "v_mfma_f32_16x16x32_bf16 v[32:35], a[4:7], a[132:135], v[32:35]\n"
    "buffer_load_dwordx4 v225, s[36:39], s91 offen offset:3072 lds\n"
    "v_mfma_f32_16x16x32_bf16 v[64:67], a[0:3], a[120:123], v[64:67]\n"
    "ds_read_b64_tr_b16 a[62:63], v232 offset:10024\n"
    "v_mfma_f32_16x16x32_bf16 v[64:67], a[4:7], a[136:139], v[64:67]\n"
    "ds_read_b64_tr_b16 a[64:65], v234 offset:0\n"
    "v_mfma_f32_16x16x32_bf16 v[96:99], a[0:3], a[124:127], v[96:99]\n"
    "s_add_u32 m0, s77, 151552\n"
    "ds_read_b64_tr_b16 a[66:67], v235 offset:0\n"
    "v_mfma_f32_16x16x32_bf16 v[96:99], a[4:7], a[140:143], v[96:99]\n"
    "v_mfma_f32_16x16x32_bf16 v[4:7], a[8:11], a[112:115], v[4:7]\n"
    "buffer_load_dwordx4 v251, s[52:55], s76 offen lds\n"
    "v_mfma_f32_16x16x32_bf16 v[4:7], a[12:15], a[128:131], v[4:7]\n"
    "ds_read_b64_tr_b16 a[68:69], v234 offset:3072\n"
    "v_mfma_f32_16x16x32_bf16 v[36:39], a[8:11], a[116:119], v[36:39]\n"
    "ds_read_b64_tr_b16 a[70:71], v235 offset:3072\n"
    "v_mfma_f32_16x16x32_bf16 v[36:39], a[12:15], a[132:135], v[36:39]\n"
    "v_mfma_f32_16x16x32_bf16 v[68:71], a[8:11], a[120:123], v[68:71]\n"
    "buffer_load_dwordx4 v252, s[52:55], s76 offen offset:1024 lds\n"
    "v_mfma_f32_16x16x32_bf16 v[68:71], a[12:15], a[136:139], v[68:71]\n"
    "ds_read_b64_tr_b16 a[72:73], v234 offset:6144\n"
    "v_mfma_f32_16x16x32_bf16 v[100:103], a[8:11], a[124:127], v[100:103]\n"
    "ds_read_b64_tr_b16 a[74:75], v235 offset:6144\n"
    "v_mfma_f32_16x16x32_bf16 v[100:103], a[12:15], a[140:143], v[100:103]\n"
    "buffer_load_dwordx4 v253, s[52:55], s76 offen offset:2048 lds\n"
    "v_mfma_f32_16x16x32_bf16 v[8:11], a[16:19], a[112:115], v[8:11]\n"
    "ds_read_b64_tr_b16 a[76:77], v234 offset:9216\n"
    "v_mfma_f32_16x16x32_bf16 v[8:11], a[20:23], a[128:131], v[8:11]\n"
    "ds_read_b64_tr_b16 a[78:79], v235 offset:9216\n"
    "v_mfma_f32_16x16x32_bf16 v[40:43], a[16:19], a[116:119], v[40:43]\n"
    "v_mfma_f32_16x16x32_bf16 v[40:43], a[20:23], a[132:135], v[40:43]\n"
    "s_add_u32 s97, s97, 1\n"
    "ds_read_b64_tr_b16 a[80:81], v234 offset:256\n"
    "v_mfma_f32_16x16x32_bf16 v[72:75], a[16:19], a[120:123], v[72:75]\n"
    "s_and_b32 s97, s97, 7\n"
    "v_mfma_f32_16x16x32_bf16 v[72:75], a[20:23], a[136:139], v[72:75]\n"
    "ds_read_b64_tr_b16 a[82:83], v235 offset:256\n"
    "v_mfma_f32_16x16x32_bf16 v[104:107], a[16:19], a[124:127], v[104:107]\n"
    "s_lshl_b32 s66, s97, 7\n"
    "ds_read_b64_tr_b16 a[84:85], v234 offset:3328\n"
    "v_mfma_f32_16x16x32_bf16 v[104:107], a[20:23], a[140:143], v[104:107]\n"
    "s_waitcnt lgkmcnt(15)\n"
    "v_mfma_f32_16x16x32_bf16 v[12:15], a[24:27], a[112:115], v[12:15]\n"
    "s_lshl_b32 s67, s97, 19\n"
    "ds_read_b64_tr_b16 a[86:87], v235 offset:3328\n"
    "v_mfma_f32_16x16x32_bf16 v[12:15], a[28:31], a[128:131], v[12:15]\n"
    "v_mfma_f32_16x16x32_bf16 v[44:47], a[24:27], a[116:119], v[44:47]\n"
    "s_mul_i32 s76, s97, 176128\n"
    "ds_read_b64_tr_b16 a[88:89], v234 offset:6400\n"
    "v_mfma_f32_16x16x32_bf16 v[44:47], a[28:31], a[132:135], v[44:47]\n"
    "v_mfma_f32_16x16x32_bf16 v[76:79], a[24:27], a[120:123], v[76:79]\n"
    "ds_read_b64_tr_b16 a[90:91], v235 offset:6400\n"
    "v_mfma_f32_16x16x32_bf16 v[76:79], a[28:31], a[136:139], v[76:79]\n"
    "v_mfma_f32_16x16x32_bf16 v[108:111], a[24:27], a[124:127], v[108:111]\n"
    "buffer_load_dwordx2 v[224:225], v254, s[56:59], 0 offen sc1\n"
    "v_mfma_f32_16x16x32_bf16 v[108:111], a[28:31], a[140:143], v[108:111]\n"
    "ds_read_b64_tr_b16 a[92:93], v234 offset:9472\n"
    "ds_read_b64_tr_b16 a[94:95], v235 offset:9472\n"
    "v_mfma_f32_16x16x32_bf16 v[16:19], a[32:35], a[112:115], v[16:19]\n"
    "v_mfma_f32_16x16x32_bf16 v[16:19], a[36:39], a[128:131], v[16:19]\n"
    "ds_read_b64_tr_b16 a[96:97], v234 offset:512\n"
    "v_mfma_f32_16x16x32_bf16 v[48:51], a[32:35], a[116:119], v[48:51]\n"
    "v_mfma_f32_16x16x32_bf16 v[48:51], a[36:39], a[132:135], v[48:51]\n"
    "ds_read_b64_tr_b16 a[98:99], v235 offset:512\n"
    "v_and_b32 v226, 15, v238\n"
    "v_mfma_f32_16x16x32_bf16 v[80:83], a[32:35], a[120:123], v[80:83]\n"
    "v_mfma_f32_16x16x32_bf16 v[80:83], a[36:39], a[136:139], v[80:83]\n"
    "ds_read_b64_tr_b16 a[100:101], v234 offset:3584\n"
    "v_mfma_f32_16x16x32_bf16 v[112:115], a[32:35], a[124:127], v[112:115]\n"
    "v_mfma_f32_16x16x32_bf16 v[112:115], a[36:39], a[140:143], v[112:115]\n"
    "ds_read_b64_tr_b16 a[102:103], v235 offset:3584\n"
    "v_lshrrev_b32 v227, 4, v238\n"
    "v_mfma_f32_16x16x32_bf16 v[20:23], a[40:43], a[112:115], v[20:23]\n"
    "ds_read_b64_tr_b16 a[104:105], v234 offset:6656\n"
    "v_mfma_f32_16x16x32_bf16 v[20:23], a[44:47], a[128:131], v[20:23]\n"
    "v_mfma_f32_16x16x32_bf16 v[52:55], a[40:43], a[116:119], v[52:55]\n"
    "ds_read_b64_tr_b16 a[106:107], v235 offset:6656\n"
    "v_lshlrev_b32 v227, 11, v227\n"
    "v_mfma_f32_16x16x32_bf16 v[52:55], a[44:47], a[132:135], v[52:55]\n"
    "v_mfma_f32_16x16x32_bf16 v[84:87], a[40:43], a[120:123], v[84:87]\n"
    "ds_read_b64_tr_b16 a[108:109], v234 offset:9728\n"
    "v_mfma_f32_16x16x32_bf16 v[84:87], a[44:47], a[136:139], v[84:87]\n"
    "v_mfma_f32_16x16x32_bf16 v[116:119], a[40:43], a[124:127], v[116:119]\n"
    "ds_read_b64_tr_b16 a[110:111], v235 offset:9728\n"
    "v_lshl_add_u32 v227, v226, 4, v227\n"
    "v_mfma_f32_16x16x32_bf16 v[116:119], a[44:47], a[140:143], v[116:119]\n"
    "v_mfma_f32_16x16x32_bf16 v[24:27], a[48:51], a[112:115], v[24:27]\n"
    "v_mfma_f32_16x16x32_bf16 v[24:27], a[52:55], a[128:131], v[24:27]\n"
    "v_mfma_f32_16x16x32_bf16 v[56:59], a[48:51], a[116:119], v[56:59]\n"
    "v_lshlrev_b32 v226, 3, v226\n"
    "v_mfma_f32_16x16x32_bf16 v[56:59], a[52:55], a[132:135], v[56:59]\n"
    "v_mfma_f32_16x16x32_bf16 v[88:91], a[48:51], a[120:123], v[88:91]\n"
    "v_mfma_f32_16x16x32_bf16 v[88:91], a[52:55], a[136:139], v[88:91]\n"
    "v_mfma_f32_16x16x32_bf16 v[120:123], a[48:51], a[124:127], v[120:123]\n"
    "v_mfma_f32_16x16x32_bf16 v[120:123], a[52:55], a[140:143], v[120:123]\n"
    "s_waitcnt vmcnt(1)\n"
    "s_waitcnt lgkmcnt(0)\n"
    "s_barrier\n"
    "v_mfma_f32_16x16x32_bf16 v[28:31], a[56:59], a[112:115], v[28:31]\n"
    "ds_read_b128 a[212:215], v230 offset:0\n"
    "s_lshl_b32 s70, s62, 2\n"
    "v_mfma_f32_16x16x32_bf16 v[28:31], a[60:63], a[128:131], v[28:31]\n"
    "ds_read_b128 a[228:231], v231 offset:0\n"
    "ds_read_b64_tr_b16 a[0:1], v233 offset:0\n"
    "v_mfma_f32_16x16x32_bf16 v[60:63], a[56:59], a[116:119], v[60:63]\n"
    "s_add_u32 s70, s70, s80\n"
    "ds_read_b64_tr_b16 a[2:3], v233 offset:256\n"
    "v_mfma_f32_16x16x32_bf16 v[60:63], a[60:63], a[132:135], v[60:63]\n"
    "ds_read_b64_tr_b16 a[4:5], v233 offset:8224\n"
    "v_mfma_f32_16x16x32_bf16 v[92:95], a[56:59], a[120:123], v[92:95]\n"
    "s_waitcnt vmcnt(0)\n"
    "v_cmp_ne_u64 vcc, s[68:69], v[224:225]\n"
    "s_nop 0\n"
    "s_cbranch_vccnz LSPA\n"
    "LSPA_r:\n"
    "v_mfma_f32_16x16x32_bf16 v[92:95], a[60:63], a[136:139], v[92:95]\n"
    "s_lshl_b32 s96, s70, 3\n"
    "ds_read_b64_tr_b16 a[6:7], v233 offset:8480\n"
    "v_mfma_f32_16x16x32_bf16 v[124:127], a[56:59], a[124:127], v[124:127]\n"
    "buffer_load_dwordx2 a[176:177], v226, s[56:59], s63 offen offset:0 sc1\n"
    "v_mfma_f32_16x16x32_bf16 v[124:127], a[60:63], a[140:143], v[124:127]\n"
    "ds_read_b128 a[216:219], v230 offset:2048\n"
    "s_lshl_b32 s83, s96, 16\n"
    "v_mfma_f32_16x16x32_bf16 v[128:131], a[64:67], a[112:115], v[128:131]\n"
    "ds_read_b128 a[232:235], v231 offset:2048\n"
    "v_mfma_f32_16x16x32_bf16 v[128:131], a[68:71], a[128:131], v[128:131]\n"
    "ds_read_b128 a[220:223], v230 offset:4096\n"
    "v_mfma_f32_16x16x32_bf16 v[152:155], a[64:67], a[116:119], v[152:155]\n"
    "buffer_load_dwordx2 a[178:179], v226, s[56:59], s63 offen offset:128 sc1\n"
    "v_mfma_f32_16x16x32_bf16 v[152:155], a[68:71], a[132:135], v[152:155]\n"
    "s_lshl_b32 s95, s81, 13\n"
    "ds_read_b128 a[236:239], v231 offset:4096\n"
    "v_mfma_f32_16x16x32_bf16 v[176:179], a[64:67], a[120:123], v[176:179]\n"
    "ds_read_b128 a[224:227], v230 offset:6144\n"
    "v_mfma_f32_16x16x32_bf16 v[176:179], a[68:71], a[136:139], v[176:179]\n"
    "ds_read_b128 a[240:243], v231 offset:6144\n"
    "s_add_u32 s83, s83, s95\n"
    "v_mfma_f32_16x16x32_bf16 v[200:203], a[64:67], a[124:127], v[200:203]\n"
    "buffer_load_dwordx2 a[180:181], v226, s[56:59], s63 offen offset:256 sc1\n"
    "v_mfma_f32_16x16x32_bf16 v[200:203], a[68:71], a[140:143], v[200:203]\n"
    "ds_read_b64_tr_b16 a[8:9], v233 offset:8\n"
    "v_mfma_f32_16x16x32_bf16 v[132:135], a[72:75], a[112:115], v[132:135]\n"
    "ds_read_b64_tr_b16 a[10:11], v233 offset:264\n"
    "s_lshl_b32 s95, s60, 11\n"
    "v_mfma_f32_16x16x32_bf16 v[132:135], a[76:79], a[128:131], v[132:135]\n"
    "ds_read_b64_tr_b16 a[12:13], v233 offset:8232\n"
    "v_mfma_f32_16x16x32_bf16 v[156:159], a[72:75], a[116:119], v[156:159]\n"
    "buffer_load_dwordx2 a[182:183], v226, s[56:59], s63 offen offset:384 sc1\n"
    "v_mfma_f32_16x16x32_bf16 v[156:159], a[76:79], a[132:135], v[156:159]\n"
    "ds_read_b64_tr_b16 a[14:15], v233 offset:8488\n"
    "s_add_u32 s83, s83, s95\n"
    "v_mfma_f32_16x16x32_bf16 v[180:183], a[72:75], a[120:123], v[180:183]\n"
    "ds_read_b64_tr_b16 a[16:17], v233 offset:512\n"
    "v_mfma_f32_16x16x32_bf16 v[180:183], a[76:79], a[136:139], v[180:183]\n"
    "ds_read_b64_tr_b16 a[18:19], v233 offset:768\n"
    "v_mfma_f32_16x16x32_bf16 v[204:207], a[72:75], a[124:127], v[204:207]\n"
    "buffer_load_dwordx4 a[144:147], v227, s[56:59], s64 offen offset:0 sc1\n"
    "v_mfma_f32_16x16x32_bf16 v[204:207], a[76:79], a[140:143], v[204:207]\n"
    "s_add_u32 s83, s83, 22827008\n"
    "ds_read_b64_tr_b16 a[20:21], v233 offset:8736\n"
    "v_mfma_f32_16x16x32_bf16 v[136:139], a[80:83], a[112:115], v[136:139]\n"
    "ds_read_b64_tr_b16 a[22:23], v233 offset:8992\n"
    "v_mfma_f32_16x16x32_bf16 v[136:139], a[84:87], a[128:131], v[136:139]\n"
    "ds_read_b64_tr_b16 a[24:25], v233 offset:520\n"
    "s_and_b32 s53, s53, 0xffff\n"
    "v_mfma_f32_16x16x32_bf16 v[160:163], a[80:83], a[116:119], v[160:163]\n"
    "buffer_load_dwordx4 a[148:151], v227, s[56:59], s64 offen offset:256 sc1\n"
    "v_mfma_f32_16x16x32_bf16 v[160:163], a[84:87], a[132:135], v[160:163]\n"
    "ds_read_b64_tr_b16 a[26:27], v233 offset:776\n"
    "v_mfma_f32_16x16x32_bf16 v[184:187], a[80:83], a[120:123], v[184:187]\n"
    "ds_read_b64_tr_b16 a[28:29], v233 offset:8744\n"
    "v_mfma_f32_16x16x32_bf16 v[184:187], a[84:87], a[136:139], v[184:187]\n"
    "s_sub_u32 s54, 0, s83\n"
    "ds_read_b64_tr_b16 a[30:31], v233 offset:9000\n"
    "v_mfma_f32_16x16x32_bf16 v[208:211], a[80:83], a[124:127], v[208:211]\n"
    "buffer_load_dwordx4 a[152:155], v227, s[56:59], s64 offen offset:512 sc1\n"
    "v_mfma_f32_16x16x32_bf16 v[208:211], a[84:87], a[140:143], v[208:211]\n"
    "ds_read_b64_tr_b16 a[32:33], v233 offset:1024\n"
    "v_mfma_f32_16x16x32_bf16 v[140:143], a[88:91], a[112:115], v[140:143]\n"
    "s_mov_b32 s55, 0x20000\n"
    "ds_read_b64_tr_b16 a[34:35], v233 offset:1280\n"
    "v_mfma_f32_16x16x32_bf16 v[140:143], a[92:95], a[128:131], v[140:143]\n"
    "buffer_load_dwordx4 a[156:159], v227, s[56:59], s64 offen offset:768 sc1\n"
    "v_mfma_f32_16x16x32_bf16 v[164:167], a[88:91], a[116:119], v[164:167]\n"
    "ds_read_b64_tr_b16 a[36:37], v233 offset:9248\n"
    "v_mfma_f32_16x16x32_bf16 v[164:167], a[92:95], a[132:135], v[164:167]\n"
    "s_mov_b32 s54, 460800\n"
    "ds_read_b64_tr_b16 a[38:39], v233 offset:9504\n"
    "v_mfma_f32_16x16x32_bf16 v[188:191], a[88:91], a[120:123], v[188:191]\n"
    "ds_read_b64_tr_b16 a[40:41], v233 offset:1032\n"
    "v_mfma_f32_16x16x32_bf16 v[188:191], a[92:95], a[136:139], v[188:191]\n"
    "buffer_load_dwordx4 a[160:163], v227, s[56:59], s64 offen offset:1024 sc1\n"
    "v_mfma_f32_16x16x32_bf16 v[212:215], a[88:91], a[124:127], v[212:215]\n"
    "ds_read_b64_tr_b16 a[42:43], v233 offset:1288\n"
    "s_mov_b32 s84, 0\n"
    "v_mfma_f32_16x16x32_bf16 v[212:215], a[92:95], a[140:143], v[212:215]\n"
    "ds_read_b64_tr_b16 a[44:45], v233 offset:9256\n"
    "v_mfma_f32_16x16x32_bf16 v[144:147], a[96:99], a[112:115], v[144:147]\n"
    "v_mfma_f32_16x16x32_bf16 v[144:147], a[100:103], a[128:131], v[144:147]\n"
    "ds_read_b64_tr_b16 a[46:47], v233 offset:9512\n"
    "s_mov_b32 s85, 65536\n"
    "v_mfma_f32_16x16x32_bf16 v[168:171], a[96:99], a[116:119], v[168:171]\n"
    "buffer_load_dwordx4 a[164:167], v227, s[56:59], s64 offen offset:1280 sc1\n"
    "v_mfma_f32_16x16x32_bf16 v[168:171], a[100:103], a[132:135], v[168:171]\n"
    "ds_read_b64_tr_b16 a[48:49], v233 offset:1536\n"
    "ds_read_b64_tr_b16 a[50:51], v233 offset:1792\n"
    "v_mfma_f32_16x16x32_bf16 v[192:195], a[96:99], a[120:123], v[192:195]\n"
    "s_add_u32 s52, s26, s83\n"
    "s_addc_u32 s53, s27, 0\n"
    "v_mfma_f32_16x16x32_bf16 v[192:195], a[100:103], a[136:139], v[192:195]\n"
    "ds_read_b64_tr_b16 a[52:53], v233 offset:9760\n"
    "v_mfma_f32_16x16x32_bf16 v[216:219], a[96:99], a[124:127], v[216:219]\n"
    "buffer_load_dwordx4 a[168:171], v227, s[56:59], s64 offen offset:1536 sc1\n"
    "v_mfma_f32_16x16x32_bf16 v[216:219], a[100:103], a[140:143], v[216:219]\n"
    "ds_read_b64_tr_b16 a[54:55], v233 offset:10016\n"
    "v_mfma_f32_16x16x32_bf16 v[148:151], a[104:107], a[112:115], v[148:151]\n"
    "v_mfma_f32_16x16x32_bf16 v[148:151], a[108:111], a[128:131], v[148:151]\n"
    "buffer_load_dwordx4 a[172:175], v227, s[56:59], s64 offen offset:1792 sc1\n"
    "v_mfma_f32_16x16x32_bf16 v[172:175], a[104:107], a[116:119], v[172:175]\n"
    "s_and_b32 s53, s53, 0xffff\n"
    "v_mfma_f32_16x16x32_bf16 v[172:175], a[108:111], a[132:135], v[172:175]\n"
    "v_mfma_f32_16x16x32_bf16 v[196:199], a[104:107], a[120:123], v[196:199]\n"
    "v_mfma_f32_16x16x32_bf16 v[196:199], a[108:111], a[136:139], v[196:199]\n"
    "v_mfma_f32_16x16x32_bf16 v[220:223], a[104:107], a[124:127], v[220:223]\n"
    "v_mfma_f32_16x16x32_bf16 v[220:223], a[108:111], a[140:143], v[220:223]\n"
    "s_waitcnt lgkmcnt(15)\n"
    "v_mfma_f32_16x16x32_bf16 v[0:3], a[0:3], a[212:215], v[0:3]\n"
    "ds_read_b64_tr_b16 a[56:57], v233 offset:1544\n"
    "ds_read_b128 a[112:115], v240 offset:24576\n"
    "v_mfma_f32_16x16x32_bf16 v[0:3], a[4:7], a[228:231], v[0:3]\n"
    "s_add_u32 m0, s75, 0\n"
    "ds_read_b64_tr_b16 a[58:59], v233 offset:1800\n"
    "v_mfma_f32_16x16x32_bf16 v[32:35], a[0:3], a[216:219], v[32:35]\n"
    "buffer_load_dwordx4 v249, s[48:51], s73 offen lds\n"
    "v_mfma_f32_16x16x32_bf16 v[32:35], a[4:7], a[232:235], v[32:35]\n"
    "ds_read_b64_tr_b16 a[60:61], v233 offset:9768\n"
    "buffer_load_dwordx4 v250, s[48:51], s73 offen offset:1024 lds\n"
    "v_mfma_f32_16x16x32_bf16 v[64:67], a[0:3], a[220:223], v[64:67]\n"
    "ds_read_b64_tr_b16 a[62:63], v233 offset:10024\n"
    "ds_read_b128 a[116:119], v241 offset:24576\n"
    "v_mfma_f32_16x16x32_bf16 v[64:67], a[4:7], a[236:239], v[64:67]\n"
    "ds_read_b64_tr_b16 a[64:65], v236 offset:0\n"
    "v_mfma_f32_16x16x32_bf16 v[96:99], a[0:3], a[224:227], v[96:99]\n"
    "buffer_load_dwordx4 v249, s[48:51], s73 offen offset:2048 lds\n"
    "v_mfma_f32_16x16x32_bf16 v[96:99], a[4:7], a[240:243], v[96:99]\n"
    "ds_read_b64_tr_b16 a[66:67], v237 offset:0\n"
    "v_mfma_f32_16x16x32_bf16 v[4:7], a[8:11], a[212:215], v[4:7]\n"
    "ds_read_b64_tr_b16 a[68:69], v236 offset:3072\n"
    "v_mfma_f32_16x16x32_bf16 v[4:7], a[12:15], a[228:231], v[4:7]\n"
    "buffer_load_dwordx4 v250, s[48:51], s73 offen offset:3072 lds\n"
    "v_mfma_f32_16x16x32_bf16 v[36:39], a[8:11], a[216:219], v[36:39]\n"
    "ds_read_b64_tr_b16 a[70:71], v237 offset:3072\n"
    "ds_read_b128 a[120:123], v242 offset:24576\n"
    "v_mfma_f32_16x16x32_bf16 v[36:39], a[12:15], a[232:235], v[36:39]\n"
    "v_mfma_f32_16x16x32_bf16 v[68:71], a[8:11], a[220:223], v[68:71]\n"
    "s_waitcnt vmcnt(9)\n"
    "v_accvgpr_read_b32 v254, a152\n"
    "s_waitcnt vmcnt(5)\n"
    "v_accvgpr_read_b32 v239, a168\n"
    "v_mfma_f32_16x16x32_bf16 v[68:71], a[12:15], a[236:239], v[68:71]\n"
    "ds_read_b64_tr_b16 a[72:73], v236 offset:6144\n"
    "v_add_f32 v254, v254, v239\n"
    "v_mfma_f32_16x16x32_bf16 v[100:103], a[8:11], a[224:227], v[100:103]\n"
    "s_add_u32 s73, s73, 16384\n"
    "v_cvt_pk_bf16_f32 v239, v254, v254\n"
    "v_and_b32 v225, 0xffff0000, v239\n"
    "v_mfma_f32_16x16x32_bf16 v[100:103], a[12:15], a[240:243], v[100:103]\n"
    "ds_read_b64_tr_b16 a[74:75], v237 offset:6144\n"
    "v_sub_f32 v254, v254, v225\n"
    "v_mfma_f32_16x16x32_bf16 v[8:11], a[16:19], a[212:215], v[8:11]\n"
    "s_and_b32 s73, s73, 0x1ffff\n"
    "v_cvt_pk_bf16_f32 v224, v254, 0\n"
    "v_mfma_f32_16x16x32_bf16 v[8:11], a[20:23], a[228:231], v[8:11]\n"
    "ds_read_b64_tr_b16 a[76:77], v236 offset:9216\n"
    "v_accvgpr_write_b32 a244, v239\n"
    "v_mfma_f32_16x16x32_bf16 v[40:43], a[16:19], a[216:219], v[40:43]\n"
    "s_lshl_b32 s95, s80, 8\n"
    "ds_read_b128 a[124:127], v243 offset:24576\n"
    "v_accvgpr_write_b32 a245, v224\n"
    "v_mfma_f32_16x16x32_bf16 v[40:43], a[20:23], a[232:235], v[40:43]\n"
    "ds_read_b64_tr_b16 a[78:79], v237 offset:9216\n"
    "v_accvgpr_read_b32 v254, a153\n"
    "v_mfma_f32_16x16x32_bf16 v[72:75], a[16:19], a[220:223], v[72:75]\n"
    "s_and_b32 s70, s81, 1\n"
    "v_accvgpr_read_b32 v239, a169\n"
    "v_mfma_f32_16x16x32_bf16 v[72:75], a[20:23], a[236:239], v[72:75]\n"
    "s_lshl_b32 s70, s70, 7\n"
    "v_add_f32 v254, v254, v239\n"
    "v_mfma_f32_16x16x32_bf16 v[104:107], a[16:19], a[224:227], v[104:107]\n"
    "ds_read_b64_tr_b16 a[80:81], v236 offset:256\n"
    "v_cvt_pk_bf16_f32 v239, v254, v254\n"
    "v_and_b32 v225, 0xffff0000, v239\n"
    "v_mfma_f32_16x16x32_bf16 v[104:107], a[20:23], a[240:243], v[104:107]\n"
    "s_add_u32 s95, s95, s70\n"
    "ds_read_b64_tr_b16 a[82:83], v237 offset:256\n"
    "s_waitcnt lgkmcnt(15)\n"
    "v_mfma_f32_16x16x32_bf16 v[12:15], a[24:27], a[212:215], v[12:15]\n"
    "v_sub_f32 v254, v254, v225\n"
    "v_cvt_pk_bf16_f32 v224, v254, 0\n"
    "v_mfma_f32_16x16x32_bf16 v[12:15], a[28:31], a[228:231], v[12:15]\n"
    "s_lshl_b32 s70, s60, 15\n"
    "ds_read_b64_tr_b16 a[84:85], v236 offset:3328\n"
    "v_mfma_f32_16x16x32_bf16 v[44:47], a[24:27], a[216:219], v[44:47]\n"
    "ds_read_b128 a[128:131], v240 offset:28672\n"
    "v_accvgpr_write_b32 a246, v239\n"
    "v_accvgpr_write_b32 a247, v224\n"
    "v_mfma_f32_16x16x32_bf16 v[44:47], a[28:31], a[232:235], v[44:47]\n"
    "s_add_u32 s95, s95, s70\n"
    "v_accvgpr_read_b32 v254, a154\n"
    "v_mfma_f32_16x16x32_bf16 v[76:79], a[24:27], a[220:223], v[76:79]\n"
    "ds_read_b64_tr_b16 a[86:87], v237 offset:3328\n"
    "v_accvgpr_read_b32 v239, a170\n"
    "v_mfma_f32_16x16x32_bf16 v[76:79], a[28:31], a[236:239], v[76:79]\n"
    "s_add_u32 s36, s20, s95\n"
    "s_addc_u32 s37, s21, 0\n"
    "v_add_f32 v254, v254, v239\n"
    "v_mfma_f32_16x16x32_bf16 v[108:111], a[24:27], a[224:227], v[108:111]\n"
    "ds_read_b64_tr_b16 a[88:89], v236 offset:6400\n"
    "v_cvt_pk_bf16_f32 v239, v254, v254\n"
    "v_mfma_f32_16x16x32_bf16 v[108:111], a[28:31], a[240:243], v[108:111]\n"
    "ds_read_b64_tr_b16 a[90:91], v237 offset:6400\n"
    "v_and_b32 v225, 0xffff0000, v239\n"
    "v_mfma_f32_16x16x32_bf16 v[16:19], a[32:35], a[212:215], v[16:19]\n"
    "ds_read_b128 a[132:135], v241 offset:28672\n"
    "v_sub_f32 v254, v254, v225\n"
    "v_mfma_f32_16x16x32_bf16 v[16:19], a[36:39], a[228:231], v[16:19]\n"
    "s_and_b32 s37, s37, 0xffff\n"
    "ds_read_b64_tr_b16 a[92:93], v236 offset:9472\n"
    "v_cvt_pk_bf16_f32 v224, v254, 0\n"
    "v_mfma_f32_16x16x32_bf16 v[48:51], a[32:35], a[216:219], v[48:51]\n"
    "s_sub_u32 s38, 1409024, s95\n"
    "v_accvgpr_write_b32 a248, v239\n"
    "v_mfma_f32_16x16x32_bf16 v[48:51], a[36:39], a[232:235], v[48:51]\n"
    "ds_read_b64_tr_b16 a[94:95], v237 offset:9472\n"
    "v_accvgpr_write_b32 a249, v224\n"
    "v_mfma_f32_16x16x32_bf16 v[80:83], a[32:35], a[220:223], v[80:83]\n"
    "s_mov_b32 s39, 0x20000\n"
    "v_accvgpr_read_b32 v254, a155\n"
    "v_mfma_f32_16x16x32_bf16 v[80:83], a[36:39], a[236:239], v[80:83]\n"
    "ds_read_b64_tr_b16 a[96:97], v236 offset:512\n"
    "v_accvgpr_read_b32 v239, a171\n"
    "v_add_f32 v254, v254, v239\n"
    "v_mfma_f32_16x16x32_bf16 v[112:115], a[32:35], a[224:227], v[112:115]\n"
    "s_and_b32 s99, s62, 3\n"
    "v_cvt_pk_bf16_f32 v239, v254, v254\n"
    "v_mfma_f32_16x16x32_bf16 v[112:115], a[36:39], a[240:243], v[112:115]\n"
    "ds_read_b64_tr_b16 a[98:99], v237 offset:512\n"
    "v_and_b32 v225, 0xffff0000, v239\n"
    "v_mfma_f32_16x16x32_bf16 v[20:23], a[40:43], a[212:215], v[20:23]\n"
    "ds_read_b128 a[136:139], v242 offset:28672\n"
    "s_lshl_b32 s67, s60, 12\n"
    "v_mfma_f32_16x16x32_bf16 v[20:23], a[44:47], a[228:231], v[20:23]\n"
    "ds_read_b64_tr_b16 a[100:101], v236 offset:3584\n"
    "v_sub_f32 v254, v254, v225\n"
    "v_cvt_pk_bf16_f32 v224, v254, 0\n"
    "v_mfma_f32_16x16x32_bf16 v[52:55], a[40:43], a[216:219], v[52:55]\n"
    "s_add_u32 s67, s67, 65536\n"
    "v_accvgpr_write_b32 a250, v239\n"
    "v_mfma_f32_16x16x32_bf16 v[52:55], a[44:47], a[232:235], v[52:55]\n"
    "ds_read_b64_tr_b16 a[102:103], v237 offset:3584\n"
    "v_accvgpr_write_b32 a251, v224\n"
    "v_mfma_f32_16x16x32_bf16 v[84:87], a[40:43], a[220:223], v[84:87]\n"
    "v_bfe_u32 v251, v238, 4, 1\n"
    "v_accvgpr_read_b32 v254, a156\n"
    "s_waitcnt vmcnt(4)\n"
    "v_accvgpr_read_b32 v239, a172\n"
    "v_mfma_f32_16x16x32_bf16 v[84:87], a[44:47], a[236:239], v[84:87]\n"
    "ds_read_b64_tr_b16 a[104:105], v236 offset:6656\n"
    "v_lshlrev_b32 v251, 1, v251\n"
    "v_mfma_f32_16x16x32_bf16 v[116:119], a[40:43], a[224:227], v[116:119]\n"
    "ds_read_b128 a[140:143], v243 offset:28672\n"
    "v_add_f32 v254, v254, v239\n"
    "v_mfma_f32_16x16x32_bf16 v[116:119], a[44:47], a[240:243], v[116:119]\n"
    "v_cvt_pk_bf16_f32 v239, v254, v254\n"
    "v_xor_b32 v251, v251, v238\n"
    "v_mfma_f32_16x16x32_bf16 v[24:27], a[48:51], a[212:215], v[24:27]\n"
    "ds_read_b64_tr_b16 a[106:107], v237 offset:6656\n"
    "v_and_b32 v225, 0xffff0000, v239\n"
    "v_sub_f32 v254, v254, v225\n"
    "v_mfma_f32_16x16x32_bf16 v[24:27], a[52:55], a[228:231], v[24:27]\n"
    "ds_read_b64_tr_b16 a[108:109], v236 offset:9728\n"
    "ds_read_b64_tr_b16 a[110:111], v237 offset:9728\n"
    "v_mfma_f32_16x16x32_bf16 v[56:59], a[48:51], a[216:219], v[56:59]\n"
    "v_and_b32 v251, 3, v251\n"
    "v_cvt_pk_bf16_f32 v224, v254, 0\n"
    "v_mfma_f32_16x16x32_bf16 v[56:59], a[52:55], a[232:235], v[56:59]\n"
    "v_accvgpr_write_b32 a252, v239\n"
    "v_lshlrev_b32 v251, 4, v251\n"
    "v_mfma_f32_16x16x32_bf16 v[88:91], a[48:51], a[220:223], v[88:91]\n"
    "v_accvgpr_write_b32 a253, v224\n"
    "v_accvgpr_read_b32 v254, a157\n"
    "v_lshrrev_b32 v252, 5, v238\n"
    "v_mfma_f32_16x16x32_bf16 v[88:91], a[52:55], a[236:239], v[88:91]\n"
    "v_accvgpr_read_b32 v239, a173\n"
    "v_add_f32 v254, v254, v239\n"
    "v_mfma_f32_16x16x32_bf16 v[120:123], a[48:51], a[224:227], v[120:123]\n"
    "v_cvt_pk_bf16_f32 v239, v254, v254\n"
    "v_and_b32 v225, 0xffff0000, v239\n"
    "v_mfma_f32_16x16x32_bf16 v[120:123], a[52:55], a[240:243], v[120:123]\n"
    "v_lshl_add_u32 v251, v252, 6, v251\n"
    "v_bfe_u32 v252, v238, 2, 3\n"
    "v_sub_f32 v254, v254, v225\n"
    "v_cvt_pk_bf16_f32 v224, v254, 0\n"
    "v_lshl_add_u32 v251, v252, 10, v251\n"
    "v_accvgpr_write_b32 a254, v239\n"
    "v_accvgpr_write_b32 a255, v224\n"
    "s_waitcnt lgkmcnt(0)\n"
    "s_barrier\n"
    "v_mfma_f32_16x16x32_bf16 v[28:31], a[56:59], a[212:215], v[28:31]\n"
    "s_add_u32 m0, s75, 16384\n"
    "v_accvgpr_read_b32 v254, a151\n"
    "v_mfma_f32_16x16x32_bf16 v[28:31], a[60:63], a[228:231], v[28:31]\n"
    "v_accvgpr_read_b32 v239, a167\n"
    "v_add_f32 v254, v254, v239\n"
    "v_cvt_pk_bf16_f32 v239, v254, v254\n"
    "v_mfma_f32_16x16x32_bf16 v[60:63], a[56:59], a[216:219], v[60:63]\n"
    "v_and_b32 v225, 0xffff0000, v239\n"
    "v_sub_f32 v254, v254, v225\n"
    "v_cvt_pk_bf16_f32 v224, v254, 0\n"
    "v_mfma_f32_16x16x32_bf16 v[60:63], a[60:63], a[232:235], v[60:63]\n"
    "buffer_load_dwordx4 v249, s[48:51], s73 offen lds\n"
    "v_mfma_f32_16x16x32_bf16 v[92:95], a[56:59], a[220:223], v[92:95]\n"
    "v_accvgpr_write_b32 a158, v239\n"
    "v_accvgpr_write_b32 a159, v224\n"
    "v_accvgpr_read_b32 v254, a150\n"
    "v_mfma_f32_16x16x32_bf16 v[92:95], a[60:63], a[236:239], v[92:95]\n"
    "v_accvgpr_read_b32 v239, a166\n"
    "v_add_f32 v254, v254, v239\n"
    "v_cvt_pk_bf16_f32 v239, v254, v254\n"
    "v_mfma_f32_16x16x32_bf16 v[124:127], a[56:59], a[224:227], v[124:127]\n"
    "v_and_b32 v225, 0xffff0000, v239\n"
    "v_sub_f32 v254, v254, v225\n"
    "v_mfma_f32_16x16x32_bf16 v[124:127], a[60:63], a[240:243], v[124:127]\n"
    "v_cvt_pk_bf16_f32 v224, v254, 0\n"
    "v_accvgpr_write_b32 a156, v239\n"
    "v_accvgpr_write_b32 a157, v224\n"
    "v_mfma_f32_16x16x32_bf16 v[128:131], a[64:67], a[212:215], v[128:131]\n"
    "buffer_load_dwordx4 v250, s[48:51], s73 offen offset:1024 lds\n"
    "v_accvgpr_read_b32 v254, a149\n"
    "v_mfma_f32_16x16x32_bf16 v[128:131], a[68:71], a[228:231], v[128:131]\n"
    "v_accvgpr_read_b32 v239, a165\n"
    "v_add_f32 v254, v254, v239\n"
    "v_mfma_f32_16x16x32_bf16 v[152:155], a[64:67], a[216:219], v[152:155]\n"
    "v_cvt_pk_bf16_f32 v239, v254, v254\n"
    "v_and_b32 v225, 0xffff0000, v239\n"
    "v_sub_f32 v254, v254, v225\n"
    "v_mfma_f32_16x16x32_bf16 v[152:155], a[68:71], a[232:235], v[152:155]\n"
    "v_cvt_pk_bf16_f32 v224, v254, 0\n"
    "v_accvgpr_write_b32 a154, v239\n"
    "v_accvgpr_write_b32 a155, v224\n"
    "v_mfma_f32_16x16x32_bf16 v[176:179], a[64:67], a[220:223], v[176:179]\n"
    "v_accvgpr_read_b32 v254, a148\n"
    "v_accvgpr_read_b32 v239, a164\n"
    "v_mfma_f32_16x16x32_bf16 v[176:179], a[68:71], a[236:239], v[176:179]\n"
    "buffer_load_dwordx4 v249, s[48:51], s73 offen offset:2048 lds\n"
    "v_add_f32 v254, v254, v239\n"
    "v_mfma_f32_16x16x32_bf16 v[200:203], a[64:67], a[224:227], v[200:203]\n"
    "v_cvt_pk_bf16_f32 v239, v254, v254\n"
    "v_and_b32 v225, 0xffff0000, v239\n"
    "v_sub_f32 v254, v254, v225\n"
    "v_mfma_f32_16x16x32_bf16 v[200:203], a[68:71], a[240:243], v[200:203]\n"
    "v_cvt_pk_bf16_f32 v224, v254, 0\n"
    "v_accvgpr_write_b32 a152, v239\n"
    "v_mfma_f32_16x16x32_bf16 v[132:135], a[72:75], a[212:215], v[132:135]\n"
    "v_accvgpr_write_b32 a153, v224\n"
    "v_accvgpr_read_b32 v254, a147\n"
    "v_accvgpr_read_b32 v239, a163\n"
    "v_mfma_f32_16x16x32_bf16 v[132:135], a[76:79], a[228:231], v[132:135]\n"
    "v_add_f32 v254, v254, v239\n"
    "v_cvt_pk_bf16_f32 v239, v254, v254\n"
    "v_and_b32 v225, 0xffff0000, v239\n"
    "v_mfma_f32_16x16x32_bf16 v[156:159], a[72:75], a[216:219], v[156:159]\n"
    "buffer_load_dwordx4 v250, s[48:51], s73 offen offset:3072 lds\n"
    "v_mfma_f32_16x16x32_bf16 v[156:159], a[76:79], a[232:235], v[156:159]\n"
    "v_sub_f32 v254, v254, v225\n"
    "v_cvt_pk_bf16_f32 v224, v254, 0\n"
    "v_accvgpr_write_b32 a150, v239\n"
    "v_mfma_f32_16x16x32_bf16 v[180:183], a[72:75], a[220:223], v[180:183]\n"
    "v_accvgpr_write_b32 a151, v224\n"
    "v_accvgpr_read_b32 v254, a146\n"
    "v_accvgpr_read_b32 v239, a162\n"
    "v_mfma_f32_16x16x32_bf16 v[180:183], a[76:79], a[236:239], v[180:183]\n"
    "v_add_f32 v254, v254, v239\n"
    "v_cvt_pk_bf16_f32 v239, v254, v254\n"
    "v_mfma_f32_16x16x32_bf16 v[204:207], a[72:75], a[224:227], v[204:207]\n"
    "s_add_u32 m0, s74, 16384\n"
    "v_and_b32 v225, 0xffff0000, v239\n"
    "v_sub_f32 v254, v254, v225\n"
    "v_mfma_f32_16x16x32_bf16 v[204:207], a[76:79], a[240:243], v[204:207]\n"
    "v_cvt_pk_bf16_f32 v224, v254, 0\n"
    "v_accvgpr_write_b32 a148, v239\n"
    "v_accvgpr_write_b32 a149, v224\n"
    "v_mfma_f32_16x16x32_bf16 v[136:139], a[80:83], a[212:215], v[136:139]\n"
    "v_accvgpr_read_b32 v254, a145\n"
    "v_accvgpr_read_b32 v239, a161\n"
    "v_mfma_f32_16x16x32_bf16 v[136:139], a[84:87], a[228:231], v[136:139]\n"
    "buffer_load_dwordx4 v245, s[44:47], s98 offen lds\n"
    "v_add_f32 v254, v254, v239\n"
    "v_mfma_f32_16x16x32_bf16 v[160:163], a[80:83], a[216:219], v[160:163]\n"
    "v_cvt_pk_bf16_f32 v239, v254, v254\n"
    "v_and_b32 v225, 0xffff0000, v239\n"
    "v_sub_f32 v254, v254, v225\n"
    "v_mfma_f32_16x16x32_bf16 v[160:163], a[84:87], a[232:235], v[160:163]\n"
    "v_cvt_pk_bf16_f32 v224, v254, 0\n"
    "v_accvgpr_write_b32 a146, v239\n"
    "v_mfma_f32_16x16x32_bf16 v[184:187], a[80:83], a[220:223], v[184:187]\n"
    "v_accvgpr_write_b32 a147, v224\n"
    "v_accvgpr_read_b32 v254, a144\n"
    "v_accvgpr_read_b32 v239, a160\n"
    "v_mfma_f32_16x16x32_bf16 v[184:187], a[84:87], a[236:239], v[184:187]\n"
    "v_add_f32 v254, v254, v239\n"
    "v_cvt_pk_bf16_f32 v239, v254, v254\n"
    "v_and_b32 v225, 0xffff0000, v239\n"
    "v_mfma_f32_16x16x32_bf16 v[208:211], a[80:83], a[224:227], v[208:211]\n"
    "buffer_load_dwordx4 v246, s[44:47], s98 offen offset:1024 lds\n"
    "v_mfma_f32_16x16x32_bf16 v[208:211], a[84:87], a[240:243], v[208:211]\n"
    "v_sub_f32 v254, v254, v225\n"
    "v_cvt_pk_bf16_f32 v224, v254, 0\n"
    "v_accvgpr_write_b32 a144, v239\n"
    "v_mfma_f32_16x16x32_bf16 v[140:143], a[88:91], a[212:215], v[140:143]\n"
    "v_accvgpr_write_b32 a145, v224\n"
    "v_accvgpr_read_b32 v254, a176\n"
    "v_sub_f32 v254, 0, v254\n"
    "v_mfma_f32_16x16x32_bf16 v[140:143], a[92:95], a[228:231], v[140:143]\n"
    "v_cvt_pk_bf16_f32 v239, v254, 0\n"
    "v_lshlrev_b32 v236, 16, v239\n"
    "v_mfma_f32_16x16x32_bf16 v[164:167], a[88:91], a[216:219], v[164:167]\n"
    "v_accvgpr_write_b32 a185, v239\n"
    "v_sub_f32 v236, v254, v236\n"
    "v_accvgpr_read_b32 v239, a177\n"
    "v_mfma_f32_16x16x32_bf16 v[164:167], a[92:95], a[232:235], v[164:167]\n"
    "buffer_load_dwordx4 v247, s[44:47], s98 offen offset:2048 lds\n"
    "v_cvt_pk_bf16_f32 v254, v254, v236\n"
    "v_mfma_f32_16x16x32_bf16 v[188:191], a[88:91], a[220:223], v[188:191]\n"
    "v_mul_f32 v224, 0x3e0293ee, v239\n"
    "v_accvgpr_write_b32 a184, v254\n"
    "v_mfma_f32_16x16x32_bf16 v[188:191], a[92:95], a[236:239], v[188:191]\n"
    "v_mul_f32 v232, 0xbfb8aa3b, v239\n"
    "v_rcp_f32 v228, v239\n"
    "v_mfma_f32_16x16x32_bf16 v[212:215], a[88:91], a[224:227], v[212:215]\n"
    "v_accvgpr_read_b32 v254, a178\n"
    "v_sub_f32 v254, 0, v254\n"
    "v_cvt_pk_bf16_f32 v239, v254, 0\n"
    "v_mfma_f32_16x16x32_bf16 v[212:215], a[92:95], a[240:243], v[212:215]\n"
    "v_lshlrev_b32 v236, 16, v239\n"
    "v_accvgpr_write_b32 a187, v239\n"
    "v_mfma_f32_16x16x32_bf16 v[144:147], a[96:99], a[212:215], v[144:147]\n"
    "buffer_load_dwordx4 v248, s[44:47], s98 offen offset:3072 lds\n"
    "v_sub_f32 v236, v254, v236\n"
    "v_mfma_f32_16x16x32_bf16 v[144:147], a[100:103], a[228:231], v[144:147]\n"
    "v_accvgpr_read_b32 v239, a179\n"
    "v_cvt_pk_bf16_f32 v254, v254, v236\n"
    "v_mul_f32 v225, 0x3e0293ee, v239\n"
    "v_mfma_f32_16x16x32_bf16 v[168:171], a[96:99], a[216:219], v[168:171]\n"
    "v_accvgpr_write_b32 a186, v254\n"
    "v_mul_f32 v233, 0xbfb8aa3b, v239\n"
    "v_mfma_f32_16x16x32_bf16 v[168:171], a[100:103], a[232:235], v[168:171]\n"
    "v_rcp_f32 v229, v239\n"
    "v_accvgpr_read_b32 v254, a180\n"
    "v_mfma_f32_16x16x32_bf16 v[192:195], a[96:99], a[220:223], v[192:195]\n"
    "v_sub_f32 v254, 0, v254\n"
    "v_cvt_pk_bf16_f32 v239, v254, 0\n"
    "v_lshlrev_b32 v236, 16, v239\n"
    "v_mfma_f32_16x16x32_bf16 v[192:195], a[100:103], a[236:239], v[192:195]\n"
    "s_add_u32 s73, s73, 16384\n"
    "v_accvgpr_write_b32 a189, v239\n"
    "v_mfma_f32_16x16x32_bf16 v[216:219], a[96:99], a[224:227], v[216:219]\n"
    "v_sub_f32 v236, v254, v236\n"
    "v_accvgpr_read_b32 v239, a181\n"
    "v_cvt_pk_bf16_f32 v254, v254, v236\n"
    "v_mfma_f32_16x16x32_bf16 v[216:219], a[100:103], a[240:243], v[216:219]\n"
    "s_and_b32 s73, s73, 0x1ffff\n"
    "v_mul_f32 v226, 0x3e0293ee, v239\n"
    "v_accvgpr_write_b32 a188, v254\n"
    "v_mfma_f32_16x16x32_bf16 v[148:151], a[104:107], a[212:215], v[148:151]\n"
    "s_add_u32 s98, s98, 16384\n"
    "v_mul_f32 v234, 0xbfb8aa3b, v239\n"
    "v_mfma_f32_16x16x32_bf16 v[148:151], a[108:111], a[228:231], v[148:151]\n"
    "v_rcp_f32 v230, v239\n"
    "v_accvgpr_read_b32 v254, a182\n"
    "v_mfma_f32_16x16x32_bf16 v[172:175], a[104:107], a[216:219], v[172:175]\n"
    "v_sub_f32 v254, 0, v254\n"
    "v_cvt_pk_bf16_f32 v239, v254, 0\n"
    "v_lshlrev_b32 v236, 16, v239\n"
    "v_mfma_f32_16x16x32_bf16 v[172:175], a[108:111], a[232:235], v[172:175]\n"
    "v_accvgpr_write_b32 a191, v239\n"
    "v_sub_f32 v236, v254, v236\n"
    "v_mfma_f32_16x16x32_bf16 v[196:199], a[104:107], a[220:223], v[196:199]\n"
    "s_and_b32 s98, s98, 0x1ffff\n"
    "v_accvgpr_read_b32 v239, a183\n"
    "v_cvt_pk_bf16_f32 v254, v254, v236\n"
    "v_mfma_f32_16x16x32_bf16 v[196:199], a[108:111], a[236:239], v[196:199]\n"
    "v_mul_f32 v227, 0x3e0293ee, v239\n"
    "v_accvgpr_write_b32 a190, v254\n"
    "v_mul_f32 v235, 0xbfb8aa3b, v239\n"
    "v_mfma_f32_16x16x32_bf16 v[220:223], a[104:107], a[224:227], v[220:223]\n"
    "v_rcp_f32 v231, v239\n"
    "v_mfma_f32_16x16x32_bf16 v[220:223], a[108:111], a[240:243], v[220:223]\n"
    "v_mfma_f32_16x16x16_bf16 v[0:3], a[144:145], a[184:185], v[0:3]\n"
    "v_mfma_f32_16x16x16_bf16 v[4:7], a[146:147], a[184:185], v[4:7]\n"
    "v_mfma_f32_16x16x16_bf16 v[8:11], a[148:149], a[184:185], v[8:11]\n"
    "s_nop 5\n"
    "v_mul_f32 v0, v0, v224\n"
    "v_mul_f32 v1, v1, v224\n"
    "v_mul_f32 v2, v2, v224\n"
    "v_mul_f32 v3, v3, v224\n"
    "v_mfma_f32_16x16x16_bf16 v[12:15], a[150:151], a[184:185], v[12:15]\n"
    "v_mul_f32 v4, v4, v224\n"
    "v_mul_f32 v5, v5, v224\n"
    "v_mul_f32 v6, v6, v224\n"
    "v_mul_f32 v7, v7, v224\n"
    "v_cvt_pk_bf16_f32 v0, v0, v1\n"
    "v_cvt_pk_bf16_f32 v1, v2, v3\n"
    "v_cvt_pk_bf16_f32 v2, v4, v5\n"
    "v_mfma_f32_16x16x16_bf16 v[16:19], a[152:153], a[184:185], v[16:19]\n"
    "v_cvt_pk_bf16_f32 v3, v6, v7\n"
    "v_mul_f32 v8, v8, v224\n"
    "v_mul_f32 v9, v9, v224\n"
    "v_mul_f32 v10, v10, v224\n"
    "v_mul_f32 v11, v11, v224\n"
    "v_mfma_f32_16x16x16_bf16 v[20:23], a[154:155], a[184:185], v[20:23]\n"
    "v_mul_f32 v12, v12, v224\n"
    "v_mul_f32 v13, v13, v224\n"
    "v_mul_f32 v14, v14, v224\n"
    "v_mul_f32 v15, v15, v224\n"
    "v_cvt_pk_bf16_f32 v8, v8, v9\n"
    "v_cvt_pk_bf16_f32 v9, v10, v11\n"
    "v_mfma_f32_16x16x16_bf16 v[24:27], a[156:157], a[184:185], v[24:27]\n"
    "v_cvt_pk_bf16_f32 v10, v12, v13\n"
    "v_cvt_pk_bf16_f32 v11, v14, v15\n"
    "v_mul_f32 v16, v16, v224\n"
    "v_mul_f32 v17, v17, v224\n"
    "v_mul_f32 v18, v18, v224\n"
    "v_mul_f32 v19, v19, v224\n"
    "v_mfma_f32_16x16x16_bf16 v[28:31], a[158:159], a[184:185], v[28:31]\n"
    "v_mul_f32 v20, v20, v224\n"
    "v_mul_f32 v21, v21, v224\n"
    "v_mul_f32 v22, v22, v224\n"
    "v_mul_f32 v23, v23, v224\n"
    "v_cvt_pk_bf16_f32 v16, v16, v17\n"
    "v_cvt_pk_bf16_f32 v17, v18, v19\n"
    "v_cvt_pk_bf16_f32 v18, v20, v21\n"
    "v_mfma_f32_16x16x16_bf16 v[32:35], a[144:145], a[186:187], v[32:35]\n"
    "v_cvt_pk_bf16_f32 v19, v22, v23\n"
    "v_mul_f32 v24, v24, v224\n"
    "v_mul_f32 v25, v25, v224\n"
    "v_mul_f32 v26, v26, v224\n"
    "v_mul_f32 v27, v27, v224\n"
    "v_mfma_f32_16x16x16_bf16 v[36:39], a[146:147], a[186:187], v[36:39]\n"
    "v_mul_f32 v28, v28, v224\n"
    "v_mul_f32 v29, v29, v224\n"
    "v_mul_f32 v30, v30, v224\n"
    "v_mul_f32 v31, v31, v224\n"
    "v_cvt_pk_bf16_f32 v24, v24, v25\n"
    "v_cvt_pk_bf16_f32 v25, v26, v27\n"
    "v_cvt_pk_bf16_f32 v26, v28, v29\n"
    "v_mfma_f32_16x16x16_bf16 v[40:43], a[148:149], a[186:187], v[40:43]\n"
    "v_cvt_pk_bf16_f32 v27, v30, v31\n"
    "v_mul_f32 v32, v32, v225\n"
    "v_mul_f32 v33, v33, v225\n"
    "v_mul_f32 v34, v34, v225\n"
    "v_mul_f32 v35, v35, v225\n"
    "v_mfma_f32_16x16x16_bf16 v[44:47], a[150:151], a[186:187], v[44:47]\n"
    "v_mul_f32 v36, v36, v225\n"
    "v_mul_f32 v37, v37, v225\n"
    "v_mul_f32 v38, v38, v225\n"
    "v_mul_f32 v39, v39, v225\n"
    "v_cvt_pk_bf16_f32 v32, v32, v33\n"
    "v_cvt_pk_bf16_f32 v33, v34, v35\n"
    "v_cvt_pk_bf16_f32 v34, v36, v37\n"
    "v_mfma_f32_16x16x16_bf16 v[48:51], a[152:153], a[186:187], v[48:51]\n"
    "s_mul_i32 s63, s61, 86\n"
    "s_add_u32 s64, s63, 86\n"
    "v_cvt_pk_bf16_f32 v35, v38, v39\n"
    "v_mul_f32 v40, v40, v225\n"
    "v_mul_f32 v41, v41, v225\n"
    "v_mul_f32 v42, v42, v225\n"
    "v_mul_f32 v43, v43, v225\n"
    "v_mfma_f32_16x16x16_bf16 v[52:55], a[154:155], a[186:187], v[52:55]\n"
    "v_mul_f32 v44, v44, v225\n"
    "v_mul_f32 v45, v45, v225\n"
    "v_mul_f32 v46, v46, v225\n"
    "v_mul_f32 v47, v47, v225\n"
    "v_cvt_pk_bf16_f32 v40, v40, v41\n"
    "v_cvt_pk_bf16_f32 v41, v42, v43\n"
    "v_mfma_f32_16x16x16_bf16 v[56:59], a[156:157], a[186:187], v[56:59]\n"
    "s_lshr_b32 s63, s63, 5\n"
    "v_cvt_pk_bf16_f32 v42, v44, v45\n"
    "v_cvt_pk_bf16_f32 v43, v46, v47\n"
    "v_mul_f32 v48, v48, v225\n"
    "v_mul_f32 v49, v49, v225\n"
    "v_mul_f32 v50, v50, v225\n"
    "v_mul_f32 v51, v51, v225\n"
    "v_mfma_f32_16x16x16_bf16 v[60:63], a[158:159], a[186:187], v[60:63]\n"
    "v_mul_f32 v52, v52, v225\n"
    "v_mul_f32 v53, v53, v225\n"
    "v_mul_f32 v54, v54, v225\n"
    "v_mul_f32 v55, v55, v225\n"
    "v_cvt_pk_bf16_f32 v48, v48, v49\n"
    "v_cvt_pk_bf16_f32 v49, v50, v51\n"
    "v_cvt_pk_bf16_f32 v50, v52, v53\n"
    "v_mfma_f32_16x16x16_bf16 v[64:67], a[144:145], a[188:189], v[64:67]\n"
    "s_lshr_b32 s64, s64, 5\n"
    "v_cvt_pk_bf16_f32 v51, v54, v55\n"
    "v_mul_f32 v56, v56, v225\n"
    "v_mul_f32 v57, v57, v225\n"
    "v_mul_f32 v58, v58, v225\n"
    "v_mul_f32 v59, v59, v225\n"
    "v_mfma_f32_16x16x16_bf16 v[68:71], a[146:147], a[188:189], v[68:71]\n"
    "v_mul_f32 v60, v60, v225\n"
    "v_mul_f32 v61, v61, v225\n"
    "v_mul_f32 v62, v62, v225\n"
    "v_mul_f32 v63, v63, v225\n"
    "v_cvt_pk_bf16_f32 v56, v56, v57\n"
    "v_cvt_pk_bf16_f32 v57, v58, v59\n"
    "v_cvt_pk_bf16_f32 v58, v60, v61\n"
    "v_mfma_f32_16x16x16_bf16 v[72:75], a[148:149], a[188:189], v[72:75]\n"
    "s_sub_u32 s64, s64, s63\n"
    "v_cvt_pk_bf16_f32 v59, v62, v63\n"
    "v_mul_f32 v64, v64, v226\n"
    "v_mul_f32 v65, v65, v226\n"
    "v_mul_f32 v66, v66, v226\n"
    "v_mul_f32 v67, v67, v226\n"
    "v_mfma_f32_16x16x16_bf16 v[76:79], a[150:151], a[188:189], v[76:79]\n"
    "v_mul_f32 v68, v68, v226\n"
    "v_mul_f32 v69, v69, v226\n"
    "v_mul_f32 v70, v70, v226\n"
    "v_mul_f32 v71, v71, v226\n"
    "v_cvt_pk_bf16_f32 v64, v64, v65\n"
    "v_cvt_pk_bf16_f32 v65, v66, v67\n"
    "v_cvt_pk_bf16_f32 v66, v68, v69\n"
    "v_mfma_f32_16x16x16_bf16 v[80:83], a[152:153], a[188:189], v[80:83]\n"
    "s_lshl_b32 s87, s62, 4\n"
    "v_cvt_pk_bf16_f32 v67, v70, v71\n"
    "v_mul_f32 v72, v72, v226\n"
    "v_mul_f32 v73, v73, v226\n"
    "v_mul_f32 v74, v74, v226\n"
    "v_mul_f32 v75, v75, v226\n"
    "v_mfma_f32_16x16x16_bf16 v[84:87], a[154:155], a[188:189], v[84:87]\n"
    "v_mul_f32 v76, v76, v226\n"
    "v_mul_f32 v77, v77, v226\n"
    "v_mul_f32 v78, v78, v226\n"
    "v_mul_f32 v79, v79, v226\n"
    "v_cvt_pk_bf16_f32 v72, v72, v73\n"
    "v_cvt_pk_bf16_f32 v73, v74, v75\n"
    "v_mfma_f32_16x16x16_bf16 v[88:91], a[156:157], a[188:189], v[88:91]\n"
    "v_cvt_pk_bf16_f32 v74, v76, v77\n"
    "v_cvt_pk_bf16_f32 v75, v78, v79\n"
    "v_mul_f32 v80, v80, v226\n"
    "v_mul_f32 v81, v81, v226\n"
    "v_mul_f32 v82, v82, v226\n"
    "v_mul_f32 v83, v83, v226\n"
    "v_mfma_f32_16x16x16_bf16 v[92:95], a[158:159], a[188:189], v[92:95]\n"
    "v_mul_f32 v84, v84, v226\n"
    "v_mul_f32 v85, v85, v226\n"
    "v_mul_f32 v86, v86, v226\n"
    "v_mul_f32 v87, v87, v226\n"
    "v_cvt_pk_bf16_f32 v80, v80, v81\n"
    "v_cvt_pk_bf16_f32 v81, v82, v83\n"
    "v_cvt_pk_bf16_f32 v82, v84, v85\n"
    "v_mfma_f32_16x16x16_bf16 v[96:99], a[144:145], a[190:191], v[96:99]\n"
    "s_lshl_b32 s88, s60, 2\n"
    "v_cvt_pk_bf16_f32 v83, v86, v87\n"
    "v_mul_f32 v88, v88, v226\n"
    "v_mul_f32 v89, v89, v226\n"
    "v_mul_f32 v90, v90, v226\n"
    "v_mul_f32 v91, v91, v226\n"
    "v_mfma_f32_16x16x16_bf16 v[100:103], a[146:147], a[190:191], v[100:103]\n"
    "v_mul_f32 v92, v92, v226\n"
    "v_mul_f32 v93, v93, v226\n"
    "v_mul_f32 v94, v94, v226\n"
    "v_mul_f32 v95, v95, v226\n"
    "v_cvt_pk_bf16_f32 v88, v88, v89\n"
    "v_cvt_pk_bf16_f32 v89, v90, v91\n"
    "v_cvt_pk_bf16_f32 v90, v92, v93\n"
    "v_mfma_f32_16x16x16_bf16 v[104:107], a[148:149], a[190:191], v[104:107]\n"
    "s_add_u32 s87, s87, s88\n"
    "v_cvt_pk_bf16_f32 v91, v94, v95\n"
    "v_mul_f32 v96, v96, v227\n"
    "v_mul_f32 v97, v97, v227\n"
    "v_mul_f32 v98, v98, v227\n"
    "v_mul_f32 v99, v99, v227\n"
    "v_mfma_f32_16x16x16_bf16 v[108:111], a[150:151], a[190:191], v[108:111]\n"
    "v_mul_f32 v100, v100, v227\n"
    "v_mul_f32 v101, v101, v227\n"
    "v_mul_f32 v102, v102, v227\n"
    "v_mul_f32 v103, v103, v227\n"
    "v_cvt_pk_bf16_f32 v96, v96, v97\n"
    "v_cvt_pk_bf16_f32 v97, v98, v99\n"
    "v_cvt_pk_bf16_f32 v98, v100, v101\n"
    "v_mfma_f32_16x16x16_bf16 v[112:115], a[152:153], a[190:191], v[112:115]\n"
    "s_mul_i32 s87, s87, 86\n"
    "v_cvt_pk_bf16_f32 v99, v102, v103\n"
    "v_mul_f32 v104, v104, v227\n"
    "v_mul_f32 v105, v105, v227\n"
    "v_mul_f32 v106, v106, v227\n"
    "v_mul_f32 v107, v107, v227\n"
    "v_mfma_f32_16x16x16_bf16 v[116:119], a[154:155], a[190:191], v[116:119]\n"
    "v_mul_f32 v108, v108, v227\n"
    "v_mul_f32 v109, v109, v227\n"
    "v_mul_f32 v110, v110, v227\n"
    "v_mul_f32 v111, v111, v227\n"
    "v_cvt_pk_bf16_f32 v104, v104, v105\n"
    "v_cvt_pk_bf16_f32 v105, v106, v107\n"
    "v_mfma_f32_16x16x16_bf16 v[120:123], a[156:157], a[190:191], v[120:123]\n"
    "s_add_u32 s87, s87, s63\n"
    "v_cvt_pk_bf16_f32 v106, v108, v109\n"
    "v_cvt_pk_bf16_f32 v107, v110, v111\n"
    "v_mul_f32 v112, v112, v227\n"
    "v_mul_f32 v113, v113, v227\n"
    "v_mul_f32 v114, v114, v227\n"
    "v_mul_f32 v115, v115, v227\n"
    "v_mfma_f32_16x16x16_bf16 v[124:127], a[158:159], a[190:191], v[124:127]\n"
    "v_mul_f32 v116, v116, v227\n"
    "v_mul_f32 v117, v117, v227\n"
    "v_mul_f32 v118, v118, v227\n"
    "v_mul_f32 v119, v119, v227\n"
    "v_cvt_pk_bf16_f32 v112, v112, v113\n"
    "v_cvt_pk_bf16_f32 v113, v114, v115\n"
    "v_cvt_pk_bf16_f32 v114, v116, v117\n"
    "v_mfma_f32_16x16x16_bf16 v[128:131], a[244:245], a[184:185], v[128:131]\n"
    "s_lshl_b32 s87, s87, 9\n"
    "v_cvt_pk_bf16_f32 v115, v118, v119\n"
    "v_mul_f32 v120, v120, v227\n"
    "v_mul_f32 v121, v121, v227\n"
    "v_mul_f32 v122, v122, v227\n"
    "v_mul_f32 v123, v123, v227\n"
    "v_mfma_f32_16x16x16_bf16 v[132:135], a[246:247], a[184:185], v[132:135]\n"
    "v_mul_f32 v124, v124, v227\n"
    "v_mul_f32 v125, v125, v227\n"
    "v_mul_f32 v126, v126, v227\n"
    "v_mul_f32 v127, v127, v227\n"
    "v_cvt_pk_bf16_f32 v120, v120, v121\n"
    "v_cvt_pk_bf16_f32 v121, v122, v123\n"
    "v_cvt_pk_bf16_f32 v122, v124, v125\n"
    "v_mfma_f32_16x16x16_bf16 v[136:139], a[248:249], a[184:185], v[136:139]\n"
    "s_add_u32 s87, s87, 17190912\n"
    "v_cvt_pk_bf16_f32 v123, v126, v127\n"
    "v_mul_f32 v128, v128, v232\n"
    "v_mul_f32 v129, v129, v232\n"
    "v_mul_f32 v130, v130, v232\n"
    "v_mul_f32 v131, v131, v232\n"
    "v_mfma_f32_16x16x16_bf16 v[140:143], a[250:251], a[184:185], v[140:143]\n"
    "v_exp_f32 v128, v128\n"
    "v_exp_f32 v129, v129\n"
    "v_exp_f32 v130, v130\n"
    "v_mfma_f32_16x16x16_bf16 v[144:147], a[252:253], a[184:185], v[144:147]\n"
    "s_add_u32 s88, s87, 512\n"
    "v_exp_f32 v131, v131\n"
    "v_fma_f32 v128, v128, v228, v228\n"
    "v_fma_f32 v129, v129, v228, v228\n"
    "v_fma_f32 v130, v130, v228, v228\n"
    "v_fma_f32 v131, v131, v228, v228\n"
    "v_mfma_f32_16x16x16_bf16 v[148:151], a[254:255], a[184:185], v[148:151]\n"
    "v_rcp_f32 v128, v128\n"
    "v_rcp_f32 v129, v129\n"
    "v_rcp_f32 v130, v130\n"
    "v_mfma_f32_16x16x16_bf16 v[152:155], a[244:245], a[186:187], v[152:155]\n"
    "v_rcp_f32 v131, v131\n"
    "v_mul_f32 v132, v132, v128\n"
    "v_mul_f32 v133, v133, v129\n"
    "v_mul_f32 v134, v134, v130\n"
    "v_mul_f32 v135, v135, v131\n"
    "v_cvt_pk_bf16_f32 v132, v132, v133\n"
    "v_mfma_f32_16x16x16_bf16 v[156:159], a[246:247], a[186:187], v[156:159]\n"
    "v_cvt_pk_bf16_f32 v133, v134, v135\n"
    "v_mul_f32 v136, v136, v232\n"
    "v_mul_f32 v137, v137, v232\n"
    "v_mul_f32 v138, v138, v232\n"
    "v_mul_f32 v139, v139, v232\n"
    "v_exp_f32 v136, v136\n"
    "v_mfma_f32_16x16x16_bf16 v[160:163], a[248:249], a[186:187], v[160:163]\n"
    "v_exp_f32 v137, v137\n"
    "v_exp_f32 v138, v138\n"
    "v_exp_f32 v139, v139\n"
    "v_mfma_f32_16x16x16_bf16 v[164:167], a[250:251], a[186:187], v[164:167]\n"
    "s_add_u32 s89, s87, 1024\n"
    "v_fma_f32 v136, v136, v228, v228\n"
    "v_fma_f32 v137, v137, v228, v228\n"
    "v_fma_f32 v138, v138, v228, v228\n"
    "v_fma_f32 v139, v139, v228, v228\n"
    "v_rcp_f32 v136, v136\n"
    "v_mfma_f32_16x16x16_bf16 v[168:171], a[252:253], a[186:187], v[168:171]\n"
    "v_rcp_f32 v137, v137\n"
    "v_rcp_f32 v138, v138\n"
    "v_rcp_f32 v139, v139\n"
    "v_mfma_f32_16x16x16_bf16 v[172:175], a[254:255], a[186:187], v[172:175]\n"
    "v_mul_f32 v140, v140, v136\n"
    "v_mul_f32 v141, v141, v137\n"
    "v_mul_f32 v142, v142, v138\n"
    "v_mul_f32 v143, v143, v139\n"
    "v_cvt_pk_bf16_f32 v140, v140, v141\n"
    "v_cvt_pk_bf16_f32 v141, v142, v143\n"
    "v_mul_f32 v144, v144, v232\n"
    "v_mfma_f32_16x16x16_bf16 v[176:179], a[244:245], a[188:189], v[176:179]\n"
    "v_mul_f32 v145, v145, v232\n"
    "v_mul_f32 v146, v146, v232\n"
    "v_mul_f32 v147, v147, v232\n"
    "v_exp_f32 v144, v144\n"
    "v_exp_f32 v145, v145\n"
    "v_mfma_f32_16x16x16_bf16 v[180:183], a[246:247], a[188:189], v[180:183]\n"
    "v_exp_f32 v146, v146\n"
    "v_exp_f32 v147, v147\n"
    "v_fma_f32 v144, v144, v228, v228\n"
    "v_fma_f32 v145, v145, v228, v228\n"
    "v_mfma_f32_16x16x16_bf16 v[184:187], a[248:249], a[188:189], v[184:187]\n"
    "v_fma_f32 v146, v146, v228, v228\n"
    "v_fma_f32 v147, v147, v228, v228\n"
    "v_rcp_f32 v144, v144\n"
    "v_rcp_f32 v145, v145\n"
    "v_mfma_f32_16x16x16_bf16 v[188:191], a[250:251], a[188:189], v[188:191]\n"
    "v_rcp_f32 v146, v146\n"
    "v_rcp_f32 v147, v147\n"
    "v_mul_f32 v148, v148, v144\n"
    "v_mul_f32 v149, v149, v145\n"
    "v_mul_f32 v150, v150, v146\n"
    "v_mfma_f32_16x16x16_bf16 v[192:195], a[252:253], a[188:189], v[192:195]\n"
    "v_mul_f32 v151, v151, v147\n"
    "v_cvt_pk_bf16_f32 v148, v148, v149\n"
    "v_cvt_pk_bf16_f32 v149, v150, v151\n"
    "v_mul_f32 v152, v152, v233\n"
    "v_mul_f32 v153, v153, v233\n"
    "v_mul_f32 v154, v154, v233\n"
    "v_mfma_f32_16x16x16_bf16 v[196:199], a[254:255], a[188:189], v[196:199]\n"
    "v_mul_f32 v155, v155, v233\n"
    "v_exp_f32 v152, v152\n"
    "v_exp_f32 v153, v153\n"
    "v_exp_f32 v154, v154\n"
    "v_mfma_f32_16x16x16_bf16 v[200:203], a[244:245], a[190:191], v[200:203]\n"
    "v_exp_f32 v155, v155\n"
    "v_fma_f32 v152, v152, v229, v229\n"
    "v_fma_f32 v153, v153, v229, v229\n"
    "v_fma_f32 v154, v154, v229, v229\n"
    "v_fma_f32 v155, v155, v229, v229\n"
    "v_mfma_f32_16x16x16_bf16 v[204:207], a[246:247], a[190:191], v[204:207]\n"
    "v_rcp_f32 v152, v152\n"
    "v_rcp_f32 v153, v153\n"
    "v_rcp_f32 v154, v154\n"
    "v_mfma_f32_16x16x16_bf16 v[208:211], a[248:249], a[190:191], v[208:211]\n"
    "v_rcp_f32 v155, v155\n"
    "v_mul_f32 v156, v156, v152\n"
    "v_mul_f32 v157, v157, v153\n"
    "v_mul_f32 v158, v158, v154\n"
    "v_mul_f32 v159, v159, v155\n"
    "v_cvt_pk_bf16_f32 v156, v156, v157\n"
    "v_mfma_f32_16x16x16_bf16 v[212:215], a[250:251], a[190:191], v[212:215]\n"
    "v_cvt_pk_bf16_f32 v157, v158, v159\n"
    "v_mul_f32 v160, v160, v233\n"
    "v_mul_f32 v161, v161, v233\n"
    "v_mul_f32 v162, v162, v233\n"
    "v_mul_f32 v163, v163, v233\n"
    "v_mfma_f32_16x16x16_bf16 v[216:219], a[252:253], a[190:191], v[216:219]\n"
    "v_exp_f32 v160, v160\n"
    "v_exp_f32 v161, v161\n"
    "v_exp_f32 v162, v162\n"
    "v_mfma_f32_16x16x16_bf16 v[220:223], a[254:255], a[190:191], v[220:223]\n"
    "s_cmp_eq_u32 s64, 3\n"
    "s_cselect_b32 s89, s89, 41832448\n"
    "v_and_b32 v252, 31, v238\n"
    "v_lshlrev_b32 v252, 4, v252\n"
    "v_lshrrev_b32 v253, 5, v238\n"
    "v_exp_f32 v163, v163\n"
    "v_fma_f32 v160, v160, v229, v229\n"
    "v_fma_f32 v161, v161, v229, v229\n"
    "v_fma_f32 v162, v162, v229, v229\n"
    "v_fma_f32 v163, v163, v229, v229\n"
    "v_rcp_f32 v160, v160\n"
    "v_lshl_add_u32 v252, v253, 3, v252\n"
    "v_rcp_f32 v161, v161\n"
    "v_rcp_f32 v162, v162\n"
    "v_rcp_f32 v163, v163\n"
    "v_mul_f32 v164, v164, v160\n"
    "v_mul_f32 v165, v165, v161\n"
    "v_mul_f32 v166, v166, v162\n"
    "v_mul_f32 v167, v167, v163\n"
    "v_cvt_pk_bf16_f32 v164, v164, v165\n"
    "v_cvt_pk_bf16_f32 v165, v166, v167\n"
    "s_waitcnt vmcnt(8)\n"
    "s_barrier\n"
    "v_mfma_f32_16x16x32_bf16 v[4:7], a[112:115], v[0:3], 0\n"
    "ds_read_b64_tr_b16 a[64:65], v244 offset:0\n"
    "ds_read_b64_tr_b16 a[66:67], v244 offset:4096\n"
    "v_mul_f32 v168, v168, v233\n"
    "v_mul_f32 v169, v169, v233\n"
    "v_mul_f32 v170, v170, v233\n"
    "v_mul_f32 v171, v171, v233\n"
    "v_exp_f32 v168, v168\n"
    "v_exp_f32 v169, v169\n"
    "v_mfma_f32_16x16x32_bf16 v[4:7], a[116:119], v[8:11], v[4:7]\n"
    "ds_read_b64_tr_b16 a[68:69], v244 offset:256\n"
    "ds_read_b64_tr_b16 a[70:71], v244 offset:4352\n"
    "v_exp_f32 v170, v170\n"
    "v_exp_f32 v171, v171\n"
    "v_fma_f32 v168, v168, v229, v229\n"
    "v_fma_f32 v169, v169, v229, v229\n"
    "v_fma_f32 v170, v170, v229, v229\n"
    "v_fma_f32 v171, v171, v229, v229\n"
    "v_mfma_f32_16x16x32_bf16 v[4:7], a[120:123], v[16:19], v[4:7]\n"
    "ds_read_b64_tr_b16 a[72:73], v244 offset:512\n"
    "v_rcp_f32 v168, v168\n"
    "v_rcp_f32 v169, v169\n"
    "v_rcp_f32 v170, v170\n"
    "v_rcp_f32 v171, v171\n"
    "v_mul_f32 v172, v172, v168\n"
    "v_mfma_f32_16x16x32_bf16 v[4:7], a[124:127], v[24:27], v[4:7]\n"
    "ds_read_b64_tr_b16 a[74:75], v244 offset:4608\n"
    "ds_read_b64_tr_b16 a[76:77], v244 offset:768\n"
    "v_mul_f32 v173, v173, v169\n"
    "v_mul_f32 v174, v174, v170\n"
    "v_mul_f32 v175, v175, v171\n"
    "v_cvt_pk_bf16_f32 v172, v172, v173\n"
    "v_cvt_pk_bf16_f32 v173, v174, v175\n"
    "v_mul_f32 v176, v176, v234\n"
    "v_mul_f32 v177, v177, v234\n"
    "v_mul_f32 v178, v178, v234\n"
    "v_mul_f32 v179, v179, v234\n"
    "v_mfma_f32_16x16x32_bf16 v[12:15], a[128:131], v[0:3], 0\n"
    "ds_read_b64_tr_b16 a[78:79], v244 offset:4864\n"
    "v_exp_f32 v176, v176\n"
    "v_exp_f32 v177, v177\n"
    "v_exp_f32 v178, v178\n"
    "v_exp_f32 v179, v179\n"
    "v_fma_f32 v176, v176, v230, v230\n"
    "v_mfma_f32_16x16x32_bf16 v[12:15], a[132:135], v[8:11], v[12:15]\n"
    "ds_read_b64_tr_b16 a[80:81], v244 offset:1024\n"
    "v_fma_f32 v177, v177, v230, v230\n"
    "v_fma_f32 v178, v178, v230, v230\n"
    "v_fma_f32 v179, v179, v230, v230\n"
    "v_rcp_f32 v176, v176\n"
    "v_rcp_f32 v177, v177\n"
    "v_rcp_f32 v178, v178\n"
    "v_mfma_f32_16x16x32_bf16 v[12:15], a[136:139], v[16:19], v[12:15]\n"
    "ds_read_b64_tr_b16 a[82:83], v244 offset:5120\n"
    "ds_read_b64_tr_b16 a[84:85], v244 offset:1280\n"
    "v_rcp_f32 v179, v179\n"
    "v_mul_f32 v180, v180, v176\n"
    "v_mul_f32 v181, v181, v177\n"
    "v_mul_f32 v182, v182, v178\n"
    "v_mul_f32 v183, v183, v179\n"
    "v_cvt_pk_bf16_f32 v180, v180, v181\n"
    "v_cvt_pk_bf16_f32 v181, v182, v183\n"
    "v_mfma_f32_16x16x32_bf16 v[12:15], a[140:143], v[24:27], v[12:15]\n"
    "ds_read_b64_tr_b16 a[86:87], v244 offset:5376\n"
    "v_mul_f32 v184, v184, v234\n"
    "v_mul_f32 v185, v185, v234\n"
    "v_mul_f32 v186, v186, v234\n"
    "v_mul_f32 v187, v187, v234\n"
    "v_exp_f32 v184, v184\n"
    "v_exp_f32 v185, v185\n"
    "v_exp_f32 v186, v186\n"
    "v_mfma_f32_16x16x32_bf16 v[36:39], a[112:115], v[32:35], 0\n"
    "ds_read_b64_tr_b16 a[88:89], v244 offset:1536\n"
    "ds_read_b64_tr_b16 a[90:91], v244 offset:5632\n"
    "v_exp_f32 v187, v187\n"
    "v_fma_f32 v184, v184, v230, v230\n"
    "v_fma_f32 v185, v185, v230, v230\n"
    "v_fma_f32 v186, v186, v230, v230\n"
    "v_fma_f32 v187, v187, v230, v230\n"
    "v_rcp_f32 v184, v184\n"
    "v_mfma_f32_16x16x32_bf16 v[36:39], a[116:119], v[40:43], v[36:39]\n"
    "ds_read_b64_tr_b16 a[92:93], v244 offset:1792\n"
    "ds_read_b64_tr_b16 a[94:95], v244 offset:5888\n"
    "v_rcp_f32 v185, v185\n"
    "v_rcp_f32 v186, v186\n"
    "v_rcp_f32 v187, v187\n"
    "v_mul_f32 v188, v188, v184\n"
    "v_mul_f32 v189, v189, v185\n"
    "v_mfma_f32_16x16x32_bf16 v[36:39], a[120:123], v[48:51], v[36:39]\n"
    "ds_read_b128 a[32:35], v240 offset:0\n"
    "ds_read_b128 a[36:39], v241 offset:0\n"
    "v_mul_f32 v190, v190, v186\n"
    "v_mul_f32 v191, v191, v187\n"
    "v_cvt_pk_bf16_f32 v188, v188, v189\n"
    "v_cvt_pk_bf16_f32 v189, v190, v191\n"
    "v_mul_f32 v192, v192, v234\n"
    "v_mul_f32 v193, v193, v234\n"
    "v_mul_f32 v194, v194, v234\n"
    "v_mul_f32 v195, v195, v234\n"
    "v_mfma_f32_16x16x32_bf16 v[36:39], a[124:127], v[56:59], v[36:39]\n"
    "ds_read_b128 a[40:43], v242 offset:0\n"
    "v_exp_f32 v192, v192\n"
    "v_exp_f32 v193, v193\n"
    "v_exp_f32 v194, v194\n"
    "v_exp_f32 v195, v195\n"
    "v_fma_f32 v192, v192, v230, v230\n"
    "v_fma_f32 v193, v193, v230, v230\n"
    "v_mfma_f32_16x16x32_bf16 v[44:47], a[128:131], v[32:35], 0\n"
    "ds_read_b128 a[44:47], v243 offset:0\n"
    "ds_read_b128 a[48:51], v240 offset:4096\n"
    "v_fma_f32 v194, v194, v230, v230\n"
    "v_fma_f32 v195, v195, v230, v230\n"
    "v_rcp_f32 v192, v192\n"
    "v_rcp_f32 v193, v193\n"
    "v_rcp_f32 v194, v194\n"
    "v_mfma_f32_16x16x32_bf16 v[44:47], a[132:135], v[40:43], v[44:47]\n"
    "ds_read_b128 a[52:55], v241 offset:4096\n"
    "v_rcp_f32 v195, v195\n"
    "v_mul_f32 v196, v196, v192\n"
    "v_mul_f32 v197, v197, v193\n"
    "v_mul_f32 v198, v198, v194\n"
    "v_mul_f32 v199, v199, v195\n"
    "v_cvt_pk_bf16_f32 v196, v196, v197\n"
    "v_cvt_pk_bf16_f32 v197, v198, v199\n"
    "v_mul_f32 v200, v200, v235\n"
    "v_mfma_f32_16x16x32_bf16 v[44:47], a[136:139], v[48:51], v[44:47]\n"
    "ds_read_b128 a[56:59], v242 offset:4096\n"
    "v_mul_f32 v201, v201, v235\n"
    "v_mul_f32 v202, v202, v235\n"
    "v_mul_f32 v203, v203, v235\n"
    "v_exp_f32 v200, v200\n"
    "v_exp_f32 v201, v201\n"
    "v_exp_f32 v202, v202\n"
    "v_mfma_f32_16x16x32_bf16 v[44:47], a[140:143], v[56:59], v[44:47]\n"
    "ds_read_b128 a[60:63], v243 offset:4096\n"
    "v_exp_f32 v203, v203\n"
    "v_fma_f32 v200, v200, v231, v231\n"
    "v_fma_f32 v201, v201, v231, v231\n"
    "v_fma_f32 v202, v202, v231, v231\n"
    "v_fma_f32 v203, v203, v231, v231\n"
    "v_mfma_f32_16x16x32_bf16 v[68:71], a[112:115], v[64:67], 0\n"
    "v_max3_f32 v20, v4, v5, v6\n"
    "v_rcp_f32 v200, v200\n"
    "v_max3_f32 v52, v36, v37, v38\n"
    "v_max3_f32 v21, v7, v12, v13\n"
    "v_rcp_f32 v201, v201\n"
    "v_max3_f32 v53, v39, v44, v45\n"
    "v_max3_f32 v20, v20, v14, v15\n"
    "v_max3_f32 v52, v52, v46, v47\n"
    "v_mfma_f32_16x16x32_bf16 v[68:71], a[116:119], v[72:75], v[68:71]\n"
    "v_rcp_f32 v202, v202\n"
    "v_max_f32 v20, v20, v21\n"
    "v_max_f32 v52, v52, v53\n"
    "v_rcp_f32 v203, v203\n"
    "v_mov_b32 v21, v20\n"
    "v_mov_b32 v53, v52\n"
    "v_mul_f32 v204, v204, v200\n"
    "v_mul_f32 v205, v205, v201\n"
    "v_mfma_f32_16x16x32_bf16 v[68:71], a[120:123], v[80:83], v[68:71]\n"
    "v_permlane16_swap_b32 v21, v20\n"
    "v_mul_f32 v206, v206, v202\n"
    "v_permlane16_swap_b32 v53, v52\n"
    "v_mul_f32 v207, v207, v203\n"
    "v_max_f32 v20, v20, v21\n"
    "v_cvt_pk_bf16_f32 v204, v204, v205\n"
    "v_max_f32 v52, v52, v53\n"
    "v_cvt_pk_bf16_f32 v205, v206, v207\n"
    "v_mov_b32 v21, v20\n"
    "v_mfma_f32_16x16x32_bf16 v[68:71], a[124:127], v[88:91], v[68:71]\n"
    "v_mul_f32 v208, v208, v235\n"
    "v_mov_b32 v53, v52\n"
    "v_mul_f32 v209, v209, v235\n"
    "v_permlane32_swap_b32 v21, v20\n"
    "v_mul_f32 v210, v210, v235\n"
    "v_permlane32_swap_b32 v53, v52\n"
    "v_mul_f32 v211, v211, v235\n"
    "v_max_f32 v20, v20, v21\n"
    "v_mfma_f32_16x16x32_bf16 v[76:79], a[128:131], v[64:67], 0\n"
    "v_exp_f32 v208, v208\n"
    "v_max_f32 v52, v52, v53\n"
    "v_exp_f32 v209, v209\n"
    "v_sub_f32 v4, v4, v20\n"
    "v_sub_f32 v36, v36, v52\n"
    "v_sub_f32 v5, v5, v20\n"
    "v_exp_f32 v210, v210\n"
    "v_mfma_f32_16x16x32_bf16 v[76:79], a[132:135], v[72:75], v[76:79]\n"
    "v_sub_f32 v37, v37, v52\n"
    "v_sub_f32 v6, v6, v20\n"
    "v_exp_f32 v211, v211\n"
    "v_sub_f32 v38, v38, v52\n"
    "v_sub_f32 v7, v7, v20\n"
    "v_fma_f32 v208, v208, v231, v231\n"
    "v_sub_f32 v39, v39, v52\n"
    "v_sub_f32 v12, v12, v20\n"
    "v_fma_f32 v209, v209, v231, v231\n"
    "v_sub_f32 v44, v44, v52\n"
    "v_mfma_f32_16x16x32_bf16 v[76:79], a[136:139], v[80:83], v[76:79]\n"
    "v_fma_f32 v210, v210, v231, v231\n"
    "v_sub_f32 v13, v13, v20\n"
    "v_fma_f32 v211, v211, v231, v231\n"
    "v_sub_f32 v45, v45, v52\n"
    "v_rcp_f32 v208, v208\n"
    "v_sub_f32 v14, v14, v20\n"
    "v_sub_f32 v46, v46, v52\n"
    "v_sub_f32 v15, v15, v20\n"
    "v_sub_f32 v47, v47, v52\n"
    "v_mfma_f32_16x16x32_bf16 v[76:79], a[140:143], v[88:91], v[76:79]\n"
    "v_rcp_f32 v209, v209\n"
    "v_sub_f32 v23, 0, v20\n"
    "v_rcp_f32 v210, v210\n"
    "v_sub_f32 v55, 0, v52\n"
    "v_sub_f32 v22, 0, v20\n"
    "v_rcp_f32 v211, v211\n"
    "v_sub_f32 v54, 0, v52\n"
    "v_sub_f32 v21, 0, v20\n"
    "v_mfma_f32_16x16x32_bf16 v[100:103], a[112:115], v[96:99], 0\n"
    "v_sub_f32 v53, 0, v52\n"
    "v_mul_f32 v212, v212, v208\n"
    "v_sub_f32 v20, 0, v20\n"
    "v_mul_f32 v213, v213, v209\n"
    "v_sub_f32 v52, 0, v52\n"
    "v_mul_f32 v214, v214, v210\n"
    "v_exp_f32 v4, v4\n"
    "v_mul_f32 v215, v215, v211\n"
    "v_cvt_pk_bf16_f32 v212, v212, v213\n"
    "v_mfma_f32_16x16x32_bf16 v[100:103], a[116:119], v[104:107], v[100:103]\n"
    "v_exp_f32 v5, v5\n"
    "v_cvt_pk_bf16_f32 v213, v214, v215\n"
    "v_exp_f32 v6, v6\n"
    "v_mul_f32 v216, v216, v235\n"
    "v_mul_f32 v217, v217, v235\n"
    "v_cvt_pk_bf16_f32 v4, v4, v5\n"
    "v_mul_f32 v218, v218, v235\n"
    "v_mul_f32 v219, v219, v235\n"
    "v_mfma_f32_16x16x32_bf16 v[100:103], a[120:123], v[112:115], v[100:103]\n"
    "v_exp_f32 v7, v7\n"
    "v_exp_f32 v12, v12\n"
    "v_exp_f32 v216, v216\n"
    "v_cvt_pk_bf16_f32 v5, v6, v7\n"
    "v_exp_f32 v217, v217\n"
    "v_exp_f32 v13, v13\n"
    "v_mfma_f32_16x16x32_bf16 v[100:103], a[124:127], v[120:123], v[100:103]\n"
    "v_exp_f32 v218, v218\n"
    "v_exp_f32 v14, v14\n"
    "v_cvt_pk_bf16_f32 v6, v12, v13\n"
    "v_exp_f32 v219, v219\n"
    "v_exp_f32 v15, v15\n"
    "v_fma_f32 v216, v216, v231, v231\n"
    "v_mfma_f32_16x16x32_bf16 v[108:111], a[128:131], v[96:99], 0\n"
    "v_cvt_pk_bf16_f32 v7, v14, v15\n"
    "v_fma_f32 v217, v217, v231, v231\n"
    "v_exp_f32 v36, v36\n"
    "v_fma_f32 v218, v218, v231, v231\n"
    "v_exp_f32 v37, v37\n"
    "v_fma_f32 v219, v219, v231, v231\n"
    "v_rcp_f32 v216, v216\n"
    "v_mfma_f32_16x16x32_bf16 v[108:111], a[132:135], v[104:107], v[108:111]\n"
    "v_exp_f32 v38, v38\n"
    "v_rcp_f32 v217, v217\n"
    "v_cvt_pk_bf16_f32 v36, v36, v37\n"
    "v_exp_f32 v39, v39\n"
    "v_rcp_f32 v218, v218\n"
    "v_exp_f32 v44, v44\n"
    "v_mfma_f32_16x16x32_bf16 v[108:111], a[136:139], v[112:115], v[108:111]\n"
    "v_rcp_f32 v219, v219\n"
    "v_cvt_pk_bf16_f32 v37, v38, v39\n"
    "v_exp_f32 v45, v45\n"
    "v_mul_f32 v220, v220, v216\n"
    "v_exp_f32 v46, v46\n"
    "v_mul_f32 v221, v221, v217\n"
    "v_mul_f32 v222, v222, v218\n"
    "v_mfma_f32_16x16x32_bf16 v[108:111], a[140:143], v[120:123], v[108:111]\n"
    "v_cvt_pk_bf16_f32 v38, v44, v45\n"
    "v_mul_f32 v223, v223, v219\n"
    "v_exp_f32 v47, v47\n"
    "v_cvt_pk_bf16_f32 v220, v220, v221\n"
    "v_cvt_pk_bf16_f32 v221, v222, v223\n"
    "v_cvt_pk_bf16_f32 v39, v46, v47\n"
    "v_mfma_f32_16x16x32_bf16 a[192:195], a[208:211], v[4:7], 0\n"
    "ds_read_b64_tr_b16 a[96:97], v244 offset:8192\n"
    "v_max3_f32 v84, v68, v69, v70\n"
    "v_mfma_f32_16x16x32_bf16 a[196:199], a[208:211], v[36:39], 0\n"
    "buffer_store_dwordx2 v[132:133], v252, s[56:59], s87 offen sc1\n"
    "v_max3_f32 v116, v100, v101, v102\n"
    "s_waitcnt lgkmcnt(15)\n"
    "v_mfma_f32_16x16x32_bf16 v[28:31], a[64:67], v[4:7], 0\n"
    "v_max3_f32 v85, v71, v76, v77\n"
    "v_max3_f32 v117, v103, v108, v109\n"
    "v_max3_f32 v84, v84, v78, v79\n"
    "v_mfma_f32_16x16x32_bf16 v[60:63], a[64:67], v[36:39], 0\n"
    "s_add_u32 s87, s87, 44032\n"
    "v_max3_f32 v116, v116, v110, v111\n"
    "v_max_f32 v84, v84, v85\n"
    "v_mfma_f32_16x16x32_bf16 v[92:95], a[68:71], v[4:7], 0\n"
    "ds_read_b64_tr_b16 a[98:99], v244 offset:12288\n"
    "v_max_f32 v116, v116, v117\n"
    "v_mov_b32 v85, v84\n"
    "v_mfma_f32_16x16x32_bf16 v[124:127], a[68:71], v[36:39], 0\n"
    "buffer_store_dwordx2 v[140:141], v252, s[56:59], s88 offen sc1\n"
    "v_mov_b32 v117, v116\n"
    "v_mfma_f32_16x16x32_bf16 v[128:131], a[72:75], v[4:7], 0\n"
    "ds_read_b64_tr_b16 a[100:101], v244 offset:8448\n"
    "v_permlane16_swap_b32 v85, v84\n"
    "v_mfma_f32_16x16x32_bf16 v[136:139], a[72:75], v[36:39], 0\n"
    "v_permlane16_swap_b32 v117, v116\n"
    "v_mfma_f32_16x16x32_bf16 v[144:147], a[76:79], v[4:7], 0\n"
    "v_max_f32 v84, v84, v85\n"
    "v_max_f32 v116, v116, v117\n"
    "v_mov_b32 v85, v84\n"
    "v_mfma_f32_16x16x32_bf16 v[152:155], a[76:79], v[36:39], 0\n"
    "v_mov_b32 v117, v116\n"
    "v_permlane32_swap_b32 v85, v84\n"
    "v_mfma_f32_16x16x32_bf16 v[160:163], a[80:83], v[4:7], 0\n"
    "s_add_u32 s88, s88, 44032\n"
    "v_permlane32_swap_b32 v117, v116\n"
    "v_mfma_f32_16x16x32_bf16 v[168:171], a[80:83], v[36:39], 0\n"
    "v_max_f32 v84, v84, v85\n"
    "v_max_f32 v116, v116, v117\n"
    "v_sub_f32 v68, v68, v84\n"
    "s_waitcnt lgkmcnt(15)\n"
    "v_mfma_f32_16x16x32_bf16 v[176:179], a[84:87], v[4:7], 0\n"
    "v_sub_f32 v100, v100, v116\n"
    "v_sub_f32 v69, v69, v84\n"
    "v_sub_f32 v101, v101, v116\n"
    "v_mfma_f32_16x16x32_bf16 v[184:187], a[84:87], v[36:39], 0\n"
    "v_sub_f32 v70, v70, v84\n"
    "v_sub_f32 v102, v102, v116\n"
    "v_sub_f32 v71, v71, v84\n"
    "s_waitcnt lgkmcnt(13)\n"
    "v_mfma_f32_16x16x32_bf16 v[192:195], a[88:91], v[4:7], 0\n"
    "v_sub_f32 v103, v103, v116\n"
    "v_sub_f32 v76, v76, v84\n"
    "v_mfma_f32_16x16x32_bf16 v[200:203], a[88:91], v[36:39], 0\n"
    "v_sub_f32 v108, v108, v116\n"
    "v_sub_f32 v77, v77, v84\n"
    "v_sub_f32 v109, v109, v116\n"
    "s_waitcnt lgkmcnt(11)\n"
    "v_mfma_f32_16x16x32_bf16 v[208:211], a[92:95], v[4:7], 0\n"
    "v_sub_f32 v78, v78, v84\n"
    "v_sub_f32 v110, v110, v116\n"
    "v_sub_f32 v79, v79, v84\n"
    "v_mfma_f32_16x16x32_bf16 v[216:219], a[92:95], v[36:39], 0\n"
    "v_sub_f32 v111, v111, v116\n"
    "v_sub_f32 v87, 0, v84\n"
    "v_sub_f32 v119, 0, v116\n"
    "s_waitcnt lgkmcnt(10)\n"
    "v_mfma_f32_16x16x32_bf16 v[4:7], a[32:35], v[0:3], v[20:23]\n"
    "v_sub_f32 v86, 0, v84\n"
    "v_sub_f32 v118, 0, v116\n"
    "v_sub_f32 v85, 0, v84\n"
    "s_waitcnt lgkmcnt(9)\n"
    "v_mfma_f32_16x16x32_bf16 v[4:7], a[36:39], v[8:11], v[4:7]\n"
    "v_sub_f32 v117, 0, v116\n"
    "v_sub_f32 v84, 0, v84\n"
    "v_sub_f32 v116, 0, v116\n"
    "s_waitcnt lgkmcnt(8)\n"
    "v_mfma_f32_16x16x32_bf16 v[4:7], a[40:43], v[16:19], v[4:7]\n"
    "ds_read_b64_tr_b16 a[102:103], v244 offset:12544\n"
    "v_exp_f32 v68, v68\n"
    "s_waitcnt lgkmcnt(8)\n"
    "v_mfma_f32_16x16x32_bf16 v[4:7], a[44:47], v[24:27], v[4:7]\n"
    "ds_read_b64_tr_b16 a[104:105], v244 offset:8704\n"
    "v_exp_f32 v69, v69\n"
    "s_waitcnt lgkmcnt(8)\n"
    "v_mfma_f32_16x16x32_bf16 v[12:15], a[48:51], v[0:3], v[20:23]\n"
    "v_exp_f32 v70, v70\n"
    "s_waitcnt lgkmcnt(7)\n"
    "v_mfma_f32_16x16x32_bf16 v[12:15], a[52:55], v[8:11], v[12:15]\n"
    "v_cvt_pk_bf16_f32 v68, v68, v69\n"
    "v_exp_f32 v71, v71\n"
    "s_waitcnt lgkmcnt(6)\n"
    "v_mfma_f32_16x16x32_bf16 v[12:15], a[56:59], v[16:19], v[12:15]\n"
    "v_exp_f32 v76, v76\n"
    "v_cvt_pk_bf16_f32 v69, v70, v71\n"
    "s_waitcnt lgkmcnt(5)\n"
    "v_mfma_f32_16x16x32_bf16 v[12:15], a[60:63], v[24:27], v[12:15]\n"
    "ds_read_b64_tr_b16 a[106:107], v244 offset:12800\n"
    "v_exp_f32 v77, v77\n"
    "v_mfma_f32_16x16x32_bf16 v[36:39], a[32:35], v[32:35], v[52:55]\n"
    "v_exp_f32 v78, v78\n"
    "v_cvt_pk_bf16_f32 v70, v76, v77\n"
    "v_mfma_f32_16x16x32_bf16 v[36:39], a[36:39], v[40:43], v[36:39]\n"
    "v_exp_f32 v79, v79\n"
    "s_nop 0\n"
    "v_cvt_pk_bf16_f32 v71, v78, v79\n"
    "v_mfma_f32_16x16x32_bf16 v[36:39], a[40:43], v[48:51], v[36:39]\n"
    "ds_read_b64_tr_b16 a[108:109], v244 offset:8960\n"
    "v_exp_f32 v100, v100\n"
    "v_mfma_f32_16x16x32_bf16 v[36:39], a[44:47], v[56:59], v[36:39]\n"
    "v_exp_f32 v101, v101\n"
    "v_mfma_f32_16x16x32_bf16 v[44:47], a[48:51], v[32:35], v[52:55]\n"
    "v_exp_f32 v102, v102\n"
    "v_cvt_pk_bf16_f32 v100, v100, v101\n"
    "v_mfma_f32_16x16x32_bf16 v[44:47], a[52:55], v[40:43], v[44:47]\n"
    "buffer_store_dwordx2 v[148:149], v252, s[56:59], s89 offen sc1\n"
    "s_add_u32 s89, s89, 44032\n"
    "v_mfma_f32_16x16x32_bf16 v[44:47], a[56:59], v[48:51], v[44:47]\n"
    "buffer_store_dwordx2 v[156:157], v252, s[56:59], s87 offen sc1\n"
    "v_exp_f32 v103, v103\n"
    "v_exp_f32 v108, v108\n"
    "v_cvt_pk_bf16_f32 v101, v102, v103\n"
    "v_exp_f32 v109, v109\n"
    "v_exp_f32 v110, v110\n"
    "v_cvt_pk_bf16_f32 v102, v108, v109\n"
    "v_exp_f32 v111, v111\n"
    "s_nop 0\n"
    "v_cvt_pk_bf16_f32 v103, v110, v111\n"
    "v_mfma_f32_16x16x32_bf16 v[44:47], a[60:63], v[56:59], v[44:47]\n"
    "s_add_u32 s87, s87, 44032\n"
    "buffer_store_dwordx2 v[164:165], v252, s[56:59], s88 offen sc1\n"
    "v_mfma_f32_16x16x32_bf16 a[200:203], a[208:211], v[68:71], 0\n"
    "s_add_u32 s88, s88, 44032\n"
    "buffer_store_dwordx2 v[172:173], v252, s[56:59], s89 offen sc1\n"
    "v_mfma_f32_16x16x32_bf16 a[204:207], a[208:211], v[100:103], 0\n"
    "s_add_u32 s89, s89, 44032\n"
    "buffer_store_dwordx2 v[180:181], v252, s[56:59], s87 offen sc1\n"
    "v_mfma_f32_16x16x32_bf16 v[224:227], a[64:67], v[68:71], 0\n"
    "s_add_u32 s87, s87, 44032\n"
    "ds_read_b64_tr_b16 a[110:111], v244 offset:13056\n"
    "v_mfma_f32_16x16x32_bf16 v[228:231], a[64:67], v[100:103], 0\n"
    "buffer_store_dwordx2 v[188:189], v252, s[56:59], s88 offen sc1\n"
    "s_add_u32 s88, s88, 44032\n"
    "v_mfma_f32_16x16x32_bf16 v[232:235], a[68:71], v[68:71], 0\n"
    "buffer_store_dwordx2 v[196:197], v252, s[56:59], s89 offen sc1\n"
    "s_add_u32 s89, s89, 44032\n"
    "v_mfma_f32_16x16x32_bf16 v[236:239], a[68:71], v[100:103], 0\n"
    "buffer_store_dwordx2 v[204:205], v252, s[56:59], s87 offen sc1\n"
    "buffer_store_dwordx2 v[212:213], v252, s[56:59], s88 offen sc1\n"
    "buffer_store_dwordx2 v[220:221], v252, s[56:59], s89 offen sc1\n"
    "v_mfma_f32_16x16x32_bf16 v[132:135], a[72:75], v[68:71], 0\n"
    "ds_read_b64_tr_b16 a[112:113], v244 offset:9216\n"
    "ds_read_b64_tr_b16 a[114:115], v244 offset:13312\n"
    "ds_read_b64_tr_b16 a[116:117], v244 offset:9472\n"
    "v_mfma_f32_16x16x32_bf16 v[140:143], a[72:75], v[100:103], 0\n"
    "ds_read_b64_tr_b16 a[118:119], v244 offset:13568\n"
    "ds_read_b64_tr_b16 a[120:121], v244 offset:9728\n"
    "ds_read_b64_tr_b16 a[122:123], v244 offset:13824\n"
    "v_mfma_f32_16x16x32_bf16 v[148:151], a[76:79], v[68:71], 0\n"
    "ds_read_b64_tr_b16 a[124:125], v244 offset:9984\n"
    "v_exp_f32 v4, v4\n"
    "v_mfma_f32_16x16x32_bf16 v[156:159], a[76:79], v[100:103], 0\n"
    "ds_read_b64_tr_b16 a[126:127], v244 offset:14080\n"
    "v_exp_f32 v5, v5\n"
    "v_mfma_f32_16x16x32_bf16 v[164:167], a[80:83], v[68:71], 0\n"
    "v_exp_f32 v6, v6\n"
    "v_mfma_f32_16x16x32_bf16 v[172:175], a[80:83], v[100:103], 0\n"
    "v_cvt_pk_bf16_f32 v4, v4, v5\n"
    "v_exp_f32 v7, v7\n"
    "v_mfma_f32_16x16x32_bf16 v[180:183], a[84:87], v[68:71], 0\n"
    "ds_read_b128 a[0:3], v240 offset:8192\n"
    "v_exp_f32 v12, v12\n"
    "v_mfma_f32_16x16x32_bf16 v[188:191], a[84:87], v[100:103], 0\n"
    "ds_read_b128 a[4:7], v241 offset:8192\n"
    "ds_read_b128 a[8:11], v242 offset:8192\n"
    "v_cvt_pk_bf16_f32 v5, v6, v7\n"
    "v_mfma_f32_16x16x32_bf16 v[196:199], a[88:91], v[68:71], 0\n"
    "ds_read_b128 a[12:15], v243 offset:8192\n"
    "v_exp_f32 v13, v13\n"
    "v_mfma_f32_16x16x32_bf16 v[204:207], a[88:91], v[100:103], 0\n"
    "v_exp_f32 v14, v14\n"
    "v_cvt_pk_bf16_f32 v6, v12, v13\n"
    "v_mfma_f32_16x16x32_bf16 v[212:215], a[92:95], v[68:71], 0\n"
    "v_exp_f32 v15, v15\n"
    "s_nop 0\n"
    "v_cvt_pk_bf16_f32 v7, v14, v15\n"
    "v_mfma_f32_16x16x32_bf16 v[220:223], a[92:95], v[100:103], 0\n"
    "ds_read_b128 a[16:19], v240 offset:12288\n"
    "ds_read_b128 a[20:23], v241 offset:12288\n"
    "v_mfma_f32_16x16x32_bf16 v[68:71], a[32:35], v[64:67], v[84:87]\n"
    "ds_read_b128 a[24:27], v242 offset:12288\n"
    "v_exp_f32 v36, v36\n"
    "v_mfma_f32_16x16x32_bf16 v[68:71], a[36:39], v[72:75], v[68:71]\n"
    "v_exp_f32 v37, v37\n"
    "v_mfma_f32_16x16x32_bf16 v[68:71], a[40:43], v[80:83], v[68:71]\n"
    "v_exp_f32 v38, v38\n"
    "v_cvt_pk_bf16_f32 v36, v36, v37\n"
    "v_mfma_f32_16x16x32_bf16 v[68:71], a[44:47], v[88:91], v[68:71]\n"
    "ds_read_b128 a[28:31], v243 offset:12288\n"
    "v_exp_f32 v39, v39\n"
    "v_mfma_f32_16x16x32_bf16 v[76:79], a[48:51], v[64:67], v[84:87]\n"
    "v_exp_f32 v44, v44\n"
    "v_mfma_f32_16x16x32_bf16 v[76:79], a[52:55], v[72:75], v[76:79]\n"
    "v_cvt_pk_bf16_f32 v37, v38, v39\n"
    "v_mfma_f32_16x16x32_bf16 v[76:79], a[56:59], v[80:83], v[76:79]\n"
    "v_exp_f32 v45, v45\n"
    "v_mfma_f32_16x16x32_bf16 v[76:79], a[60:63], v[88:91], v[76:79]\n"
    "v_mfma_f32_16x16x32_bf16 v[100:103], a[32:35], v[96:99], v[116:119]\n"
    "v_exp_f32 v46, v46\n"
    "v_mfma_f32_16x16x32_bf16 v[100:103], a[36:39], v[104:107], v[100:103]\n"
    "v_cvt_pk_bf16_f32 v38, v44, v45\n"
    "v_mfma_f32_16x16x32_bf16 v[100:103], a[40:43], v[112:115], v[100:103]\n"
    "v_xor_b32 v240, 0x4000, v240\n"
    "v_xor_b32 v241, 0x4000, v241\n"
    "v_xor_b32 v242, 0x4000, v242\n"
    "v_mfma_f32_16x16x32_bf16 v[100:103], a[44:47], v[120:123], v[100:103]\n"
    "v_exp_f32 v47, v47\n"
    "v_xor_b32 v243, 0x4000, v243\n"
    "v_mfma_f32_16x16x32_bf16 v[108:111], a[48:51], v[96:99], v[116:119]\n"
    "v_cvt_pk_bf16_f32 v39, v46, v47\n"
    "v_xor_b32 v244, 0x4000, v244\n"
    "v_mfma_f32_16x16x32_bf16 v[108:111], a[52:55], v[104:107], v[108:111]\n"
    "v_mfma_f32_16x16x32_bf16 v[108:111], a[56:59], v[112:115], v[108:111]\n"
    "v_mfma_f32_16x16x32_bf16 v[108:111], a[60:63], v[120:123], v[108:111]\n"
    "s_waitcnt vmcnt(12)\n"
    "s_waitcnt lgkmcnt(0)\n"
    "s_barrier\n"
    "s_lshl_b32 s63, s62, 12\n"
    "s_lshl_b32 s64, s61, 7\n"
    "s_add_u32 s63, s63, s64\n"
    "s_add_u32 s87, s63, 69632\n"
    "v_mov_b32 v254, s68\n"
    "v_accvgpr_write_b32 a248, v254\n"
    "v_mov_b32 v254, s69\n"
    "v_accvgpr_write_b32 a249, v254\n"
    "s_cmp_eq_u32 s60, 0\n"
    "s_cselect_b32 s88, 32, 0\n"
    "v_mbcnt_lo_u32_b32 v254, -1, 0\n"
    "v_mbcnt_hi_u32_b32 v254, -1, v254\n"
    "v_lshlrev_b32 v252, 4, v254\n"
    "v_min_u32 v254, 6, v254\n"
    "v_lshlrev_b32 v254, 7, v254\n"
    "s_mov_b32 s70, 6\n"
    "LATT:\n"
    "v_mfma_f32_16x16x32_bf16 a[192:195], a[208:211], v[4:7], a[192:195]\n"
    "v_exp_f32 v68, v68\n"
    "v_mfma_f32_16x16x32_bf16 a[196:199], a[208:211], v[36:39], a[196:199]\n"
    "ds_read_b64_tr_b16 a[64:65], v244 offset:0\n"
    "s_mov_b32 m0, s75\n"
    "v_mfma_f32_16x16x32_bf16 v[28:31], a[96:99], v[4:7], v[28:31]\n"
    "s_bitcmp1_b32 s88, s70\n"
    "s_cbranch_scc1 LPH\n"
    "LPH_r:\n"
    "v_mfma_f32_16x16x32_bf16 v[60:63], a[96:99], v[36:39], v[60:63]\n"
    "v_exp_f32 v69, v69\n"
    "v_mfma_f32_16x16x32_bf16 v[92:95], a[100:103], v[4:7], v[92:95]\n"
    "ds_read_b64_tr_b16 a[66:67], v244 offset:4096\n"
    "ds_read_b64_tr_b16 a[68:69], v244 offset:256\n"
    "v_mfma_f32_16x16x32_bf16 v[124:127], a[100:103], v[36:39], v[124:127]\n"
    "buffer_load_dwordx4 v249, s[48:51], s73 offen lds\n"
    "v_mfma_f32_16x16x32_bf16 v[128:131], a[104:107], v[4:7], v[128:131]\n"
    "v_exp_f32 v70, v70\n"
    "v_mfma_f32_16x16x32_bf16 v[136:139], a[104:107], v[36:39], v[136:139]\n"
    "ds_read_b64_tr_b16 a[70:71], v244 offset:4352\n"
    "v_cvt_pk_bf16_f32 v68, v68, v69\n"
    "v_mfma_f32_16x16x32_bf16 v[144:147], a[108:111], v[4:7], v[144:147]\n"
    "v_exp_f32 v71, v71\n"
    "v_mfma_f32_16x16x32_bf16 v[152:155], a[108:111], v[36:39], v[152:155]\n"
    "v_exp_f32 v76, v76\n"
    "v_mfma_f32_16x16x32_bf16 v[160:163], a[112:115], v[4:7], v[160:163]\n"
    "buffer_load_dwordx4 v250, s[48:51], s73 offen offset:1024 lds\n"
    "v_mfma_f32_16x16x32_bf16 v[168:171], a[112:115], v[36:39], v[168:171]\n"
    "ds_read_b64_tr_b16 a[72:73], v244 offset:512\n"
    "v_cvt_pk_bf16_f32 v69, v70, v71\n"
    "v_mfma_f32_16x16x32_bf16 v[176:179], a[116:119], v[4:7], v[176:179]\n"
    "v_exp_f32 v77, v77\n"
    "v_mfma_f32_16x16x32_bf16 v[184:187], a[116:119], v[36:39], v[184:187]\n"
    "v_exp_f32 v78, v78\n"
    "v_mfma_f32_16x16x32_bf16 v[192:195], a[120:123], v[4:7], v[192:195]\n"
    "ds_read_b64_tr_b16 a[74:75], v244 offset:4608\n"
    "v_cvt_pk_bf16_f32 v70, v76, v77\n"
    "v_mfma_f32_16x16x32_bf16 v[200:203], a[120:123], v[36:39], v[200:203]\n"
    "buffer_load_dwordx4 v249, s[48:51], s73 offen offset:2048 lds\n"
    "v_mfma_f32_16x16x32_bf16 v[208:211], a[124:127], v[4:7], v[208:211]\n"
    "v_exp_f32 v79, v79\n"
    "v_mfma_f32_16x16x32_bf16 v[216:219], a[124:127], v[36:39], v[216:219]\n"
    "ds_read_b64_tr_b16 a[76:77], v244 offset:768\n"
    "v_cvt_pk_bf16_f32 v71, v78, v79\n"
    "v_mfma_f32_16x16x32_bf16 v[4:7], a[0:3], v[0:3], v[20:23]\n"
    "v_exp_f32 v100, v100\n"
    "v_mfma_f32_16x16x32_bf16 v[4:7], a[4:7], v[8:11], v[4:7]\n"
    "v_exp_f32 v101, v101\n"
    "v_mfma_f32_16x16x32_bf16 v[4:7], a[8:11], v[16:19], v[4:7]\n"
    "ds_read_b64_tr_b16 a[78:79], v244 offset:4864\n"
    "ds_read_b64_tr_b16 a[80:81], v244 offset:1024\n"
    "v_mfma_f32_16x16x32_bf16 v[4:7], a[12:15], v[24:27], v[4:7]\n"
    "buffer_load_dwordx4 v250, s[48:51], s73 offen offset:3072 lds\n"
    "v_mfma_f32_16x16x32_bf16 v[12:15], a[16:19], v[0:3], v[20:23]\n"
    "v_exp_f32 v102, v102\n"
    "v_mfma_f32_16x16x32_bf16 v[12:15], a[20:23], v[8:11], v[12:15]\n"
    "ds_read_b64_tr_b16 a[82:83], v244 offset:5120\n"
    "v_cvt_pk_bf16_f32 v100, v100, v101\n"
    "v_mfma_f32_16x16x32_bf16 v[12:15], a[24:27], v[16:19], v[12:15]\n"
    "v_exp_f32 v103, v103\n"
    "v_mfma_f32_16x16x32_bf16 v[12:15], a[28:31], v[24:27], v[12:15]\n"
    "v_exp_f32 v108, v108\n"
    "v_mfma_f32_16x16x32_bf16 v[36:39], a[0:3], v[32:35], v[52:55]\n"
    "s_mov_b32 m0, s74\n"
    "v_cvt_pk_bf16_f32 v101, v102, v103\n"
    "v_mfma_f32_16x16x32_bf16 v[36:39], a[4:7], v[40:43], v[36:39]\n"
    "v_exp_f32 v109, v109\n"
    "v_mfma_f32_16x16x32_bf16 v[36:39], a[8:11], v[48:51], v[36:39]\n"
    "ds_read_b64_tr_b16 a[84:85], v244 offset:1280\n"
    "ds_read_b64_tr_b16 a[86:87], v244 offset:5376\n"
    "v_mfma_f32_16x16x32_bf16 v[36:39], a[12:15], v[56:59], v[36:39]\n"
    "buffer_load_dwordx4 v245, s[44:47], s98 offen lds\n"
    "v_mfma_f32_16x16x32_bf16 v[44:47], a[16:19], v[32:35], v[52:55]\n"
    "v_exp_f32 v110, v110\n"
    "v_mfma_f32_16x16x32_bf16 v[44:47], a[20:23], v[40:43], v[44:47]\n"
    "ds_read_b64_tr_b16 a[88:89], v244 offset:1536\n"
    "v_cvt_pk_bf16_f32 v102, v108, v109\n"
    "v_mfma_f32_16x16x32_bf16 v[44:47], a[24:27], v[48:51], v[44:47]\n"
    "v_exp_f32 v111, v111\n"
    "s_nop 0\n"
    "v_cvt_pk_bf16_f32 v103, v110, v111\n"
    "v_mfma_f32_16x16x32_bf16 v[44:47], a[28:31], v[56:59], v[44:47]\n"
    "buffer_load_dwordx4 v246, s[44:47], s98 offen offset:1024 lds\n"
    "v_mfma_f32_16x16x32_bf16 a[200:203], a[208:211], v[68:71], a[200:203]\n"
    "v_exp_f32 v4, v4\n"
    "v_mfma_f32_16x16x32_bf16 a[204:207], a[208:211], v[100:103], a[204:207]\n"
    "ds_read_b64_tr_b16 a[90:91], v244 offset:5632\n"
    "ds_read_b64_tr_b16 a[92:93], v244 offset:1792\n"
    "v_mfma_f32_16x16x32_bf16 v[224:227], a[96:99], v[68:71], v[224:227]\n"
    "v_exp_f32 v5, v5\n"
    "v_mfma_f32_16x16x32_bf16 v[228:231], a[96:99], v[100:103], v[228:231]\n"
    "v_exp_f32 v6, v6\n"
    "v_mfma_f32_16x16x32_bf16 v[232:235], a[100:103], v[68:71], v[232:235]\n"
    "buffer_load_dwordx4 v247, s[44:47], s98 offen offset:2048 lds\n"
    "v_mfma_f32_16x16x32_bf16 v[236:239], a[100:103], v[100:103], v[236:239]\n"
    "ds_read_b64_tr_b16 a[94:95], v244 offset:5888\n"
    "v_cvt_pk_bf16_f32 v4, v4, v5\n"
    "v_mfma_f32_16x16x32_bf16 v[132:135], a[104:107], v[68:71], v[132:135]\n"
    "v_exp_f32 v7, v7\n"
    "v_mfma_f32_16x16x32_bf16 v[140:143], a[104:107], v[100:103], v[140:143]\n"
    "v_exp_f32 v12, v12\n"
    "v_mfma_f32_16x16x32_bf16 v[148:151], a[108:111], v[68:71], v[148:151]\n"
    "ds_read_b128 a[32:35], v240 offset:0\n"
    "v_cvt_pk_bf16_f32 v5, v6, v7\n"
    "v_mfma_f32_16x16x32_bf16 v[156:159], a[108:111], v[100:103], v[156:159]\n"
    "buffer_load_dwordx4 v248, s[44:47], s98 offen offset:3072 lds\n"
    "v_mfma_f32_16x16x32_bf16 v[164:167], a[112:115], v[68:71], v[164:167]\n"
    "v_exp_f32 v13, v13\n"
    "v_mfma_f32_16x16x32_bf16 v[172:175], a[112:115], v[100:103], v[172:175]\n"
    "ds_read_b128 a[36:39], v241 offset:0\n"
    "ds_read_b128 a[40:43], v242 offset:0\n"
    "v_mfma_f32_16x16x32_bf16 v[180:183], a[116:119], v[68:71], v[180:183]\n"
    "v_exp_f32 v14, v14\n"
    "v_mfma_f32_16x16x32_bf16 v[188:191], a[116:119], v[100:103], v[188:191]\n"
    "s_add_u32 s73, s73, 16384\n"
    "v_cvt_pk_bf16_f32 v6, v12, v13\n"
    "v_mfma_f32_16x16x32_bf16 v[196:199], a[120:123], v[68:71], v[196:199]\n"
    "v_exp_f32 v15, v15\n"
    "v_mfma_f32_16x16x32_bf16 v[204:207], a[120:123], v[100:103], v[204:207]\n"
    "ds_read_b128 a[44:47], v243 offset:0\n"
    "v_cvt_pk_bf16_f32 v7, v14, v15\n"
    "v_mfma_f32_16x16x32_bf16 v[212:215], a[124:127], v[68:71], v[212:215]\n"
    "v_exp_f32 v36, v36\n"
    "v_mfma_f32_16x16x32_bf16 v[220:223], a[124:127], v[100:103], v[220:223]\n"
    "s_and_b32 s73, s73, 0x1ffff\n"
    "ds_read_b128 a[48:51], v240 offset:4096\n"
    "v_mfma_f32_16x16x32_bf16 v[68:71], a[0:3], v[64:67], v[84:87]\n"
    "v_exp_f32 v37, v37\n"
    "v_mfma_f32_16x16x32_bf16 v[68:71], a[4:7], v[72:75], v[68:71]\n"
    "v_exp_f32 v38, v38\n"
    "v_mfma_f32_16x16x32_bf16 v[68:71], a[8:11], v[80:83], v[68:71]\n"
    "s_add_u32 s98, s98, 16384\n"
    "v_cvt_pk_bf16_f32 v36, v36, v37\n"
    "v_mfma_f32_16x16x32_bf16 v[68:71], a[12:15], v[88:91], v[68:71]\n"
    "ds_read_b128 a[52:55], v241 offset:4096\n"
    "s_and_b32 s98, s98, 0x1ffff\n"
    "v_mfma_f32_16x16x32_bf16 v[76:79], a[16:19], v[64:67], v[84:87]\n"
    "v_exp_f32 v39, v39\n"
    "v_mfma_f32_16x16x32_bf16 v[76:79], a[20:23], v[72:75], v[76:79]\n"
    "v_exp_f32 v44, v44\n"
    "v_mfma_f32_16x16x32_bf16 v[76:79], a[24:27], v[80:83], v[76:79]\n"
    "ds_read_b128 a[56:59], v242 offset:4096\n"
    "v_cvt_pk_bf16_f32 v37, v38, v39\n"
    "v_mfma_f32_16x16x32_bf16 v[76:79], a[28:31], v[88:91], v[76:79]\n"
    "s_xor_b32 s74, s74, 0x4000\n"
    "ds_read_b128 a[60:63], v243 offset:4096\n"
    "v_mfma_f32_16x16x32_bf16 v[100:103], a[0:3], v[96:99], v[116:119]\n"
    "v_exp_f32 v45, v45\n"
    "v_mfma_f32_16x16x32_bf16 v[100:103], a[4:7], v[104:107], v[100:103]\n"
    "v_exp_f32 v46, v46\n"
    "v_mfma_f32_16x16x32_bf16 v[100:103], a[8:11], v[112:115], v[100:103]\n"
    "s_xor_b32 s75, s75, 0x4000\n"
    "v_cvt_pk_bf16_f32 v38, v44, v45\n"
    "v_mfma_f32_16x16x32_bf16 v[100:103], a[12:15], v[120:123], v[100:103]\n"
    "v_exp_f32 v47, v47\n"
    "v_mfma_f32_16x16x32_bf16 v[108:111], a[16:19], v[96:99], v[116:119]\n"
    "s_lshl_b32 s66, s99, 14\n"
    "v_cvt_pk_bf16_f32 v39, v46, v47\n"
    "v_mfma_f32_16x16x32_bf16 v[108:111], a[20:23], v[104:107], v[108:111]\n"
    "v_mfma_f32_16x16x32_bf16 v[108:111], a[24:27], v[112:115], v[108:111]\n"
    "s_waitcnt lgkmcnt(0)\n"
    "s_add_u32 m0, s67, s66\n"
    "v_mfma_f32_16x16x32_bf16 v[108:111], a[28:31], v[120:123], v[108:111]\n"
    "v_mfma_f32_16x16x32_bf16 a[192:195], a[208:211], v[4:7], a[192:195]\n"
    "v_exp_f32 v68, v68\n"
    "v_mfma_f32_16x16x32_bf16 a[196:199], a[208:211], v[36:39], a[196:199]\n"
    "ds_read_b64_tr_b16 a[96:97], v244 offset:8192\n"
    "ds_read_b64_tr_b16 a[98:99], v244 offset:12288\n"
    "v_mfma_f32_16x16x32_bf16 v[28:31], a[64:67], v[4:7], v[28:31]\n"
    "v_exp_f32 v69, v69\n"
    "v_mfma_f32_16x16x32_bf16 v[60:63], a[64:67], v[36:39], v[60:63]\n"
    "s_lshl_b32 s66, s99, 17\n"
    "ds_read_b64_tr_b16 a[100:101], v244 offset:8448\n"
    "v_mfma_f32_16x16x32_bf16 v[92:95], a[68:71], v[4:7], v[92:95]\n"
    "v_exp_f32 v70, v70\n"
    "v_mfma_f32_16x16x32_bf16 v[124:127], a[68:71], v[36:39], v[124:127]\n"
    "ds_read_b64_tr_b16 a[102:103], v244 offset:12544\n"
    "v_cvt_pk_bf16_f32 v68, v68, v69\n"
    "v_mfma_f32_16x16x32_bf16 v[128:131], a[72:75], v[4:7], v[128:131]\n"
    "buffer_load_dwordx4 v251, s[36:39], s66 offen lds\n"
    "v_mfma_f32_16x16x32_bf16 v[136:139], a[72:75], v[36:39], v[136:139]\n"
    "v_exp_f32 v71, v71\n"
    "v_mfma_f32_16x16x32_bf16 v[144:147], a[76:79], v[4:7], v[144:147]\n"
    "v_exp_f32 v76, v76\n"
    "v_mfma_f32_16x16x32_bf16 v[152:155], a[76:79], v[36:39], v[152:155]\n"
    "ds_read_b64_tr_b16 a[104:105], v244 offset:8704\n"
    "v_cvt_pk_bf16_f32 v69, v70, v71\n"
    "v_mfma_f32_16x16x32_bf16 v[160:163], a[80:83], v[4:7], v[160:163]\n"
    "v_exp_f32 v77, v77\n"
    "v_mfma_f32_16x16x32_bf16 v[168:171], a[80:83], v[36:39], v[168:171]\n"
    "s_add_u32 s66, s66, 7168\n"
    "ds_read_b64_tr_b16 a[106:107], v244 offset:12800\n"
    "v_mfma_f32_16x16x32_bf16 v[176:179], a[84:87], v[4:7], v[176:179]\n"
    "v_exp_f32 v78, v78\n"
    "v_mfma_f32_16x16x32_bf16 v[184:187], a[84:87], v[36:39], v[184:187]\n"
    "ds_read_b64_tr_b16 a[108:109], v244 offset:8960\n"
    "v_cvt_pk_bf16_f32 v70, v76, v77\n"
    "v_mfma_f32_16x16x32_bf16 v[192:195], a[88:91], v[4:7], v[192:195]\n"
    "buffer_load_dwordx4 v251, s[36:39], s66 offen offset:1024 lds\n"
    "v_mfma_f32_16x16x32_bf16 v[200:203], a[88:91], v[36:39], v[200:203]\n"
    "v_exp_f32 v79, v79\n"
    "v_mfma_f32_16x16x32_bf16 v[208:211], a[92:95], v[4:7], v[208:211]\n"
    "ds_read_b64_tr_b16 a[110:111], v244 offset:13056\n"
    "v_cvt_pk_bf16_f32 v71, v78, v79\n"
    "v_mfma_f32_16x16x32_bf16 v[216:219], a[92:95], v[36:39], v[216:219]\n"
    "v_exp_f32 v100, v100\n"
    "v_mfma_f32_16x16x32_bf16 v[4:7], a[32:35], v[0:3], v[20:23]\n"
    "v_exp_f32 v101, v101\n"
    "v_mfma_f32_16x16x32_bf16 v[4:7], a[36:39], v[8:11], v[4:7]\n"
    "s_add_u32 s66, s66, 7168\n"
    "ds_read_b64_tr_b16 a[112:113], v244 offset:9216\n"
    "v_mfma_f32_16x16x32_bf16 v[4:7], a[40:43], v[16:19], v[4:7]\n"
    "v_exp_f32 v102, v102\n"
    "v_mfma_f32_16x16x32_bf16 v[4:7], a[44:47], v[24:27], v[4:7]\n"
    "ds_read_b64_tr_b16 a[114:115], v244 offset:13312\n"
    "v_cvt_pk_bf16_f32 v100, v100, v101\n"
    "v_mfma_f32_16x16x32_bf16 v[12:15], a[48:51], v[0:3], v[20:23]\n"
    "buffer_load_dwordx4 v251, s[36:39], s66 offen offset:2048 lds\n"
    "v_mfma_f32_16x16x32_bf16 v[12:15], a[52:55], v[8:11], v[12:15]\n"
    "v_exp_f32 v103, v103\n"
    "v_mfma_f32_16x16x32_bf16 v[12:15], a[56:59], v[16:19], v[12:15]\n"
    "v_exp_f32 v108, v108\n"
    "v_mfma_f32_16x16x32_bf16 v[12:15], a[60:63], v[24:27], v[12:15]\n"
    "ds_read_b64_tr_b16 a[116:117], v244 offset:9472\n"
    "v_cvt_pk_bf16_f32 v101, v102, v103\n"
    "v_mfma_f32_16x16x32_bf16 v[36:39], a[32:35], v[32:35], v[52:55]\n"
    "v_exp_f32 v109, v109\n"
    "v_mfma_f32_16x16x32_bf16 v[36:39], a[36:39], v[40:43], v[36:39]\n"
    "s_add_u32 s66, s66, 7168\n"
    "ds_read_b64_tr_b16 a[118:119], v244 offset:13568\n"
    "v_mfma_f32_16x16x32_bf16 v[36:39], a[40:43], v[48:51], v[36:39]\n"
    "v_exp_f32 v110, v110\n"
    "v_mfma_f32_16x16x32_bf16 v[36:39], a[44:47], v[56:59], v[36:39]\n"
    "ds_read_b64_tr_b16 a[120:121], v244 offset:9728\n"
    "v_cvt_pk_bf16_f32 v102, v108, v109\n"
    "v_mfma_f32_16x16x32_bf16 v[44:47], a[48:51], v[32:35], v[52:55]\n"
    "buffer_load_dwordx4 v251, s[36:39], s66 offen offset:3072 lds\n"
    "v_mfma_f32_16x16x32_bf16 v[44:47], a[52:55], v[40:43], v[44:47]\n"
    "v_exp_f32 v111, v111\n"
    "v_mfma_f32_16x16x32_bf16 v[44:47], a[56:59], v[48:51], v[44:47]\n"
    "ds_read_b64_tr_b16 a[122:123], v244 offset:13824\n"
    "v_cvt_pk_bf16_f32 v103, v110, v111\n"
    "v_mfma_f32_16x16x32_bf16 v[44:47], a[60:63], v[56:59], v[44:47]\n"
    "s_add_u32 s99, s99, 1\n"
    "v_mfma_f32_16x16x32_bf16 a[200:203], a[208:211], v[68:71], a[200:203]\n"
    "v_exp_f32 v4, v4\n"
    "v_mfma_f32_16x16x32_bf16 a[204:207], a[208:211], v[100:103], a[204:207]\n"
    "v_exp_f32 v5, v5\n"
    "v_mfma_f32_16x16x32_bf16 v[224:227], a[64:67], v[68:71], v[224:227]\n"
    "v_exp_f32 v6, v6\n"
    "v_mfma_f32_16x16x32_bf16 v[228:231], a[64:67], v[100:103], v[228:231]\n"
    "ds_read_b64_tr_b16 a[124:125], v244 offset:9984\n"
    "v_cvt_pk_bf16_f32 v4, v4, v5\n"
    "v_mfma_f32_16x16x32_bf16 v[232:235], a[68:71], v[68:71], v[232:235]\n"
    "s_cmp_eq_u32 s99, 6\n"
    "s_cselect_b32 s99, 0, s99\n"
    "v_mfma_f32_16x16x32_bf16 v[236:239], a[68:71], v[100:103], v[236:239]\n"
    "v_exp_f32 v7, v7\n"
    "v_mfma_f32_16x16x32_bf16 v[132:135], a[72:75], v[68:71], v[132:135]\n"
    "ds_read_b64_tr_b16 a[126:127], v244 offset:14080\n"
    "ds_read_b128 a[0:3], v240 offset:8192\n"
    "v_mfma_f32_16x16x32_bf16 v[140:143], a[72:75], v[100:103], v[140:143]\n"
    "v_exp_f32 v12, v12\n"
    "v_mfma_f32_16x16x32_bf16 v[148:151], a[76:79], v[68:71], v[148:151]\n"
    "v_cvt_pk_bf16_f32 v5, v6, v7\n"
    "v_mfma_f32_16x16x32_bf16 v[156:159], a[76:79], v[100:103], v[156:159]\n"
    "s_waitcnt vmcnt(13)\n"
    "buffer_load_dword a212, v254, s[12:15], s93 offen\n"
    "v_mfma_f32_16x16x32_bf16 v[164:167], a[80:83], v[68:71], v[164:167]\n"
    "v_exp_f32 v13, v13\n"
    "v_mfma_f32_16x16x32_bf16 v[172:175], a[80:83], v[100:103], v[172:175]\n"
    "ds_read_b128 a[4:7], v241 offset:8192\n"
    "ds_read_b128 a[8:11], v242 offset:8192\n"
    "v_mfma_f32_16x16x32_bf16 v[180:183], a[84:87], v[68:71], v[180:183]\n"
    "v_exp_f32 v14, v14\n"
    "v_mfma_f32_16x16x32_bf16 v[188:191], a[84:87], v[100:103], v[188:191]\n"
    "s_add_u32 s93, s93, 896\n"
    "v_cvt_pk_bf16_f32 v6, v12, v13\n"
    "v_mfma_f32_16x16x32_bf16 v[196:199], a[88:91], v[68:71], v[196:199]\n"
    "v_exp_f32 v15, v15\n"
    "v_mfma_f32_16x16x32_bf16 v[204:207], a[88:91], v[100:103], v[204:207]\n"
    "ds_read_b128 a[12:15], v243 offset:8192\n"
    "v_cvt_pk_bf16_f32 v7, v14, v15\n"
    "v_mfma_f32_16x16x32_bf16 v[212:215], a[92:95], v[68:71], v[212:215]\n"
    "v_exp_f32 v36, v36\n"
    "v_mfma_f32_16x16x32_bf16 v[220:223], a[92:95], v[100:103], v[220:223]\n"
    "s_waitcnt vmcnt(13)\n"
    "buffer_load_dword a213, v254, s[20:23], s94 offen\n"
    "v_mfma_f32_16x16x32_bf16 v[68:71], a[32:35], v[64:67], v[84:87]\n"
    "v_exp_f32 v37, v37\n"
    "v_mfma_f32_16x16x32_bf16 v[68:71], a[36:39], v[72:75], v[68:71]\n"
    "ds_read_b128 a[16:19], v240 offset:12288\n"
    "ds_read_b128 a[20:23], v241 offset:12288\n"
    "v_mfma_f32_16x16x32_bf16 v[68:71], a[40:43], v[80:83], v[68:71]\n"
    "v_exp_f32 v38, v38\n"
    "v_mfma_f32_16x16x32_bf16 v[68:71], a[44:47], v[88:91], v[68:71]\n"
    "s_add_u32 s94, s94, 128\n"
    "v_cvt_pk_bf16_f32 v36, v36, v37\n"
    "v_mfma_f32_16x16x32_bf16 v[76:79], a[48:51], v[64:67], v[84:87]\n"
    "v_exp_f32 v39, v39\n"
    "v_mfma_f32_16x16x32_bf16 v[76:79], a[52:55], v[72:75], v[76:79]\n"
    "v_exp_f32 v44, v44\n"
    "v_mfma_f32_16x16x32_bf16 v[76:79], a[56:59], v[80:83], v[76:79]\n"
    "ds_read_b128 a[24:27], v242 offset:12288\n"
    "v_cvt_pk_bf16_f32 v37, v38, v39\n"
    "v_mfma_f32_16x16x32_bf16 v[76:79], a[60:63], v[88:91], v[76:79]\n"
    "v_exp_f32 v45, v45\n"
    "v_mfma_f32_16x16x32_bf16 v[100:103], a[32:35], v[96:99], v[116:119]\n"
    "ds_read_b128 a[28:31], v243 offset:12288\n"
    "v_mfma_f32_16x16x32_bf16 v[100:103], a[36:39], v[104:107], v[100:103]\n"
    "v_exp_f32 v46, v46\n"
    "v_mfma_f32_16x16x32_bf16 v[100:103], a[40:43], v[112:115], v[100:103]\n"
    "v_xor_b32 v240, 0x4000, v240\n"
    "v_xor_b32 v241, 0x4000, v241\n"
    "v_mfma_f32_16x16x32_bf16 v[100:103], a[44:47], v[120:123], v[100:103]\n"
    "v_cvt_pk_bf16_f32 v38, v44, v45\n"
    "v_xor_b32 v242, 0x4000, v242\n"
    "v_mfma_f32_16x16x32_bf16 v[108:111], a[48:51], v[96:99], v[116:119]\n"
    "v_exp_f32 v47, v47\n"
    "v_mfma_f32_16x16x32_bf16 v[108:111], a[52:55], v[104:107], v[108:111]\n"
    "v_xor_b32 v243, 0x4000, v243\n"
    "v_cvt_pk_bf16_f32 v39, v46, v47\n"
    "v_mfma_f32_16x16x32_bf16 v[108:111], a[56:59], v[112:115], v[108:111]\n"
    "v_xor_b32 v244, 0x4000, v244\n"
    "v_mfma_f32_16x16x32_bf16 v[108:111], a[60:63], v[120:123], v[108:111]\n"
    "s_waitcnt vmcnt(6)\n"
    "s_waitcnt lgkmcnt(0)\n"
    "s_barrier\n"
    "s_sub_u32 s70, s70, 1\n"
    "s_cmp_lg_u32 s70, 0\n"
    "s_cbranch_scc1 LATT\n"
    "v_mfma_f32_16x16x32_bf16 a[192:195], a[208:211], v[4:7], a[192:195]\n"
    "v_exp_f32 v68, v68\n"
    "v_mfma_f32_16x16x32_bf16 a[196:199], a[208:211], v[36:39], a[196:199]\n"
    "ds_read_b64_tr_b16 a[64:65], v244 offset:0\n"
    "v_mbcnt_lo_u32_b32 v249, -1, 0\n"
    "v_mfma_f32_16x16x32_bf16 v[28:31], a[96:99], v[4:7], v[28:31]\n"
    "v_mbcnt_hi_u32_b32 v249, -1, v249\n"
    "v_exp_f32 v69, v69\n"
    "v_mfma_f32_16x16x32_bf16 v[60:63], a[96:99], v[36:39], v[60:63]\n"
    "s_lshl_b32 s63, s62, 12\n"
    "ds_read_b64_tr_b16 a[66:67], v244 offset:4096\n"
    "v_mfma_f32_16x16x32_bf16 v[92:95], a[100:103], v[4:7], v[92:95]\n"
    "s_add_u32 s63, s63, 69632\n"
    "v_exp_f32 v70, v70\n"
    "v_mfma_f32_16x16x32_bf16 v[124:127], a[100:103], v[36:39], v[124:127]\n"
    "v_cvt_pk_bf16_f32 v68, v68, v69\n"
    "v_and_b32 v250, 31, v249\n"
    "v_mfma_f32_16x16x32_bf16 v[128:131], a[104:107], v[4:7], v[128:131]\n"
    "ds_read_b64_tr_b16 a[68:69], v244 offset:256\n"
    "v_exp_f32 v71, v71\n"
    "v_mfma_f32_16x16x32_bf16 v[136:139], a[104:107], v[36:39], v[136:139]\n"
    "v_lshlrev_b32 v250, 7, v250\n"
    "v_add_u32 v250, s63, v250\n"
    "v_mfma_f32_16x16x32_bf16 v[144:147], a[108:111], v[4:7], v[144:147]\n"
    "ds_read_b64_tr_b16 a[70:71], v244 offset:4352\n"
    "v_exp_f32 v76, v76\n"
    "v_mfma_f32_16x16x32_bf16 v[152:155], a[108:111], v[36:39], v[152:155]\n"
    "buffer_load_dwordx2 v[252:253], v250, s[56:59], 0 offen sc1\n"
    "v_mfma_f32_16x16x32_bf16 v[160:163], a[112:115], v[4:7], v[160:163]\n"
    "v_cvt_pk_bf16_f32 v69, v70, v71\n"
    "v_exp_f32 v77, v77\n"
    "v_mfma_f32_16x16x32_bf16 v[168:171], a[112:115], v[36:39], v[168:171]\n"
    "ds_read_b64_tr_b16 a[72:73], v244 offset:512\n"
    "v_lshlrev_b32 v254, 4, v249\n"
    "v_mfma_f32_16x16x32_bf16 v[176:179], a[116:119], v[4:7], v[176:179]\n"
    "v_exp_f32 v78, v78\n"
    "v_mfma_f32_16x16x32_bf16 v[184:187], a[116:119], v[36:39], v[184:187]\n"
    "s_lshl_b32 s78, s62, 21\n"
    "ds_read_b64_tr_b16 a[74:75], v244 offset:4608\n"
    "v_cvt_pk_bf16_f32 v70, v76, v77\n"
    "v_mfma_f32_16x16x32_bf16 v[192:195], a[120:123], v[4:7], v[192:195]\n"
    "s_lshl_b32 s64, s60, 19\n"
    "s_add_u32 s78, s78, s64\n"
    "v_mfma_f32_16x16x32_bf16 v[200:203], a[120:123], v[36:39], v[200:203]\n"
    "v_exp_f32 v79, v79\n"
    "s_nop 0\n"
    "v_cvt_pk_bf16_f32 v71, v78, v79\n"
    "v_mfma_f32_16x16x32_bf16 v[208:211], a[124:127], v[4:7], v[208:211]\n"
    "ds_read_b64_tr_b16 a[76:77], v244 offset:768\n"
    "s_lshl_b32 s64, s61, 12\n"
    "v_mfma_f32_16x16x32_bf16 v[216:219], a[124:127], v[36:39], v[216:219]\n"
    "s_add_u32 s78, s78, s64\n"
    "v_exp_f32 v100, v100\n"
    "v_mfma_f32_16x16x32_bf16 v[4:7], a[0:3], v[0:3], v[20:23]\n"
    "v_exp_f32 v101, v101\n"
    "v_mfma_f32_16x16x32_bf16 v[4:7], a[4:7], v[8:11], v[4:7]\n"
    "ds_read_b64_tr_b16 a[78:79], v244 offset:4864\n"
    "s_add_u32 s78, s78, 413696\n"
    "s_lshl_b32 s63, s62, 4\n"
    "v_mfma_f32_16x16x32_bf16 v[4:7], a[8:11], v[16:19], v[4:7]\n"
    "v_exp_f32 v102, v102\n"
    "v_mfma_f32_16x16x32_bf16 v[4:7], a[12:15], v[24:27], v[4:7]\n"
    "ds_read_b64_tr_b16 a[80:81], v244 offset:1024\n"
    "s_nop 0\n"
    "v_cvt_pk_bf16_f32 v100, v100, v101\n"
    "v_mfma_f32_16x16x32_bf16 v[12:15], a[16:19], v[0:3], v[20:23]\n"
    "v_exp_f32 v103, v103\n"
    "v_mfma_f32_16x16x32_bf16 v[12:15], a[20:23], v[8:11], v[12:15]\n"
    "s_lshl_b32 s64, s79, 2\n"
    "ds_read_b64_tr_b16 a[82:83], v244 offset:5120\n"
    "v_mfma_f32_16x16x32_bf16 v[12:15], a[24:27], v[16:19], v[12:15]\n"
    "s_add_u32 s63, s63, s64\n"
    "v_exp_f32 v108, v108\n"
    "v_mfma_f32_16x16x32_bf16 v[12:15], a[28:31], v[24:27], v[12:15]\n"
    "s_add_u32 s86, s63, s60\n"
    "v_cvt_pk_bf16_f32 v101, v102, v103\n"
    "v_mfma_f32_16x16x32_bf16 v[36:39], a[0:3], v[32:35], v[52:55]\n"
    "s_mul_i32 s63, s86, 44032\n"
    "v_exp_f32 v109, v109\n"
    "v_mfma_f32_16x16x32_bf16 v[36:39], a[4:7], v[40:43], v[36:39]\n"
    "ds_read_b64_tr_b16 a[84:85], v244 offset:1280\n"
    "s_add_u32 s63, s63, 17190912\n"
    "v_mfma_f32_16x16x32_bf16 v[36:39], a[8:11], v[48:51], v[36:39]\n"
    "ds_read_b64_tr_b16 a[86:87], v244 offset:5376\n"
    "v_exp_f32 v110, v110\n"
    "v_mfma_f32_16x16x32_bf16 v[36:39], a[12:15], v[56:59], v[36:39]\n"
    "s_add_u32 s40, s26, s63\n"
    "s_addc_u32 s41, s27, 0\n"
    "v_mfma_f32_16x16x32_bf16 v[44:47], a[16:19], v[32:35], v[52:55]\n"
    "v_cvt_pk_bf16_f32 v102, v108, v109\n"
    "v_exp_f32 v111, v111\n"
    "v_mfma_f32_16x16x32_bf16 v[44:47], a[20:23], v[40:43], v[44:47]\n"
    "s_and_b32 s41, s41, 0xffff\n"
    "ds_read_b64_tr_b16 a[88:89], v244 offset:1536\n"
    "v_mfma_f32_16x16x32_bf16 v[44:47], a[24:27], v[48:51], v[44:47]\n"
    "s_sub_u32 s42, 0, s63\n"
    "s_mov_b32 s43, 0x20000\n"
    "v_cvt_pk_bf16_f32 v103, v110, v111\n"
    "v_mfma_f32_16x16x32_bf16 v[44:47], a[28:31], v[56:59], v[44:47]\n"
    "ds_read_b64_tr_b16 a[90:91], v244 offset:5632\n"
    "s_mov_b32 s42, 44032\n"
    "v_mfma_f32_16x16x32_bf16 a[200:203], a[208:211], v[68:71], a[200:203]\n"
    "v_exp_f32 v4, v4\n"
    "v_mfma_f32_16x16x32_bf16 a[204:207], a[208:211], v[100:103], a[204:207]\n"
    "s_mov_b32 s95, 0\n"
    "v_exp_f32 v5, v5\n"
    "v_mfma_f32_16x16x32_bf16 v[224:227], a[96:99], v[68:71], v[224:227]\n"
    "v_exp_f32 v6, v6\n"
    "v_mfma_f32_16x16x32_bf16 v[228:231], a[96:99], v[100:103], v[228:231]\n"
    "ds_read_b64_tr_b16 a[92:93], v244 offset:1792\n"
    "s_lshl_b32 s63, s81, 19\n"
    "v_cvt_pk_bf16_f32 v4, v4, v5\n"
    "v_mfma_f32_16x16x32_bf16 v[232:235], a[100:103], v[68:71], v[232:235]\n"
    "s_lshl_b32 s64, s60, 15\n"
    "ds_read_b64_tr_b16 a[94:95], v244 offset:5888\n"
    "v_mfma_f32_16x16x32_bf16 v[236:239], a[100:103], v[100:103], v[236:239]\n"
    "s_add_u32 s63, s63, s64\n"
    "v_exp_f32 v7, v7\n"
    "v_mfma_f32_16x16x32_bf16 v[132:135], a[104:107], v[68:71], v[132:135]\n"
    "v_exp_f32 v12, v12\n"
    "v_mfma_f32_16x16x32_bf16 v[140:143], a[104:107], v[100:103], v[140:143]\n"
    "s_lshl_b32 s64, s80, 8\n"
    "ds_read_b128 a[32:35], v240 offset:0\n"
    "v_cvt_pk_bf16_f32 v5, v6, v7\n"
    "v_mfma_f32_16x16x32_bf16 v[148:151], a[108:111], v[68:71], v[148:151]\n"
    "s_add_u32 s63, s63, s64\n"
    "v_mfma_f32_16x16x32_bf16 v[156:159], a[108:111], v[100:103], v[156:159]\n"
    "ds_read_b128 a[36:39], v241 offset:0\n"
    "v_exp_f32 v13, v13\n"
    "v_mfma_f32_16x16x32_bf16 v[164:167], a[112:115], v[68:71], v[164:167]\n"
    "s_add_u32 s52, s16, s63\n"
    "s_addc_u32 s53, s17, 0\n"
    "s_sub_u32 s52, s52, 3072\n"
    "s_subb_u32 s53, s53, 0\n"
    "v_mfma_f32_16x16x32_bf16 v[172:175], a[112:115], v[100:103], v[172:175]\n"
    "v_exp_f32 v14, v14\n"
    "v_mfma_f32_16x16x32_bf16 v[180:183], a[116:119], v[68:71], v[180:183]\n"
    "ds_read_b128 a[40:43], v242 offset:0\n"
    "v_cvt_pk_bf16_f32 v6, v12, v13\n"
    "v_mfma_f32_16x16x32_bf16 v[188:191], a[116:119], v[100:103], v[188:191]\n"
    "v_exp_f32 v15, v15\n"
    "v_mfma_f32_16x16x32_bf16 v[196:199], a[120:123], v[68:71], v[196:199]\n"
    "s_and_b32 s53, s53, 0xffff\n"
    "ds_read_b128 a[44:47], v243 offset:0\n"
    "v_cvt_pk_bf16_f32 v7, v14, v15\n"
    "v_mfma_f32_16x16x32_bf16 v[204:207], a[120:123], v[100:103], v[204:207]\n"
    "v_exp_f32 v36, v36\n"
    "v_mfma_f32_16x16x32_bf16 v[212:215], a[124:127], v[68:71], v[212:215]\n"
    "s_sub_u32 s54, 4197376, s63\n"
    "s_mov_b32 s55, 0x20000\n"
    "v_mfma_f32_16x16x32_bf16 v[220:223], a[124:127], v[100:103], v[220:223]\n"
    "v_exp_f32 v37, v37\n"
    "v_mfma_f32_16x16x32_bf16 v[68:71], a[0:3], v[64:67], v[84:87]\n"
    "ds_read_b128 a[48:51], v240 offset:4096\n"
    "s_waitcnt vmcnt(0)\n"
    "v_cmp_ne_u64 vcc, s[68:69], v[252:253]\n"
    "s_nop 0\n"
    "s_cbranch_vccnz LSPH\n"
    "LSPH_r:\n"
    "v_mfma_f32_16x16x32_bf16 v[68:71], a[4:7], v[72:75], v[68:71]\n"
    "v_exp_f32 v38, v38\n"
    "v_mfma_f32_16x16x32_bf16 v[68:71], a[8:11], v[80:83], v[68:71]\n"
    "buffer_load_dwordx4 a[128:131], v254, s[40:43], s95 offen offset:0 sc1\n"
    "v_cvt_pk_bf16_f32 v36, v36, v37\n"
    "v_mfma_f32_16x16x32_bf16 v[68:71], a[12:15], v[88:91], v[68:71]\n"
    "ds_read_b128 a[52:55], v241 offset:4096\n"
    "v_mfma_f32_16x16x32_bf16 v[76:79], a[16:19], v[64:67], v[84:87]\n"
    "v_exp_f32 v39, v39\n"
    "v_mfma_f32_16x16x32_bf16 v[76:79], a[20:23], v[72:75], v[76:79]\n"
    "ds_read_b128 a[56:59], v242 offset:4096\n"
    "v_exp_f32 v44, v44\n"
    "v_mfma_f32_16x16x32_bf16 v[76:79], a[24:27], v[80:83], v[76:79]\n"
    "buffer_load_dwordx4 a[132:135], v254, s[40:43], s95 offen offset:1024 sc1\n"
    "v_mfma_f32_16x16x32_bf16 v[76:79], a[28:31], v[88:91], v[76:79]\n"
    "v_cvt_pk_bf16_f32 v37, v38, v39\n"
    "v_exp_f32 v45, v45\n"
    "v_mfma_f32_16x16x32_bf16 v[100:103], a[0:3], v[96:99], v[116:119]\n"
    "buffer_load_dwordx4 a[136:139], v254, s[40:43], s95 offen offset:2048 sc1\n"
    "v_mfma_f32_16x16x32_bf16 v[100:103], a[4:7], v[104:107], v[100:103]\n"
    "ds_read_b128 a[60:63], v243 offset:4096\n"
    "v_exp_f32 v46, v46\n"
    "v_mfma_f32_16x16x32_bf16 v[100:103], a[8:11], v[112:115], v[100:103]\n"
    "buffer_load_dwordx4 a[140:143], v254, s[40:43], s95 offen offset:3072 sc1\n"
    "v_mfma_f32_16x16x32_bf16 v[100:103], a[12:15], v[120:123], v[100:103]\n"
    "v_cvt_pk_bf16_f32 v38, v44, v45\n"
    "v_exp_f32 v47, v47\n"
    "v_mfma_f32_16x16x32_bf16 v[108:111], a[16:19], v[96:99], v[116:119]\n"
    "s_add_u32 s95, s95, 4096\n"
    "v_cvt_pk_bf16_f32 v39, v46, v47\n"
    "v_mfma_f32_16x16x32_bf16 v[108:111], a[20:23], v[104:107], v[108:111]\n"
    "buffer_load_dwordx4 a[144:147], v254, s[40:43], s95 offen offset:0 sc1\n"
    "v_mfma_f32_16x16x32_bf16 v[108:111], a[24:27], v[112:115], v[108:111]\n"
    "s_waitcnt lgkmcnt(0)\n"
    "v_mfma_f32_16x16x32_bf16 v[108:111], a[28:31], v[120:123], v[108:111]\n"
    "buffer_load_dwordx4 a[148:151], v254, s[40:43], s95 offen offset:1024 sc1\n"
    "v_mfma_f32_16x16x32_bf16 a[192:195], a[208:211], v[4:7], a[192:195]\n"
    "v_exp_f32 v68, v68\n"
    "v_mfma_f32_16x16x32_bf16 a[196:199], a[208:211], v[36:39], a[196:199]\n"
    "ds_read_b64_tr_b16 a[96:97], v244 offset:8192\n"
    "ds_read_b64_tr_b16 a[98:99], v244 offset:12288\n"
    "v_mfma_f32_16x16x32_bf16 v[28:31], a[64:67], v[4:7], v[28:31]\n"
    "v_exp_f32 v69, v69\n"
    "v_mov_b32 v240, v254\n"
    "v_mfma_f32_16x16x32_bf16 v[60:63], a[64:67], v[36:39], v[60:63]\n"
    "v_exp_f32 v70, v70\n"
    "v_mfma_f32_16x16x32_bf16 v[92:95], a[68:71], v[4:7], v[92:95]\n"
    "buffer_load_dwordx4 a[152:155], v254, s[40:43], s95 offen offset:2048 sc1\n"
    "v_cvt_pk_bf16_f32 v68, v68, v69\n"
    "v_mfma_f32_16x16x32_bf16 v[124:127], a[68:71], v[36:39], v[124:127]\n"
    "ds_read_b64_tr_b16 a[100:101], v244 offset:8448\n"
    "v_and_b32 v242, 0x818, v244\n"
    "v_mfma_f32_16x16x32_bf16 v[128:131], a[72:75], v[4:7], v[128:131]\n"
    "v_exp_f32 v71, v71\n"
    "v_and_b32 v243, 0xe0, v244\n"
    "v_mfma_f32_16x16x32_bf16 v[136:139], a[72:75], v[36:39], v[136:139]\n"
    "buffer_load_dwordx4 a[156:159], v254, s[40:43], s95 offen offset:3072 sc1\n"
    "v_mfma_f32_16x16x32_bf16 v[144:147], a[76:79], v[4:7], v[144:147]\n"
    "v_exp_f32 v76, v76\n"
    "v_cvt_pk_bf16_f32 v69, v70, v71\n"
    "v_mfma_f32_16x16x32_bf16 v[152:155], a[76:79], v[36:39], v[152:155]\n"
    "s_add_u32 s95, s95, 4096\n"
    "v_lshl_add_u32 v242, v243, 1, v242\n"
    "v_mfma_f32_16x16x32_bf16 v[160:163], a[80:83], v[4:7], v[160:163]\n"
    "ds_read_b64_tr_b16 a[102:103], v244 offset:12544\n"
    "v_exp_f32 v77, v77\n"
    "v_mfma_f32_16x16x32_bf16 v[168:171], a[80:83], v[36:39], v[168:171]\n"
    "buffer_load_dwordx4 a[160:163], v254, s[40:43], s95 offen offset:0 sc1\n"
    "v_mfma_f32_16x16x32_bf16 v[176:179], a[84:87], v[4:7], v[176:179]\n"
    "v_exp_f32 v78, v78\n"
    "v_mfma_f32_16x16x32_bf16 v[184:187], a[84:87], v[36:39], v[184:187]\n"
    "buffer_load_dwordx4 a[164:167], v254, s[40:43], s95 offen offset:1024 sc1\n"
    "v_cvt_pk_bf16_f32 v70, v76, v77\n"
    "v_mfma_f32_16x16x32_bf16 v[192:195], a[88:91], v[4:7], v[192:195]\n"
    "v_exp_f32 v79, v79\n"
    "v_mfma_f32_16x16x32_bf16 v[200:203], a[88:91], v[36:39], v[200:203]\n"
    "ds_read_b64_tr_b16 a[104:105], v244 offset:8704\n"
    "v_and_b32 v243, 0x80, v244\n"
    "v_cvt_pk_bf16_f32 v71, v78, v79\n"
    "v_mfma_f32_16x16x32_bf16 v[208:211], a[92:95], v[4:7], v[208:211]\n"
    "buffer_load_dwordx4 a[168:171], v254, s[40:43], s95 offen offset:2048 sc1\n"
    "v_mfma_f32_16x16x32_bf16 v[216:219], a[92:95], v[36:39], v[216:219]\n"
    "v_exp_f32 v100, v100\n"
    "v_lshrrev_b32 v243, 2, v243\n"
    "v_mfma_f32_16x16x32_bf16 v[4:7], a[32:35], v[0:3], v[20:23]\n"
    "v_exp_f32 v101, v101\n"
    "v_mfma_f32_16x16x32_bf16 v[4:7], a[36:39], v[8:11], v[4:7]\n"
    "buffer_load_dwordx4 a[172:175], v254, s[40:43], s95 offen offset:3072 sc1\n"
    "ds_read_b64_tr_b16 a[106:107], v244 offset:12800\n"
    "v_mfma_f32_16x16x32_bf16 v[4:7], a[40:43], v[16:19], v[4:7]\n"
    "v_exp_f32 v102, v102\n"
    "v_mfma_f32_16x16x32_bf16 v[4:7], a[44:47], v[24:27], v[4:7]\n"
    "s_add_u32 s95, s95, 4096\n"
    "v_add_u32 v242, v242, v243\n"
    "v_cvt_pk_bf16_f32 v100, v100, v101\n"
    "v_mfma_f32_16x16x32_bf16 v[12:15], a[48:51], v[0:3], v[20:23]\n"
    "buffer_load_dwordx4 a[176:179], v254, s[40:43], s95 offen offset:0 sc1\n"
    "v_mfma_f32_16x16x32_bf16 v[12:15], a[52:55], v[8:11], v[12:15]\n"
    "v_exp_f32 v103, v103\n"
    "v_mfma_f32_16x16x32_bf16 v[12:15], a[56:59], v[16:19], v[12:15]\n"
    "ds_read_b64_tr_b16 a[108:109], v244 offset:8960\n"
    "v_exp_f32 v108, v108\n"
    "v_mfma_f32_16x16x32_bf16 v[12:15], a[60:63], v[24:27], v[12:15]\n"
    "v_xor_b32 v243, 32, v242\n"
    "v_cvt_pk_bf16_f32 v101, v102, v103\n"
    "v_mfma_f32_16x16x32_bf16 v[36:39], a[32:35], v[32:35], v[52:55]\n"
    "buffer_load_dwordx4 a[180:183], v254, s[40:43], s95 offen offset:1024 sc1\n"
    "v_add_u32 v247, 65536, v242\n"
    "v_mfma_f32_16x16x32_bf16 v[36:39], a[36:39], v[40:43], v[36:39]\n"
    "v_exp_f32 v109, v109\n"
    "v_mfma_f32_16x16x32_bf16 v[36:39], a[40:43], v[48:51], v[36:39]\n"
    "buffer_load_dwordx4 a[184:187], v254, s[40:43], s95 offen offset:2048 sc1\n"
    "ds_read_b64_tr_b16 a[110:111], v244 offset:13056\n"
    "v_mfma_f32_16x16x32_bf16 v[36:39], a[44:47], v[56:59], v[36:39]\n"
    "v_exp_f32 v110, v110\n"
    "v_mfma_f32_16x16x32_bf16 v[44:47], a[48:51], v[32:35], v[52:55]\n"
    "buffer_load_dwordx4 a[188:191], v254, s[40:43], s95 offen offset:3072 sc1\n"
    "v_cvt_pk_bf16_f32 v102, v108, v109\n"
    "v_mfma_f32_16x16x32_bf16 v[44:47], a[52:55], v[40:43], v[44:47]\n"
    "ds_read_b64_tr_b16 a[112:113], v244 offset:9216\n"
    "v_add_u32 v248, 65536, v243\n"
    "v_mfma_f32_16x16x32_bf16 v[44:47], a[56:59], v[48:51], v[44:47]\n"
    "v_exp_f32 v111, v111\n"
    "s_nop 0\n"
    "v_cvt_pk_bf16_f32 v103, v110, v111\n"
    "v_mfma_f32_16x16x32_bf16 v[44:47], a[60:63], v[56:59], v[44:47]\n"
    "s_add_u32 s95, s95, 4096\n"
    "ds_read_b64_tr_b16 a[114:115], v244 offset:13312\n"
    "v_mfma_f32_16x16x32_bf16 a[200:203], a[208:211], v[68:71], a[200:203]\n"
    "s_cmp_le_u32 s80, 0\n"
    "s_cselect_b32 s99, 1, 0\n"
    "v_mfma_f32_16x16x32_bf16 a[204:207], a[208:211], v[100:103], a[204:207]\n"
    "buffer_load_dwordx4 a[212:215], v254, s[40:43], s95 offen offset:0 sc1\n"
    "v_mfma_f32_16x16x32_bf16 v[224:227], a[64:67], v[68:71], v[224:227]\n"
    "v_exp_f32 v4, v4\n"
    "v_mfma_f32_16x16x32_bf16 v[228:231], a[64:67], v[100:103], v[228:231]\n"
    "ds_read_b64_tr_b16 a[116:117], v244 offset:9472\n"
    "v_exp_f32 v5, v5\n"
    "v_mfma_f32_16x16x32_bf16 v[232:235], a[68:71], v[68:71], v[232:235]\n"
    "buffer_load_dwordx4 a[216:219], v254, s[40:43], s95 offen offset:1024 sc1\n"
    "v_mfma_f32_16x16x32_bf16 v[236:239], a[68:71], v[100:103], v[236:239]\n"
    "v_exp_f32 v6, v6\n"
    "v_cvt_pk_bf16_f32 v4, v4, v5\n"
    "v_mfma_f32_16x16x32_bf16 v[132:135], a[72:75], v[68:71], v[132:135]\n"
    "buffer_load_dwordx4 a[220:223], v254, s[40:43], s95 offen offset:2048 sc1\n"
    "v_mfma_f32_16x16x32_bf16 v[140:143], a[72:75], v[100:103], v[140:143]\n"
    "s_cmp_le_u32 s80, 1\n"
    "s_cselect_b32 s76, 2, 1\n"
    "ds_read_b64_tr_b16 a[118:119], v244 offset:13568\n"
    "v_mfma_f32_16x16x32_bf16 v[148:151], a[76:79], v[68:71], v[148:151]\n"
    "v_exp_f32 v7, v7\n"
    "v_mfma_f32_16x16x32_bf16 v[156:159], a[76:79], v[100:103], v[156:159]\n"
    "buffer_load_dwordx4 a[224:227], v254, s[40:43], s95 offen offset:3072 sc1\n"
    "s_add_u32 s95, s95, 4096\n"
    "v_mfma_f32_16x16x32_bf16 v[164:167], a[80:83], v[68:71], v[164:167]\n"
    "v_exp_f32 v12, v12\n"
    "v_mfma_f32_16x16x32_bf16 v[172:175], a[80:83], v[100:103], v[172:175]\n"
    "ds_read_b64_tr_b16 a[120:121], v244 offset:9728\n"
    "v_cvt_pk_bf16_f32 v5, v6, v7\n"
    "v_mfma_f32_16x16x32_bf16 v[180:183], a[84:87], v[68:71], v[180:183]\n"
    "v_exp_f32 v13, v13\n"
    "v_mfma_f32_16x16x32_bf16 v[188:191], a[84:87], v[100:103], v[188:191]\n"
    "buffer_load_dwordx4 a[228:231], v254, s[40:43], s95 offen offset:0 sc1\n"
    "v_mfma_f32_16x16x32_bf16 v[196:199], a[88:91], v[68:71], v[196:199]\n"
    "v_exp_f32 v14, v14\n"
    "v_cvt_pk_bf16_f32 v6, v12, v13\n"
    "v_mfma_f32_16x16x32_bf16 v[204:207], a[88:91], v[100:103], v[204:207]\n"
    "s_cmp_le_u32 s80, 2\n"
    "s_cselect_b32 s77, 3, 2\n"
    "v_mfma_f32_16x16x32_bf16 v[212:215], a[92:95], v[68:71], v[212:215]\n"
    "buffer_load_dwordx4 a[232:235], v254, s[40:43], s95 offen offset:1024 sc1\n"
    "ds_read_b64_tr_b16 a[122:123], v244 offset:13824\n"
    "v_mfma_f32_16x16x32_bf16 v[220:223], a[92:95], v[100:103], v[220:223]\n"
    "v_exp_f32 v15, v15\n"
    "v_mfma_f32_16x16x32_bf16 v[68:71], a[32:35], v[64:67], v[84:87]\n"
    "buffer_load_dwordx4 a[236:239], v254, s[40:43], s95 offen offset:2048 sc1\n"
    "v_cvt_pk_bf16_f32 v7, v14, v15\n"
    "v_mfma_f32_16x16x32_bf16 v[68:71], a[36:39], v[72:75], v[68:71]\n"
    "v_exp_f32 v36, v36\n"
    "v_mfma_f32_16x16x32_bf16 v[68:71], a[40:43], v[80:83], v[68:71]\n"
    "s_lshl_b32 s92, s60, 13\n"
    "v_exp_f32 v37, v37\n"
    "v_mfma_f32_16x16x32_bf16 v[68:71], a[44:47], v[88:91], v[68:71]\n"
    "buffer_load_dwordx4 a[240:243], v254, s[40:43], s95 offen offset:3072 sc1\n"
    "v_mfma_f32_16x16x32_bf16 v[76:79], a[48:51], v[64:67], v[84:87]\n"
    "v_exp_f32 v38, v38\n"
    "v_mfma_f32_16x16x32_bf16 v[76:79], a[52:55], v[72:75], v[76:79]\n"
    "s_add_u32 s95, s95, 4096\n"
    "s_lshl_b32 s67, s60, 12\n"
    "v_cvt_pk_bf16_f32 v36, v36, v37\n"
    "v_mfma_f32_16x16x32_bf16 v[76:79], a[56:59], v[80:83], v[76:79]\n"
    "ds_read_b64_tr_b16 a[124:125], v244 offset:9984\n"
    "s_mov_b32 s66, 786432\n"
    "v_mfma_f32_16x16x32_bf16 v[76:79], a[60:63], v[88:91], v[76:79]\n"
    "ds_read_b64_tr_b16 a[126:127], v244 offset:14080\n"
    "v_exp_f32 v39, v39\n"
    "v_mfma_f32_16x16x32_bf16 v[100:103], a[32:35], v[96:99], v[116:119]\n"
    "buffer_load_dwordx4 a[244:247], v254, s[40:43], s95 offen offset:0 sc1\n"
    "v_mfma_f32_16x16x32_bf16 v[100:103], a[36:39], v[104:107], v[100:103]\n"
    "v_exp_f32 v44, v44\n"
    "v_cvt_pk_bf16_f32 v37, v38, v39\n"
    "v_mfma_f32_16x16x32_bf16 v[100:103], a[40:43], v[112:115], v[100:103]\n"
    "buffer_load_dwordx4 a[248:251], v254, s[40:43], s95 offen offset:1024 sc1\n"
    "v_mfma_f32_16x16x32_bf16 v[100:103], a[44:47], v[120:123], v[100:103]\n"
    "buffer_load_dwordx4 a[252:255], v254, s[40:43], s95 offen offset:2048 sc1\n"
    "v_add_u32 v245, 3072, v251\n"
    "v_mfma_f32_16x16x32_bf16 v[108:111], a[48:51], v[96:99], v[116:119]\n"
    "v_exp_f32 v45, v45\n"
    "v_mfma_f32_16x16x32_bf16 v[108:111], a[52:55], v[104:107], v[108:111]\n"
    "v_exp_f32 v46, v46\n"
    "v_cvt_pk_bf16_f32 v38, v44, v45\n"
    "v_mfma_f32_16x16x32_bf16 v[108:111], a[56:59], v[112:115], v[108:111]\n"
    "v_exp_f32 v47, v47\n"
    "s_nop 0\n"
    "v_cvt_pk_bf16_f32 v39, v46, v47\n"
    "v_mfma_f32_16x16x32_bf16 v[108:111], a[60:63], v[120:123], v[108:111]\n"
    "v_add_u32 v246, 2176, v251\n"
    "s_waitcnt lgkmcnt(0)\n"
    "s_barrier\n"
    "v_mfma_f32_16x16x32_bf16 a[192:195], a[208:211], v[4:7], a[192:195]\n"
    "v_exp_f32 v68, v68\n"
    "v_mfma_f32_16x16x32_bf16 a[196:199], a[208:211], v[36:39], a[196:199]\n"
    "s_lshl_b32 s90, s80, 17\n"
    "v_exp_f32 v69, v69\n"
    "v_mfma_f32_16x16x32_bf16 v[28:31], a[96:99], v[4:7], v[28:31]\n"
    "s_add_u32 m0, s92, 0\n"
    "v_exp_f32 v70, v70\n"
    "v_mfma_f32_16x16x32_bf16 v[60:63], a[96:99], v[36:39], v[60:63]\n"
    "buffer_load_dwordx4 v245, s[52:55], s90 offen lds\n"
    "v_mov_b32 v52, 0x53800000\n"
    "v_mfma_f32_16x16x32_bf16 v[92:95], a[100:103], v[4:7], v[92:95]\n"
    "v_cvt_pk_bf16_f32 v68, v68, v69\n"
    "v_mfma_f32_16x16x32_bf16 v[124:127], a[100:103], v[36:39], v[124:127]\n"
    "v_exp_f32 v71, v71\n"
    "v_add_u32 v244, 131072, v242\n"
    "v_mfma_f32_16x16x32_bf16 v[128:131], a[104:107], v[4:7], v[128:131]\n"
    "v_exp_f32 v76, v76\n"
    "v_cvt_pk_bf16_f32 v69, v70, v71\n"
    "v_mfma_f32_16x16x32_bf16 v[136:139], a[104:107], v[36:39], v[136:139]\n"
    "v_exp_f32 v77, v77\n"
    "v_mfma_f32_16x16x32_bf16 v[144:147], a[108:111], v[4:7], v[144:147]\n"
    "v_exp_f32 v78, v78\n"
    "v_mfma_f32_16x16x32_bf16 v[152:155], a[108:111], v[36:39], v[152:155]\n"
    "v_cvt_pk_bf16_f32 v70, v76, v77\n"
    "v_exp_f32 v79, v79\n"
    "v_mfma_f32_16x16x32_bf16 v[160:163], a[112:115], v[4:7], v[160:163]\n"
    "v_cvt_pk_bf16_f32 v71, v78, v79\n"
    "v_exp_f32 v100, v100\n"
    "v_mfma_f32_16x16x32_bf16 v[168:171], a[112:115], v[36:39], v[168:171]\n"
    "ds_read_b64_tr_b16 a[0:1], v247 offset:0\n"
    "v_exp_f32 v101, v101\n"
    "v_mfma_f32_16x16x32_bf16 v[176:179], a[116:119], v[4:7], v[176:179]\n"
    "v_exp_f32 v102, v102\n"
    "v_mfma_f32_16x16x32_bf16 v[184:187], a[116:119], v[36:39], v[184:187]\n"
    "v_cvt_pk_bf16_f32 v100, v100, v101\n"
    "v_exp_f32 v103, v103\n"
    "v_mfma_f32_16x16x32_bf16 v[192:195], a[120:123], v[4:7], v[192:195]\n"
    "buffer_load_dwordx4 v246, s[52:55], s90 offen offset:1024 lds\n"
    "s_add_u32 s91, s90, 6144\n"
    "v_mfma_f32_16x16x32_bf16 v[200:203], a[120:123], v[36:39], v[200:203]\n"
    "buffer_load_dwordx4 v245, s[52:55], s91 offen offset:2048 lds\n"
    "ds_read_b64_tr_b16 a[2:3], v247 offset:1024\n"
    "v_mfma_f32_16x16x32_bf16 v[208:211], a[124:127], v[4:7], v[208:211]\n"
    "buffer_load_dwordx4 v246, s[52:55], s91 offen offset:3072 lds\n"
    "v_exp_f32 v108, v108\n"
    "v_cvt_pk_bf16_f32 v101, v102, v103\n"
    "v_exp_f32 v109, v109\n"
    "v_exp_f32 v110, v110\n"
    "v_cvt_pk_bf16_f32 v102, v108, v109\n"
    "v_exp_f32 v111, v111\n"
    "s_nop 0\n"
    "v_cvt_pk_bf16_f32 v103, v110, v111\n"
    "v_mfma_f32_16x16x32_bf16 v[216:219], a[124:127], v[36:39], v[216:219]\n"
    "s_add_u32 s90, s90, 16384\n"
    "s_add_u32 m0, s92, 4096\n"
    "ds_read_b64_tr_b16 a[4:5], v248 offset:0\n"
    "v_mfma_f32_16x16x32_bf16 a[200:203], a[208:211], v[68:71], a[200:203]\n"
    "buffer_load_dwordx4 v245, s[52:55], s90 offen lds\n"
    "ds_read_b64_tr_b16 a[6:7], v248 offset:1024\n"
    "v_mfma_f32_16x16x32_bf16 a[204:207], a[208:211], v[100:103], a[204:207]\n"
    "buffer_load_dwordx4 v246, s[52:55], s90 offen offset:1024 lds\n"
    "s_add_u32 s91, s90, 6144\n"
    "buffer_load_dwordx4 v245, s[52:55], s91 offen offset:2048 lds\n"
    "buffer_load_dwordx4 v246, s[52:55], s91 offen offset:3072 lds\n"
    "v_add_u32 v250, 131072, v243\n"
    "v_mfma_f32_16x16x32_bf16 v[224:227], a[96:99], v[68:71], v[224:227]\n"
    "ds_read_b64_tr_b16 a[8:9], v247 offset:512\n"
    "ds_read_b64_tr_b16 a[10:11], v247 offset:1536\n"
    "v_mfma_f32_16x16x32_bf16 v[228:231], a[96:99], v[100:103], v[228:231]\n"
    "ds_read_b64_tr_b16 a[12:13], v248 offset:512\n"
    "ds_read_b64_tr_b16 a[14:15], v248 offset:1536\n"
    "ds_read_b64_tr_b16 a[16:17], v247 offset:4096\n"
    "v_mfma_f32_16x16x32_bf16 v[232:235], a[100:103], v[68:71], v[232:235]\n"
    "ds_read_b64_tr_b16 a[18:19], v247 offset:5120\n"
    "v_mfma_f32_16x16x32_bf16 v[236:239], a[100:103], v[100:103], v[236:239]\n"
    "v_accvgpr_read_b32 v20, a192\n"
    "v_accvgpr_read_b32 v21, a196\n"
    "v_accvgpr_read_b32 v22, a200\n"
    "v_mfma_f32_16x16x32_bf16 v[132:135], a[104:107], v[68:71], v[132:135]\n"
    "ds_read_b64_tr_b16 a[20:21], v248 offset:4096\n"
    "v_accvgpr_read_b32 v23, a204\n"
    "v_mfma_f32_16x16x32_bf16 v[140:143], a[104:107], v[100:103], v[140:143]\n"
    "ds_read_b64_tr_b16 a[22:23], v248 offset:5120\n"
    "v_max3_u32 v254, v20, v21, v22\n"
    "v_max_u32 v254, v254, v23\n"
    "v_mfma_f32_16x16x32_bf16 v[148:151], a[108:111], v[68:71], v[148:151]\n"
    "ds_read_b64_tr_b16 a[24:25], v247 offset:4608\n"
    "v_cmp_ge_u32 vcc, v254, v52\n"
    "s_nop 0\n"
    "s_cbranch_vccnz LFB\n"
    "LFB_r:\n"
    "v_mfma_f32_16x16x32_bf16 v[156:159], a[108:111], v[100:103], v[156:159]\n"
    "v_rcp_f32 v32, v20\n"
    "v_mfma_f32_16x16x32_bf16 v[164:167], a[112:115], v[68:71], v[164:167]\n"
    "ds_read_b64_tr_b16 a[26:27], v247 offset:5632\n"
    "v_mfma_f32_16x16x32_bf16 v[172:175], a[112:115], v[100:103], v[172:175]\n"
    "v_rcp_f32 v33, v21\n"
    "v_mfma_f32_16x16x32_bf16 v[180:183], a[116:119], v[68:71], v[180:183]\n"
    "ds_read_b64_tr_b16 a[28:29], v248 offset:4608\n"
    "v_rcp_f32 v34, v22\n"
    "v_mfma_f32_16x16x32_bf16 v[188:191], a[116:119], v[100:103], v[188:191]\n"
    "v_mfma_f32_16x16x32_bf16 v[196:199], a[120:123], v[68:71], v[196:199]\n"
    "ds_read_b64_tr_b16 a[30:31], v248 offset:5632\n"
    "v_mfma_f32_16x16x32_bf16 v[204:207], a[120:123], v[100:103], v[204:207]\n"
    "v_rcp_f32 v35, v23\n"
    "v_mfma_f32_16x16x32_bf16 v[212:215], a[124:127], v[68:71], v[212:215]\n"
    "v_mfma_f32_16x16x32_bf16 v[220:223], a[124:127], v[100:103], v[220:223]\n"
    "s_waitcnt vmcnt(34) lgkmcnt(14)\n"
    "v_mfma_f32_16x16x32_bf16 v[0:3], a[0:3], a[128:131], 0\n"
    "ds_read_b64_tr_b16 a[32:33], v247 offset:8192\n"
    "s_add_u32 s65, s78, 0\n"
    "ds_read_b64_tr_b16 a[34:35], v247 offset:9216\n"
    "v_mul_f32 v28, v28, v32\n"
    "s_waitcnt vmcnt(33) lgkmcnt(8)\n"
    "v_mfma_f32_16x16x32_bf16 v[0:3], a[16:19], a[132:135], v[0:3]\n"
    "ds_read_b64_tr_b16 a[36:37], v248 offset:8192\n"
    "ds_read_b64_tr_b16 a[38:39], v248 offset:9216\n"
    "v_mul_f32 v29, v29, v32\n"
    "v_mul_f32 v30, v30, v32\n"
    "v_mul_f32 v31, v31, v32\n"
    "v_mfma_f32_16x16x32_bf16 v[8:11], a[4:7], a[128:131], 0\n"
    "ds_read_b64_tr_b16 a[40:41], v247 offset:8704\n"
    "ds_read_b64_tr_b16 a[42:43], v247 offset:9728\n"
    "v_mul_f32 v92, v92, v32\n"
    "v_mul_f32 v93, v93, v32\n"
    "v_mul_f32 v94, v94, v32\n"
    "s_waitcnt lgkmcnt(10)\n"
    "v_mfma_f32_16x16x32_bf16 v[8:11], a[20:23], a[132:135], v[8:11]\n"
    "ds_read_b64_tr_b16 a[44:45], v248 offset:8704\n"
    "ds_read_b64_tr_b16 a[46:47], v248 offset:9728\n"
    "v_mul_f32 v95, v95, v32\n"
    "v_cvt_pk_bf16_f32 v28, v28, v29\n"
    "v_cvt_pk_bf16_f32 v29, v30, v31\n"
    "v_mfma_f32_16x16x32_bf16 v[16:19], a[8:11], a[128:131], 0\n"
    "ds_read_b64_tr_b16 a[48:49], v247 offset:12288\n"
    "ds_read_b64_tr_b16 a[50:51], v247 offset:13312\n"
    "ds_read_b64_tr_b16 a[52:53], v248 offset:12288\n"
    "v_cvt_pk_bf16_f32 v30, v92, v93\n"
    "v_cvt_pk_bf16_f32 v31, v94, v95\n"
    "s_waitcnt lgkmcnt(13)\n"
    "v_mfma_f32_16x16x32_bf16 v[16:19], a[24:27], a[132:135], v[16:19]\n"
    "buffer_store_dwordx4 v[28:31], v240, s[56:59], s65 offen offset:0 sc1\n"
    "ds_read_b64_tr_b16 a[54:55], v248 offset:13312\n"
    "v_mul_f32 v128, v128, v32\n"
    "v_mul_f32 v129, v129, v32\n"
    "v_mfma_f32_16x16x32_bf16 v[24:27], a[12:15], a[128:131], 0\n"
    "ds_read_b64_tr_b16 a[56:57], v247 offset:12800\n"
    "ds_read_b64_tr_b16 a[58:59], v247 offset:13824\n"
    "v_mul_f32 v130, v130, v32\n"
    "v_mul_f32 v131, v131, v32\n"
    "v_mul_f32 v144, v144, v32\n"
    "s_waitcnt lgkmcnt(14)\n"
    "v_mfma_f32_16x16x32_bf16 v[24:27], a[28:31], a[132:135], v[24:27]\n"
    "ds_read_b64_tr_b16 a[60:61], v248 offset:12800\n"
    "ds_read_b64_tr_b16 a[62:63], v248 offset:13824\n"
    "v_mul_f32 v145, v145, v32\n"
    "v_mul_f32 v146, v146, v32\n"
    "v_mul_f32 v147, v147, v32\n"
    "s_waitcnt vmcnt(33) lgkmcnt(14)\n"
    "v_mfma_f32_16x16x32_bf16 v[0:3], a[32:35], a[136:139], v[0:3]\n"
    "ds_read_b64_tr_b16 a[0:1], v247 offset:16384\n"
    "ds_read_b64_tr_b16 a[2:3], v247 offset:17408\n"
    "v_cvt_pk_bf16_f32 v128, v128, v129\n"
    "v_cvt_pk_bf16_f32 v129, v130, v131\n"
    "v_cvt_pk_bf16_f32 v130, v144, v145\n"
    "s_waitcnt vmcnt(32) lgkmcnt(8)\n"
    "v_mfma_f32_16x16x32_bf16 v[0:3], a[48:51], a[140:143], v[0:3]\n"
    "ds_read_b64_tr_b16 a[4:5], v248 offset:16384\n"
    "ds_read_b64_tr_b16 a[6:7], v248 offset:17408\n"
    "ds_read_b64_tr_b16 a[8:9], v247 offset:16896\n"
    "ds_read_b64_tr_b16 a[10:11], v247 offset:17920\n"
    "v_cvt_pk_bf16_f32 v131, v146, v147\n"
    "v_mfma_f32_16x16x32_bf16 v[8:11], a[36:39], a[136:139], v[8:11]\n"
    "buffer_store_dwordx4 v[128:131], v240, s[56:59], s65 offen offset:1024 sc1\n"
    "v_mul_f32 v160, v160, v32\n"
    "v_mul_f32 v161, v161, v32\n"
    "v_mul_f32 v162, v162, v32\n"
    "s_waitcnt lgkmcnt(10)\n"
    "v_mfma_f32_16x16x32_bf16 v[8:11], a[52:55], a[140:143], v[8:11]\n"
    "ds_read_b64_tr_b16 a[12:13], v248 offset:16896\n"
    "ds_read_b64_tr_b16 a[14:15], v248 offset:17920\n"
    "ds_read_b64_tr_b16 a[16:17], v247 offset:20480\n"
    "v_mul_f32 v163, v163, v32\n"
    "v_mul_f32 v176, v176, v32\n"
    "v_mfma_f32_16x16x32_bf16 v[16:19], a[40:43], a[136:139], v[16:19]\n"
    "ds_read_b64_tr_b16 a[18:19], v247 offset:21504\n"
    "ds_read_b64_tr_b16 a[20:21], v248 offset:20480\n"
    "v_mul_f32 v177, v177, v32\n"
    "v_mul_f32 v178, v178, v32\n"
    "v_mul_f32 v179, v179, v32\n"
    "s_waitcnt lgkmcnt(13)\n"
    "v_mfma_f32_16x16x32_bf16 v[16:19], a[56:59], a[140:143], v[16:19]\n"
    "ds_read_b64_tr_b16 a[22:23], v248 offset:21504\n"
    "ds_read_b64_tr_b16 a[24:25], v247 offset:20992\n"
    "v_cvt_pk_bf16_f32 v160, v160, v161\n"
    "v_cvt_pk_bf16_f32 v161, v162, v163\n"
    "v_cvt_pk_bf16_f32 v162, v176, v177\n"
    "v_mfma_f32_16x16x32_bf16 v[24:27], a[44:47], a[136:139], v[24:27]\n"
    "ds_read_b64_tr_b16 a[26:27], v247 offset:22016\n"
    "ds_read_b64_tr_b16 a[28:29], v248 offset:20992\n"
    "ds_read_b64_tr_b16 a[30:31], v248 offset:22016\n"
    "v_cvt_pk_bf16_f32 v163, v178, v179\n"
    "s_waitcnt lgkmcnt(15)\n"
    "v_mfma_f32_16x16x32_bf16 v[24:27], a[60:63], a[140:143], v[24:27]\n"
    "buffer_store_dwordx4 v[160:163], v240, s[56:59], s65 offen offset:2048 sc1\n"
    "v_mul_f32 v192, v192, v32\n"
    "v_mul_f32 v193, v193, v32\n"
    "v_mul_f32 v194, v194, v32\n"
    "s_waitcnt vmcnt(33) lgkmcnt(14)\n"
    "v_mfma_f32_16x16x32_bf16 v[0:3], a[0:3], a[144:147], v[0:3]\n"
    "ds_read_b64_tr_b16 a[32:33], v247 offset:24576\n"
    "ds_read_b64_tr_b16 a[34:35], v247 offset:25600\n"
    "v_mul_f32 v195, v195, v32\n"
    "v_mul_f32 v208, v208, v32\n"
    "v_mul_f32 v209, v209, v32\n"
    "s_waitcnt vmcnt(32) lgkmcnt(8)\n"
    "v_mfma_f32_16x16x32_bf16 v[0:3], a[16:19], a[148:151], v[0:3]\n"
    "ds_read_b64_tr_b16 a[36:37], v248 offset:24576\n"
    "ds_read_b64_tr_b16 a[38:39], v248 offset:25600\n"
    "ds_read_b64_tr_b16 a[40:41], v247 offset:25088\n"
    "v_mul_f32 v210, v210, v32\n"
    "v_mul_f32 v211, v211, v32\n"
    "v_mfma_f32_16x16x32_bf16 v[8:11], a[4:7], a[144:147], v[8:11]\n"
    "ds_read_b64_tr_b16 a[42:43], v247 offset:26112\n"
    "ds_read_b64_tr_b16 a[44:45], v248 offset:25088\n"
    "v_cvt_pk_bf16_f32 v192, v192, v193\n"
    "v_cvt_pk_bf16_f32 v193, v194, v195\n"
    "v_cvt_pk_bf16_f32 v194, v208, v209\n"
    "s_waitcnt lgkmcnt(11)\n"
    "v_mfma_f32_16x16x32_bf16 v[8:11], a[20:23], a[148:151], v[8:11]\n"
    "ds_read_b64_tr_b16 a[46:47], v248 offset:26112\n"
    "ds_read_b64_tr_b16 a[48:49], v247 offset:28672\n"
    "ds_read_b64_tr_b16 a[50:51], v247 offset:29696\n"
    "ds_read_b64_tr_b16 a[52:53], v248 offset:28672\n"
    "v_cvt_pk_bf16_f32 v195, v210, v211\n"
    "v_mfma_f32_16x16x32_bf16 v[16:19], a[8:11], a[144:147], v[16:19]\n"
    "buffer_store_dwordx4 v[192:195], v240, s[56:59], s65 offen offset:3072 sc1\n"
    "s_add_u32 s65, s78, 131072\n"
    "v_mul_f32 v60, v60, v33\n"
    "v_mul_f32 v61, v61, v33\n"
    "s_waitcnt lgkmcnt(13)\n"
    "v_mfma_f32_16x16x32_bf16 v[16:19], a[24:27], a[148:151], v[16:19]\n"
    "ds_read_b64_tr_b16 a[54:55], v248 offset:29696\n"
    "v_mul_f32 v62, v62, v33\n"
    "v_mul_f32 v63, v63, v33\n"
    "v_mul_f32 v124, v124, v33\n"
    "v_mfma_f32_16x16x32_bf16 v[24:27], a[12:15], a[144:147], v[24:27]\n"
    "ds_read_b64_tr_b16 a[56:57], v247 offset:29184\n"
    "ds_read_b64_tr_b16 a[58:59], v247 offset:30208\n"
    "v_mul_f32 v125, v125, v33\n"
    "v_mul_f32 v126, v126, v33\n"
    "v_mul_f32 v127, v127, v33\n"
    "s_waitcnt lgkmcnt(14)\n"
    "v_mfma_f32_16x16x32_bf16 v[24:27], a[28:31], a[148:151], v[24:27]\n"
    "ds_read_b64_tr_b16 a[60:61], v248 offset:29184\n"
    "ds_read_b64_tr_b16 a[62:63], v248 offset:30208\n"
    "v_cvt_pk_bf16_f32 v60, v60, v61\n"
    "v_cvt_pk_bf16_f32 v61, v62, v63\n"
    "v_cvt_pk_bf16_f32 v62, v124, v125\n"
    "s_waitcnt vmcnt(32) lgkmcnt(14)\n"
    "v_mfma_f32_16x16x32_bf16 v[0:3], a[32:35], a[152:155], v[0:3]\n"
    "ds_read_b64_tr_b16 a[0:1], v247 offset:32768\n"
    "ds_read_b64_tr_b16 a[2:3], v247 offset:33792\n"
    "ds_read_b64_tr_b16 a[4:5], v248 offset:32768\n"
    "ds_read_b64_tr_b16 a[6:7], v248 offset:33792\n"
    "v_cvt_pk_bf16_f32 v63, v126, v127\n"
    "s_waitcnt vmcnt(31) lgkmcnt(10)\n"
    "v_mfma_f32_16x16x32_bf16 v[0:3], a[48:51], a[156:159], v[0:3]\n"
    "buffer_store_dwordx4 v[60:63], v240, s[56:59], s65 offen offset:0 sc1\n"
    "ds_read_b64_tr_b16 a[8:9], v247 offset:33280\n"
    "v_mul_f32 v136, v136, v33\n"
    "v_mul_f32 v137, v137, v33\n"
    "v_mfma_f32_16x16x32_bf16 v[8:11], a[36:39], a[152:155], v[8:11]\n"
    "ds_read_b64_tr_b16 a[10:11], v247 offset:34304\n"
    "ds_read_b64_tr_b16 a[12:13], v248 offset:33280\n"
    "v_mul_f32 v138, v138, v33\n"
    "v_mul_f32 v139, v139, v33\n"
    "v_mul_f32 v152, v152, v33\n"
    "s_waitcnt lgkmcnt(11)\n"
    "v_mfma_f32_16x16x32_bf16 v[8:11], a[52:55], a[156:159], v[8:11]\n"
    "ds_read_b64_tr_b16 a[14:15], v248 offset:34304\n"
    "ds_read_b64_tr_b16 a[16:17], v247 offset:36864\n"
    "v_mul_f32 v153, v153, v33\n"
    "v_mul_f32 v154, v154, v33\n"
    "v_mul_f32 v155, v155, v33\n"
    "v_mfma_f32_16x16x32_bf16 v[16:19], a[40:43], a[152:155], v[16:19]\n"
    "ds_read_b64_tr_b16 a[18:19], v247 offset:37888\n"
    "ds_read_b64_tr_b16 a[20:21], v248 offset:36864\n"
    "v_cvt_pk_bf16_f32 v136, v136, v137\n"
    "v_cvt_pk_bf16_f32 v137, v138, v139\n"
    "v_cvt_pk_bf16_f32 v138, v152, v153\n"
    "s_waitcnt lgkmcnt(13)\n"
    "v_mfma_f32_16x16x32_bf16 v[16:19], a[56:59], a[156:159], v[16:19]\n"
    "ds_read_b64_tr_b16 a[22:23], v248 offset:37888\n"
    "ds_read_b64_tr_b16 a[24:25], v247 offset:37376\n"
    "ds_read_b64_tr_b16 a[26:27], v247 offset:38400\n"
    "ds_read_b64_tr_b16 a[28:29], v248 offset:37376\n"
    "v_cvt_pk_bf16_f32 v139, v154, v155\n"
    "v_mfma_f32_16x16x32_bf16 v[24:27], a[44:47], a[152:155], v[24:27]\n"
    "buffer_store_dwordx4 v[136:139], v240, s[56:59], s65 offen offset:1024 sc1\n"
    "v_mul_f32 v168, v168, v33\n"
    "v_mul_f32 v169, v169, v33\n"
    "v_mul_f32 v170, v170, v33\n"
    "s_waitcnt lgkmcnt(15)\n"
    "v_mfma_f32_16x16x32_bf16 v[24:27], a[60:63], a[156:159], v[24:27]\n"
    "ds_read_b64_tr_b16 a[30:31], v248 offset:38400\n"
    "v_mul_f32 v171, v171, v33\n"
    "v_mul_f32 v184, v184, v33\n"
    "v_mul_f32 v185, v185, v33\n"
    "v_mul_f32 v186, v186, v33\n"
    "s_waitcnt vmcnt(32) lgkmcnt(14)\n"
    "v_mfma_f32_16x16x32_bf16 v[0:3], a[0:3], a[160:163], v[0:3]\n"
    "ds_read_b64_tr_b16 a[32:33], v247 offset:40960\n"
    "ds_read_b64_tr_b16 a[34:35], v247 offset:41984\n"
    "ds_read_b64_tr_b16 a[36:37], v248 offset:40960\n"
    "v_mul_f32 v187, v187, v33\n"
    "v_cvt_pk_bf16_f32 v168, v168, v169\n"
    "s_waitcnt vmcnt(31) lgkmcnt(9)\n"
    "v_mfma_f32_16x16x32_bf16 v[0:3], a[16:19], a[164:167], v[0:3]\n"
    "ds_read_b64_tr_b16 a[38:39], v248 offset:41984\n"
    "ds_read_b64_tr_b16 a[40:41], v247 offset:41472\n"
    "v_cvt_pk_bf16_f32 v169, v170, v171\n"
    "v_cvt_pk_bf16_f32 v170, v184, v185\n"
    "v_cvt_pk_bf16_f32 v171, v186, v187\n"
    "v_mfma_f32_16x16x32_bf16 v[8:11], a[4:7], a[160:163], v[8:11]\n"
    "ds_read_b64_tr_b16 a[42:43], v247 offset:42496\n"
    "buffer_store_dwordx4 v[168:171], v240, s[56:59], s65 offen offset:2048 sc1\n"
    "ds_read_b64_tr_b16 a[44:45], v248 offset:41472\n"
    "v_mul_f32 v200, v200, v33\n"
    "s_waitcnt lgkmcnt(11)\n"
    "v_mfma_f32_16x16x32_bf16 v[8:11], a[20:23], a[164:167], v[8:11]\n"
    "ds_read_b64_tr_b16 a[46:47], v248 offset:42496\n"
    "ds_read_b64_tr_b16 a[48:49], v247 offset:45056\n"
    "v_mul_f32 v201, v201, v33\n"
    "v_mul_f32 v202, v202, v33\n"
    "v_mul_f32 v203, v203, v33\n"
    "v_mfma_f32_16x16x32_bf16 v[16:19], a[8:11], a[160:163], v[16:19]\n"
    "ds_read_b64_tr_b16 a[50:51], v247 offset:46080\n"
    "ds_read_b64_tr_b16 a[52:53], v248 offset:45056\n"
    "v_mul_f32 v216, v216, v33\n"
    "v_mul_f32 v217, v217, v33\n"
    "v_mul_f32 v218, v218, v33\n"
    "s_waitcnt lgkmcnt(13)\n"
    "v_mfma_f32_16x16x32_bf16 v[16:19], a[24:27], a[164:167], v[16:19]\n"
    "ds_read_b64_tr_b16 a[54:55], v248 offset:46080\n"
    "ds_read_b64_tr_b16 a[56:57], v247 offset:45568\n"
    "v_mul_f32 v219, v219, v33\n"
    "v_cvt_pk_bf16_f32 v200, v200, v201\n"
    "v_cvt_pk_bf16_f32 v201, v202, v203\n"
    "v_mfma_f32_16x16x32_bf16 v[24:27], a[12:15], a[160:163], v[24:27]\n"
    "ds_read_b64_tr_b16 a[58:59], v247 offset:46592\n"
    "ds_read_b64_tr_b16 a[60:61], v248 offset:45568\n"
    "ds_read_b64_tr_b16 a[62:63], v248 offset:46592\n"
    "v_cvt_pk_bf16_f32 v202, v216, v217\n"
    "v_cvt_pk_bf16_f32 v203, v218, v219\n"
    "s_waitcnt lgkmcnt(15)\n"
    "v_mfma_f32_16x16x32_bf16 v[24:27], a[28:31], a[164:167], v[24:27]\n"
    "buffer_store_dwordx4 v[200:203], v240, s[56:59], s65 offen offset:3072 sc1\n"
    "s_add_u32 s65, s78, 262144\n"
    "v_mul_f32 v224, v224, v34\n"
    "v_mul_f32 v225, v225, v34\n"
    "s_waitcnt vmcnt(32) lgkmcnt(14)\n"
    "v_mfma_f32_16x16x32_bf16 v[0:3], a[32:35], a[168:171], v[0:3]\n"
    "ds_read_b64_tr_b16 a[0:1], v247 offset:49152\n"
    "ds_read_b64_tr_b16 a[2:3], v247 offset:50176\n"
    "ds_read_b64_tr_b16 a[4:5], v248 offset:49152\n"
    "ds_read_b64_tr_b16 a[6:7], v248 offset:50176\n"
    "v_mul_f32 v226, v226, v34\n"
    "s_waitcnt vmcnt(31) lgkmcnt(10)\n"
    "v_mfma_f32_16x16x32_bf16 v[0:3], a[48:51], a[172:175], v[0:3]\n"
    "ds_read_b64_tr_b16 a[8:9], v247 offset:49664\n"
    "ds_read_b64_tr_b16 a[10:11], v247 offset:50688\n"
    "v_mul_f32 v227, v227, v34\n"
    "v_mul_f32 v232, v232, v34\n"
    "v_mul_f32 v233, v233, v34\n"
    "v_mfma_f32_16x16x32_bf16 v[8:11], a[36:39], a[168:171], v[8:11]\n"
    "ds_read_b64_tr_b16 a[12:13], v248 offset:49664\n"
    "v_mul_f32 v234, v234, v34\n"
    "v_mul_f32 v235, v235, v34\n"
    "v_cvt_pk_bf16_f32 v224, v224, v225\n"
    "s_waitcnt lgkmcnt(11)\n"
    "v_mfma_f32_16x16x32_bf16 v[8:11], a[52:55], a[172:175], v[8:11]\n"
    "ds_read_b64_tr_b16 a[14:15], v248 offset:50688\n"
    "ds_read_b64_tr_b16 a[16:17], v247 offset:53248\n"
    "v_cvt_pk_bf16_f32 v225, v226, v227\n"
    "v_cvt_pk_bf16_f32 v226, v232, v233\n"
    "v_cvt_pk_bf16_f32 v227, v234, v235\n"
    "v_mfma_f32_16x16x32_bf16 v[16:19], a[40:43], a[168:171], v[16:19]\n"
    "ds_read_b64_tr_b16 a[18:19], v247 offset:54272\n"
    "buffer_store_dwordx4 v[224:227], v240, s[56:59], s65 offen offset:0 sc1\n"
    "ds_read_b64_tr_b16 a[20:21], v248 offset:53248\n"
    "ds_read_b64_tr_b16 a[22:23], v248 offset:54272\n"
    "s_waitcnt lgkmcnt(14)\n"
    "v_mfma_f32_16x16x32_bf16 v[16:19], a[56:59], a[172:175], v[16:19]\n"
    "ds_read_b64_tr_b16 a[24:25], v247 offset:53760\n"
    "ds_read_b64_tr_b16 a[26:27], v247 offset:54784\n"
    "v_mul_f32 v132, v132, v34\n"
    "v_mul_f32 v133, v133, v34\n"
    "v_mul_f32 v134, v134, v34\n"
    "v_mfma_f32_16x16x32_bf16 v[24:27], a[44:47], a[168:171], v[24:27]\n"
    "ds_read_b64_tr_b16 a[28:29], v248 offset:53760\n"
    "ds_read_b64_tr_b16 a[30:31], v248 offset:54784\n"
    "v_mul_f32 v135, v135, v34\n"
    "v_mul_f32 v148, v148, v34\n"
    "v_mul_f32 v149, v149, v34\n"
    "s_waitcnt lgkmcnt(15)\n"
    "v_mfma_f32_16x16x32_bf16 v[24:27], a[60:63], a[172:175], v[24:27]\n"
    "v_mul_f32 v150, v150, v34\n"
    "s_waitcnt vmcnt(9)\n"
    "s_barrier\n"
    "s_waitcnt lgkmcnt(14)\n"
    "v_mfma_f32_16x16x32_bf16 v[0:3], a[0:3], a[176:179], v[0:3]\n"
    "ds_read_b64_tr_b16 a[32:33], v247 offset:57344\n"
    "ds_read_b64_tr_b16 a[64:65], v242 offset:0\n"
    "ds_read_b64_tr_b16 a[34:35], v247 offset:58368\n"
    "v_mul_f32 v151, v151, v34\n"
    "s_waitcnt lgkmcnt(9)\n"
    "v_mfma_f32_16x16x32_bf16 v[0:3], a[16:19], a[180:183], v[0:3]\n"
    "ds_read_b64_tr_b16 a[36:37], v248 offset:57344\n"
    "ds_read_b64_tr_b16 a[38:39], v248 offset:58368\n"
    "v_cvt_pk_bf16_f32 v132, v132, v133\n"
    "v_cvt_pk_bf16_f32 v133, v134, v135\n"
    "v_mfma_f32_16x16x32_bf16 v[8:11], a[4:7], a[176:179], v[8:11]\n"
    "ds_read_b64_tr_b16 a[66:67], v242 offset:4096\n"
    "ds_read_b64_tr_b16 a[40:41], v247 offset:57856\n"
    "ds_read_b64_tr_b16 a[42:43], v247 offset:58880\n"
    "v_cvt_pk_bf16_f32 v134, v148, v149\n"
    "v_cvt_pk_bf16_f32 v135, v150, v151\n"
    "s_waitcnt lgkmcnt(12)\n"
    "v_mfma_f32_16x16x32_bf16 v[8:11], a[20:23], a[180:183], v[8:11]\n"
    "buffer_store_dwordx4 v[132:135], v240, s[56:59], s65 offen offset:1024 sc1\n"
    "ds_read_b64_tr_b16 a[44:45], v248 offset:57856\n"
    "ds_read_b64_tr_b16 a[80:81], v242 offset:8192\n"
    "v_mfma_f32_16x16x32_bf16 v[16:19], a[8:11], a[176:179], v[16:19]\n"
    "ds_read_b64_tr_b16 a[46:47], v248 offset:58880\n"
    "ds_read_b64_tr_b16 a[48:49], v247 offset:61440\n"
    "v_mul_f32 v164, v164, v34\n"
    "v_mul_f32 v165, v165, v34\n"
    "s_waitcnt lgkmcnt(14)\n"
    "v_mfma_f32_16x16x32_bf16 v[16:19], a[24:27], a[180:183], v[16:19]\n"
    "ds_read_b64_tr_b16 a[50:51], v247 offset:62464\n"
    "ds_read_b64_tr_b16 a[82:83], v242 offset:12288\n"
    "ds_read_b64_tr_b16 a[52:53], v248 offset:61440\n"
    "v_mul_f32 v166, v166, v34\n"
    "v_mul_f32 v167, v167, v34\n"
    "v_mfma_f32_16x16x32_bf16 v[24:27], a[12:15], a[176:179], v[24:27]\n"
    "ds_read_b64_tr_b16 a[54:55], v248 offset:62464\n"
    "ds_read_b64_tr_b16 a[56:57], v247 offset:61952\n"
    "ds_read_b64_tr_b16 a[96:97], v242 offset:16384\n"
    "v_mul_f32 v180, v180, v34\n"
    "s_waitcnt lgkmcnt(15)\n"
    "v_mfma_f32_16x16x32_bf16 v[24:27], a[28:31], a[180:183], v[24:27]\n"
    "ds_read_b64_tr_b16 a[58:59], v247 offset:62976\n"
    "ds_read_b64_tr_b16 a[60:61], v248 offset:61952\n"
    "ds_read_b64_tr_b16 a[62:63], v248 offset:62976\n"
    "v_mul_f32 v181, v181, v34\n"
    "v_mfma_f32_16x16x32_bf16 v[0:3], a[32:35], a[184:187], v[0:3]\n"
    "ds_read_b64_tr_b16 a[98:99], v242 offset:20480\n"
    "ds_read_b64_tr_b16 a[0:1], v244 offset:0\n"
    "v_mul_f32 v182, v182, v34\n"
    "v_mul_f32 v183, v183, v34\n"
    "v_cvt_pk_bf16_f32 v164, v164, v165\n"
    "s_waitcnt lgkmcnt(10)\n"
    "v_mfma_f32_16x16x32_bf16 v[0:3], a[48:51], a[188:191], v[0:3]\n"
    "ds_read_b64_tr_b16 a[2:3], v244 offset:1024\n"
    "ds_read_b64_tr_b16 a[4:5], v250 offset:0\n"
    "v_cvt_pk_bf16_f32 v165, v166, v167\n"
    "v_cvt_pk_bf16_f32 v166, v180, v181\n"
    "v_mfma_f32_16x16x32_bf16 v[8:11], a[36:39], a[184:187], v[8:11]\n"
    "ds_read_b64_tr_b16 a[112:113], v242 offset:24576\n"
    "ds_read_b64_tr_b16 a[6:7], v250 offset:1024\n"
    "ds_read_b64_tr_b16 a[8:9], v244 offset:512\n"
    "v_cvt_pk_bf16_f32 v167, v182, v183\n"
    "s_waitcnt lgkmcnt(12)\n"
    "v_mfma_f32_16x16x32_bf16 v[8:11], a[52:55], a[188:191], v[8:11]\n"
    "buffer_store_dwordx4 v[164:167], v240, s[56:59], s65 offen offset:2048 sc1\n"
    "ds_read_b64_tr_b16 a[10:11], v244 offset:1536\n"
    "ds_read_b64_tr_b16 a[114:115], v242 offset:28672\n"
    "v_mul_f32 v196, v196, v34\n"
    "v_mfma_f32_16x16x32_bf16 v[16:19], a[40:43], a[184:187], v[16:19]\n"
    "ds_read_b64_tr_b16 a[12:13], v250 offset:512\n"
    "ds_read_b64_tr_b16 a[14:15], v250 offset:1536\n"
    "ds_read_b64_tr_b16 a[16:17], v244 offset:4096\n"
    "v_mul_f32 v197, v197, v34\n"
    "s_waitcnt lgkmcnt(14)\n"
    "v_mfma_f32_16x16x32_bf16 v[16:19], a[56:59], a[188:191], v[16:19]\n"
    "ds_read_b64_tr_b16 a[68:69], v243 offset:0\n"
    "ds_read_b64_tr_b16 a[18:19], v244 offset:5120\n"
    "v_mul_f32 v198, v198, v34\n"
    "v_mul_f32 v199, v199, v34\n"
    "v_mfma_f32_16x16x32_bf16 v[24:27], a[44:47], a[184:187], v[24:27]\n"
    "ds_read_b64_tr_b16 a[20:21], v250 offset:4096\n"
    "ds_read_b64_tr_b16 a[22:23], v250 offset:5120\n"
    "ds_read_b64_tr_b16 a[70:71], v243 offset:4096\n"
    "v_mul_f32 v212, v212, v34\n"
    "v_mul_f32 v213, v213, v34\n"
    "s_waitcnt lgkmcnt(15)\n"
    "v_mfma_f32_16x16x32_bf16 v[24:27], a[60:63], a[188:191], v[24:27]\n"
    "ds_read_b64_tr_b16 a[24:25], v244 offset:4608\n"
    "ds_read_b64_tr_b16 a[26:27], v244 offset:5632\n"
    "ds_read_b64_tr_b16 a[28:29], v250 offset:4608\n"
    "ds_read_b64_tr_b16 a[30:31], v250 offset:5632\n"
    "s_waitcnt lgkmcnt(15)\n"
    "v_mfma_f32_16x16x32_bf16 v[0:3], a[0:3], a[212:215], v[0:3]\n"
    "ds_read_b64_tr_b16 a[84:85], v243 offset:8192\n"
    "v_mul_f32 v214, v214, v34\n"
    "v_mul_f32 v215, v215, v34\n"
    "v_cvt_pk_bf16_f32 v196, v196, v197\n"
    "v_cvt_pk_bf16_f32 v197, v198, v199\n"
    "s_waitcnt lgkmcnt(8)\n"
    "v_mfma_f32_16x16x32_bf16 v[0:3], a[16:19], a[216:219], v[0:3]\n"
    "ds_read_b64_tr_b16 a[32:33], v244 offset:8192\n"
    "ds_read_b64_tr_b16 a[34:35], v244 offset:9216\n"
    "ds_read_b64_tr_b16 a[86:87], v243 offset:12288\n"
    "v_cvt_pk_bf16_f32 v198, v212, v213\n"
    "v_mfma_f32_16x16x32_bf16 v[8:11], a[4:7], a[212:215], v[8:11]\n"
    "ds_read_b64_tr_b16 a[36:37], v250 offset:8192\n"
    "ds_read_b64_tr_b16 a[38:39], v250 offset:9216\n"
    "ds_read_b64_tr_b16 a[40:41], v244 offset:8704\n"
    "v_cvt_pk_bf16_f32 v199, v214, v215\n"
    "s_waitcnt lgkmcnt(12)\n"
    "v_mfma_f32_16x16x32_bf16 v[8:11], a[20:23], a[216:219], v[8:11]\n"
    "buffer_store_dwordx4 v[196:199], v240, s[56:59], s65 offen offset:3072 sc1\n"
    "ds_read_b64_tr_b16 a[100:101], v243 offset:16384\n"
    "s_add_u32 s65, s78, 393216\n"
    "ds_read_b64_tr_b16 a[42:43], v244 offset:9728\n"
    "v_mfma_f32_16x16x32_bf16 v[16:19], a[8:11], a[212:215], v[16:19]\n"
    "ds_read_b64_tr_b16 a[44:45], v250 offset:8704\n"
    "ds_read_b64_tr_b16 a[46:47], v250 offset:9728\n"
    "v_mul_f32 v228, v228, v35\n"
    "v_mul_f32 v229, v229, v35\n"
    "s_waitcnt lgkmcnt(13)\n"
    "v_mfma_f32_16x16x32_bf16 v[16:19], a[24:27], a[216:219], v[16:19]\n"
    "ds_read_b64_tr_b16 a[102:103], v243 offset:20480\n"
    "ds_read_b64_tr_b16 a[48:49], v244 offset:12288\n"
    "v_mul_f32 v230, v230, v35\n"
    "v_mul_f32 v231, v231, v35\n"
    "v_mfma_f32_16x16x32_bf16 v[24:27], a[12:15], a[212:215], v[24:27]\n"
    "ds_read_b64_tr_b16 a[50:51], v244 offset:13312\n"
    "ds_read_b64_tr_b16 a[52:53], v250 offset:12288\n"
    "ds_read_b64_tr_b16 a[116:117], v243 offset:24576\n"
    "v_mul_f32 v236, v236, v35\n"
    "v_mul_f32 v237, v237, v35\n"
    "s_waitcnt lgkmcnt(15)\n"
    "v_mfma_f32_16x16x32_bf16 v[24:27], a[28:31], a[216:219], v[24:27]\n"
    "ds_read_b64_tr_b16 a[54:55], v250 offset:13312\n"
    "ds_read_b64_tr_b16 a[56:57], v244 offset:12800\n"
    "ds_read_b64_tr_b16 a[58:59], v244 offset:13824\n"
    "ds_read_b64_tr_b16 a[60:61], v250 offset:12800\n"
    "ds_read_b64_tr_b16 a[62:63], v250 offset:13824\n"
    "s_waitcnt lgkmcnt(15)\n"
    "v_mfma_f32_16x16x32_bf16 v[0:3], a[32:35], a[220:223], v[0:3]\n"
    "ds_read_b64_tr_b16 a[118:119], v243 offset:28672\n"
    "v_mul_f32 v238, v238, v35\n"
    "v_mul_f32 v239, v239, v35\n"
    "v_cvt_pk_bf16_f32 v228, v228, v229\n"
    "s_waitcnt lgkmcnt(8)\n"
    "v_mfma_f32_16x16x32_bf16 v[0:3], a[48:51], a[224:227], v[0:3]\n"
    "ds_read_b64_tr_b16 a[0:1], v244 offset:16384\n"
    "ds_read_b64_tr_b16 a[72:73], v242 offset:512\n"
    "ds_read_b64_tr_b16 a[2:3], v244 offset:17408\n"
    "v_cvt_pk_bf16_f32 v229, v230, v231\n"
    "v_cvt_pk_bf16_f32 v230, v236, v237\n"
    "v_mfma_f32_16x16x32_bf16 v[8:11], a[36:39], a[220:223], v[8:11]\n"
    "ds_read_b64_tr_b16 a[4:5], v250 offset:16384\n"
    "ds_read_b64_tr_b16 a[6:7], v250 offset:17408\n"
    "ds_read_b64_tr_b16 a[74:75], v242 offset:4608\n"
    "v_cvt_pk_bf16_f32 v231, v238, v239\n"
    "s_waitcnt lgkmcnt(11)\n"
    "v_mfma_f32_16x16x32_bf16 v[8:11], a[52:55], a[224:227], v[8:11]\n"
    "buffer_store_dwordx4 v[228:231], v240, s[56:59], s65 offen offset:0 sc1\n"
    "ds_read_b64_tr_b16 a[8:9], v244 offset:16896\n"
    "v_mul_f32 v140, v140, v35\n"
    "v_mfma_f32_16x16x32_bf16 v[16:19], a[40:43], a[220:223], v[16:19]\n"
    "ds_read_b64_tr_b16 a[10:11], v244 offset:17920\n"
    "ds_read_b64_tr_b16 a[12:13], v250 offset:16896\n"
    "ds_read_b64_tr_b16 a[88:89], v242 offset:8704\n"
    "v_mul_f32 v141, v141, v35\n"
    "v_mul_f32 v142, v142, v35\n"
    "s_waitcnt lgkmcnt(13)\n"
    "v_mfma_f32_16x16x32_bf16 v[16:19], a[56:59], a[224:227], v[16:19]\n"
    "ds_read_b64_tr_b16 a[14:15], v250 offset:17920\n"
    "ds_read_b64_tr_b16 a[16:17], v244 offset:20480\n"
    "v_mul_f32 v143, v143, v35\n"
    "v_mul_f32 v156, v156, v35\n"
    "v_mfma_f32_16x16x32_bf16 v[24:27], a[44:47], a[220:223], v[24:27]\n"
    "ds_read_b64_tr_b16 a[18:19], v244 offset:21504\n"
    "ds_read_b64_tr_b16 a[90:91], v242 offset:12800\n"
    "ds_read_b64_tr_b16 a[20:21], v250 offset:20480\n"
    "v_mul_f32 v157, v157, v35\n"
    "s_waitcnt lgkmcnt(15)\n"
    "v_mfma_f32_16x16x32_bf16 v[24:27], a[60:63], a[224:227], v[24:27]\n"
    "ds_read_b64_tr_b16 a[22:23], v250 offset:21504\n"
    "ds_read_b64_tr_b16 a[24:25], v244 offset:20992\n"
    "ds_read_b64_tr_b16 a[26:27], v244 offset:22016\n"
    "ds_read_b64_tr_b16 a[28:29], v250 offset:20992\n"
    "ds_read_b64_tr_b16 a[30:31], v250 offset:22016\n"
    "s_waitcnt lgkmcnt(15)\n"
    "v_mfma_f32_16x16x32_bf16 v[0:3], a[0:3], a[228:231], v[0:3]\n"
    "ds_read_b64_tr_b16 a[104:105], v242 offset:16896\n"
    "v_mul_f32 v158, v158, v35\n"
    "v_mul_f32 v159, v159, v35\n"
    "v_cvt_pk_bf16_f32 v140, v140, v141\n"
    "s_waitcnt lgkmcnt(8)\n"
    "v_mfma_f32_16x16x32_bf16 v[0:3], a[16:19], a[232:235], v[0:3]\n"
    "ds_read_b64_tr_b16 a[106:107], v242 offset:20992\n"
    "ds_read_b64_tr_b16 a[32:33], v244 offset:24576\n"
    "v_cvt_pk_bf16_f32 v141, v142, v143\n"
    "v_cvt_pk_bf16_f32 v142, v156, v157\n"
    "v_cvt_pk_bf16_f32 v143, v158, v159\n"
    "v_mfma_f32_16x16x32_bf16 v[8:11], a[4:7], a[228:231], v[8:11]\n"
    "buffer_store_dwordx4 v[140:143], v240, s[56:59], s65 offen offset:1024 sc1\n"
    "ds_read_b64_tr_b16 a[34:35], v244 offset:25600\n"
    "ds_read_b64_tr_b16 a[36:37], v250 offset:24576\n"
    "s_waitcnt lgkmcnt(9)\n"
    "v_mfma_f32_16x16x32_bf16 v[8:11], a[20:23], a[232:235], v[8:11]\n"
    "ds_read_b64_tr_b16 a[120:121], v242 offset:25088\n"
    "ds_read_b64_tr_b16 a[38:39], v250 offset:25600\n"
    "v_mul_f32 v172, v172, v35\n"
    "v_mul_f32 v173, v173, v35\n"
    "v_mfma_f32_16x16x32_bf16 v[16:19], a[8:11], a[228:231], v[16:19]\n"
    "ds_read_b64_tr_b16 a[40:41], v244 offset:25088\n"
    "ds_read_b64_tr_b16 a[42:43], v244 offset:26112\n"
    "ds_read_b64_tr_b16 a[122:123], v242 offset:29184\n"
    "v_mul_f32 v174, v174, v35\n"
    "v_mul_f32 v175, v175, v35\n"
    "s_waitcnt lgkmcnt(12)\n"
    "v_mfma_f32_16x16x32_bf16 v[16:19], a[24:27], a[232:235], v[16:19]\n"
    "ds_read_b64_tr_b16 a[44:45], v250 offset:25088\n"
    "ds_read_b64_tr_b16 a[46:47], v250 offset:26112\n"
    "ds_read_b64_tr_b16 a[48:49], v244 offset:28672\n"
    "v_mul_f32 v188, v188, v35\n"
    "v_mfma_f32_16x16x32_bf16 v[24:27], a[12:15], a[228:231], v[24:27]\n"
    "ds_read_b64_tr_b16 a[76:77], v243 offset:512\n"
    "ds_read_b64_tr_b16 a[50:51], v244 offset:29696\n"
    "v_mul_f32 v189, v189, v35\n"
    "v_mul_f32 v190, v190, v35\n"
    "s_waitcnt lgkmcnt(15)\n"
    "v_mfma_f32_16x16x32_bf16 v[24:27], a[28:31], a[232:235], v[24:27]\n"
    "ds_read_b64_tr_b16 a[52:53], v250 offset:28672\n"
    "ds_read_b64_tr_b16 a[54:55], v250 offset:29696\n"
    "ds_read_b64_tr_b16 a[56:57], v244 offset:29184\n"
    "ds_read_b64_tr_b16 a[58:59], v244 offset:30208\n"
    "ds_read_b64_tr_b16 a[60:61], v250 offset:29184\n"
    "ds_read_b64_tr_b16 a[62:63], v250 offset:30208\n"
    "s_waitcnt lgkmcnt(15)\n"
    "v_mfma_f32_16x16x32_bf16 v[0:3], a[32:35], a[236:239], v[0:3]\n"
    "ds_read_b64_tr_b16 a[78:79], v243 offset:4608\n"
    "v_mul_f32 v191, v191, v35\n"
    "v_cvt_pk_bf16_f32 v172, v172, v173\n"
    "v_cvt_pk_bf16_f32 v173, v174, v175\n"
    "s_waitcnt lgkmcnt(7)\n"
    "v_mfma_f32_16x16x32_bf16 v[0:3], a[48:51], a[240:243], v[0:3]\n"
    "ds_read_b64_tr_b16 a[92:93], v243 offset:8704\n"
    "ds_read_b64_tr_b16 a[94:95], v243 offset:12800\n"
    "v_cvt_pk_bf16_f32 v174, v188, v189\n"
    "v_cvt_pk_bf16_f32 v175, v190, v191\n"
    "v_mfma_f32_16x16x32_bf16 v[8:11], a[36:39], a[236:239], v[8:11]\n"
    "buffer_store_dwordx4 v[172:175], v240, s[56:59], s65 offen offset:2048 sc1\n"
    "v_mul_f32 v204, v204, v35\n"
    "v_mul_f32 v205, v205, v35\n"
    "v_mul_f32 v206, v206, v35\n"
    "s_waitcnt lgkmcnt(7)\n"
    "v_mfma_f32_16x16x32_bf16 v[8:11], a[52:55], a[240:243], v[8:11]\n"
    "ds_read_b64_tr_b16 a[108:109], v243 offset:16896\n"
    "v_mul_f32 v207, v207, v35\n"
    "v_mul_f32 v220, v220, v35\n"
    "v_mul_f32 v221, v221, v35\n"
    "v_mfma_f32_16x16x32_bf16 v[16:19], a[40:43], a[236:239], v[16:19]\n"
    "ds_read_b64_tr_b16 a[110:111], v243 offset:20992\n"
    "v_mul_f32 v222, v222, v35\n"
    "v_mul_f32 v223, v223, v35\n"
    "v_cvt_pk_bf16_f32 v204, v204, v205\n"
    "s_waitcnt lgkmcnt(7)\n"
    "v_mfma_f32_16x16x32_bf16 v[16:19], a[56:59], a[240:243], v[16:19]\n"
    "ds_read_b64_tr_b16 a[124:125], v243 offset:25088\n"
    "ds_read_b64_tr_b16 a[126:127], v243 offset:29184\n"
    "v_cvt_pk_bf16_f32 v205, v206, v207\n"
    "v_cvt_pk_bf16_f32 v206, v220, v221\n"
    "v_cvt_pk_bf16_f32 v207, v222, v223\n"
    "v_mfma_f32_16x16x32_bf16 v[24:27], a[44:47], a[236:239], v[24:27]\n"
    "buffer_store_dwordx4 v[204:207], v240, s[56:59], s65 offen offset:3072 sc1\n"
    "s_waitcnt lgkmcnt(7)\n"
    "v_mfma_f32_16x16x32_bf16 v[24:27], a[60:63], a[240:243], v[24:27]\n"
    "v_mfma_f32_16x16x32_bf16 v[40:43], a[64:67], v[28:31], 0\n"
    "ds_read_b64_tr_b16 a[0:1], v242 offset:1024\n"
    "s_lshl_b32 s63, s62, 5\n"
    "v_mfma_f32_16x16x32_bf16 v[40:43], a[80:83], v[128:131], v[40:43]\n"
    "s_lshl_b32 s90, s99, 17\n"
    "ds_read_b64_tr_b16 a[2:3], v242 offset:5120\n"
    "v_mfma_f32_16x16x32_bf16 v[40:43], a[96:99], v[160:163], v[40:43]\n"
    "s_lshl_b32 s64, s81, 2\n"
    "s_add_u32 m0, s92, 32768\n"
    "v_mfma_f32_16x16x32_bf16 v[40:43], a[112:115], v[192:195], v[40:43]\n"
    "ds_read_b64_tr_b16 a[16:17], v242 offset:9216\n"
    "s_add_u32 s63, s63, s64\n"
    "v_mfma_f32_16x16x32_bf16 v[48:51], a[68:71], v[28:31], 0\n"
    "buffer_load_dwordx4 v245, s[52:55], s90 offen lds\n"
    "v_mfma_f32_16x16x32_bf16 v[48:51], a[84:87], v[128:131], v[48:51]\n"
    "ds_read_b64_tr_b16 a[18:19], v242 offset:13312\n"
    "s_lshl_b32 s63, s63, 7\n"
    "v_mfma_f32_16x16x32_bf16 v[48:51], a[100:103], v[160:163], v[48:51]\n"
    "ds_read_b64_tr_b16 a[32:33], v242 offset:17408\n"
    "s_add_u32 s63, s63, 36864\n"
    "v_mfma_f32_16x16x32_bf16 v[48:51], a[116:119], v[192:195], v[48:51]\n"
    "ds_read_b64_tr_b16 a[34:35], v242 offset:21504\n"
    "ds_read_b64_tr_b16 a[48:49], v242 offset:25600\n"
    "v_mfma_f32_16x16x32_bf16 v[56:59], a[72:75], v[28:31], 0\n"
    "buffer_load_dwordx4 v246, s[52:55], s90 offen offset:1024 lds\n"
    "v_mfma_f32_16x16x32_bf16 v[56:59], a[88:91], v[128:131], v[56:59]\n"
    "ds_read_b64_tr_b16 a[50:51], v242 offset:29696\n"
    "v_and_b32 v241, 0x30, v240\n"
    "v_mfma_f32_16x16x32_bf16 v[56:59], a[104:107], v[160:163], v[56:59]\n"
    "ds_read_b64_tr_b16 a[4:5], v243 offset:1024\n"
    "v_lshlrev_b32 v241, 3, v241\n"
    "v_mfma_f32_16x16x32_bf16 v[56:59], a[120:123], v[192:195], v[56:59]\n"
    "s_add_u32 s91, s90, 6144\n"
    "v_add_u32 v241, s63, v241\n"
    "s_waitcnt lgkmcnt(15)\n"
    "v_mfma_f32_16x16x32_bf16 v[64:67], a[76:79], v[28:31], 0\n"
    "ds_read_b64_tr_b16 a[6:7], v243 offset:5120\n"
    "s_lshl_b32 s63, s62, 21\n"
    "s_waitcnt lgkmcnt(14)\n"
    "v_mfma_f32_16x16x32_bf16 v[64:67], a[92:95], v[128:131], v[64:67]\n"
    "ds_read_b64_tr_b16 a[20:21], v243 offset:9216\n"
    "s_waitcnt lgkmcnt(13)\n"
    "v_mfma_f32_16x16x32_bf16 v[64:67], a[108:111], v[160:163], v[64:67]\n"
    "buffer_load_dwordx4 v245, s[52:55], s91 offen offset:2048 lds\n"
    "s_waitcnt lgkmcnt(11)\n"
    "v_mfma_f32_16x16x32_bf16 v[64:67], a[124:127], v[192:195], v[64:67]\n"
    "ds_read_b64_tr_b16 a[22:23], v243 offset:13312\n"
    "s_lshl_b32 s64, s60, 19\n"
    "v_mfma_f32_16x16x32_bf16 v[104:107], a[64:67], v[60:63], 0\n"
    "ds_read_b64_tr_b16 a[36:37], v243 offset:17408\n"
    "s_add_u32 s63, s63, s64\n"
    "v_mfma_f32_16x16x32_bf16 v[104:107], a[80:83], v[136:139], v[104:107]\n"
    "ds_read_b64_tr_b16 a[38:39], v243 offset:21504\n"
    "v_mfma_f32_16x16x32_bf16 v[104:107], a[96:99], v[168:171], v[104:107]\n"
    "buffer_load_dwordx4 v246, s[52:55], s91 offen offset:3072 lds\n"
    "v_mfma_f32_16x16x32_bf16 v[104:107], a[112:115], v[200:203], v[104:107]\n"
    "s_lshl_b32 s64, s81, 14\n"
    "ds_read_b64_tr_b16 a[52:53], v243 offset:25600\n"
    "v_mfma_f32_16x16x32_bf16 v[112:115], a[68:71], v[60:63], 0\n"
    "s_add_u32 s63, s63, s64\n"
    "ds_read_b64_tr_b16 a[54:55], v243 offset:29696\n"
    "v_mfma_f32_16x16x32_bf16 v[112:115], a[84:87], v[136:139], v[112:115]\n"
    "ds_read_b64_tr_b16 a[8:9], v242 offset:1536\n"
    "s_add_u32 s63, s63, 413696\n"
    "v_mfma_f32_16x16x32_bf16 v[112:115], a[100:103], v[168:171], v[112:115]\n"
    "s_add_u32 s90, s90, 16384\n"
    "v_mfma_f32_16x16x32_bf16 v[112:115], a[116:119], v[200:203], v[112:115]\n"
    "ds_read_b64_tr_b16 a[10:11], v242 offset:5632\n"
    "v_mfma_f32_16x16x32_bf16 v[120:123], a[72:75], v[60:63], 0\n"
    "s_add_u32 s44, s26, s63\n"
    "s_addc_u32 s45, s27, 0\n"
    "v_mfma_f32_16x16x32_bf16 v[120:123], a[88:91], v[136:139], v[120:123]\n"
    "s_add_u32 m0, s92, 36864\n"
    "ds_read_b64_tr_b16 a[24:25], v242 offset:9728\n"
    "v_mfma_f32_16x16x32_bf16 v[120:123], a[104:107], v[168:171], v[120:123]\n"
    "buffer_load_dwordx4 v245, s[52:55], s90 offen lds\n"
    "v_mfma_f32_16x16x32_bf16 v[120:123], a[120:123], v[200:203], v[120:123]\n"
    "ds_read_b64_tr_b16 a[26:27], v242 offset:13824\n"
    "s_and_b32 s45, s45, 0xffff\n"
    "v_mfma_f32_16x16x32_bf16 v[4:7], a[76:79], v[60:63], 0\n"
    "ds_read_b64_tr_b16 a[40:41], v242 offset:17920\n"
    "v_mfma_f32_16x16x32_bf16 v[4:7], a[92:95], v[136:139], v[4:7]\n"
    "s_sub_u32 s46, 41832448, s63\n"
    "ds_read_b64_tr_b16 a[42:43], v242 offset:22016\n"
    "v_mfma_f32_16x16x32_bf16 v[4:7], a[108:111], v[168:171], v[4:7]\n"
    "buffer_load_dwordx4 v246, s[52:55], s90 offen offset:1024 lds\n"
    "v_mfma_f32_16x16x32_bf16 v[4:7], a[124:127], v[200:203], v[4:7]\n"
    "ds_read_b64_tr_b16 a[56:57], v242 offset:26112\n"
    "s_mov_b32 s47, 0x20000\n"
    "v_mfma_f32_16x16x32_bf16 v[76:79], a[64:67], v[224:227], 0\n"
    "ds_read_b64_tr_b16 a[58:59], v242 offset:30208\n"
    "v_mfma_f32_16x16x32_bf16 v[76:79], a[80:83], v[132:135], v[76:79]\n"
    "s_lshl_b32 s63, s62, 12\n"
    "ds_read_b64_tr_b16 a[12:13], v243 offset:1536\n"
    "v_mfma_f32_16x16x32_bf16 v[76:79], a[96:99], v[164:167], v[76:79]\n"
    "s_lshl_b32 s64, s61, 7\n"
    "v_mfma_f32_16x16x32_bf16 v[76:79], a[112:115], v[196:199], v[76:79]\n"
    "s_add_u32 s91, s90, 6144\n"
    "ds_read_b64_tr_b16 a[14:15], v243 offset:5632\n"
    "v_mfma_f32_16x16x32_bf16 v[100:103], a[68:71], v[224:227], 0\n"
    "s_add_u32 s63, s63, s64\n"
    "ds_read_b64_tr_b16 a[28:29], v243 offset:9728\n"
    "v_mfma_f32_16x16x32_bf16 v[100:103], a[84:87], v[132:135], v[100:103]\n"
    "buffer_load_dwordx4 v245, s[52:55], s91 offen offset:2048 lds\n"
    "v_mfma_f32_16x16x32_bf16 v[100:103], a[100:103], v[164:167], v[100:103]\n"
    "ds_read_b64_tr_b16 a[30:31], v243 offset:13824\n"
    "s_add_u32 s63, s63, 36864\n"
    "v_mfma_f32_16x16x32_bf16 v[100:103], a[116:119], v[196:199], v[100:103]\n"
    "ds_read_b64_tr_b16 a[44:45], v243 offset:17920\n"
    "v_mfma_f32_16x16x32_bf16 v[108:111], a[72:75], v[224:227], 0\n"
    "v_mov_b32 v252, s68\n"
    "v_mfma_f32_16x16x32_bf16 v[108:111], a[88:91], v[132:135], v[108:111]\n"
    "ds_read_b64_tr_b16 a[46:47], v243 offset:22016\n"
    "v_mfma_f32_16x16x32_bf16 v[108:111], a[104:107], v[164:167], v[108:111]\n"
    "buffer_load_dwordx4 v246, s[52:55], s91 offen offset:3072 lds\n"
    "v_mfma_f32_16x16x32_bf16 v[108:111], a[120:123], v[196:199], v[108:111]\n"
    "ds_read_b64_tr_b16 a[60:61], v243 offset:26112\n"
    "v_mov_b32 v253, s69\n"
    "v_mfma_f32_16x16x32_bf16 v[20:23], a[76:79], v[224:227], 0\n"
    "ds_read_b64_tr_b16 a[62:63], v243 offset:30208\n"
    "v_mov_b32 v254, 0\n"
    "v_mfma_f32_16x16x32_bf16 v[20:23], a[92:95], v[132:135], v[20:23]\n"
    "v_mfma_f32_16x16x32_bf16 v[20:23], a[108:111], v[164:167], v[20:23]\n"
    "v_mfma_f32_16x16x32_bf16 v[20:23], a[124:127], v[196:199], v[20:23]\n"
    "s_waitcnt vmcnt(8)\n"
    "s_waitcnt lgkmcnt(0)\n"
    "s_barrier\n"
    "s_cmp_eq_u32 s60, 0\n"
    "s_cbranch_scc0 LP36864\n"
    "s_mov_b64 exec, 1\n"
    "s_nop 0\n"
    "buffer_store_dwordx2 v[252:253], v254, s[56:59], s63 offen sc1\n"
    "s_mov_b64 exec, -1\n"
    "LP36864:\n"
    "s_nop 0\n"
    "v_mfma_f32_16x16x32_bf16 v[144:147], a[64:67], v[228:231], 0\n"
    "s_lshl_b32 s90, s76, 17\n"
    "s_add_u32 m0, s92, 65536\n"
    "v_mov_b32 v252, s68\n"
    "v_mfma_f32_16x16x32_bf16 v[144:147], a[80:83], v[140:143], v[144:147]\n"
    "buffer_load_dwordx4 v245, s[52:55], s90 offen lds\n"
    "v_mov_b32 v253, s69\n"
    "v_mfma_f32_16x16x32_bf16 v[144:147], a[96:99], v[172:175], v[144:147]\n"
    "buffer_load_dwordx4 v246, s[52:55], s90 offen offset:1024 lds\n"
    "s_add_u32 s91, s90, 6144\n"
    "v_mov_b32 v254, 0\n"
    "v_mfma_f32_16x16x32_bf16 v[144:147], a[112:115], v[204:207], v[144:147]\n"
    "s_or_b32 s63, %1, s60\n"
    "buffer_load_dwordx4 v245, s[52:55], s91 offen offset:2048 lds\n"
    "v_mfma_f32_16x16x32_bf16 v[176:179], a[68:71], v[228:231], 0\n"
    "s_cmp_eq_u32 s63, 0\n"
    "s_cbranch_scc0 LEPB\n"
    "s_mov_b64 exec, 1\n"
    "s_nop 0\n"
    "buffer_store_dwordx2 v[252:253], v254, s[56:59], 0 offen sc1\n"
    "s_mov_b64 exec, -1\n"
    "LEPB:\n"
    "s_nop 0\n"
    "v_mfma_f32_16x16x32_bf16 v[176:179], a[84:87], v[140:143], v[176:179]\n"
    "buffer_load_dwordx4 v246, s[52:55], s91 offen offset:3072 lds\n"
    "s_add_u32 s90, s90, 16384\n"
    "v_mfma_f32_16x16x32_bf16 v[176:179], a[100:103], v[172:175], v[176:179]\n"
    "s_add_u32 m0, s92, 69632\n"
    "s_nop 0\n"
    "buffer_load_dwordx4 v245, s[52:55], s90 offen lds\n"
    "s_add_u32 s63, s96, s81\n"
    "v_mfma_f32_16x16x32_bf16 v[176:179], a[116:119], v[204:207], v[176:179]\n"
    "buffer_load_dwordx4 v246, s[52:55], s90 offen offset:1024 lds\n"
    "s_add_u32 s91, s90, 6144\n"
    "v_mfma_f32_16x16x32_bf16 v[208:211], a[72:75], v[228:231], 0\n"
    "buffer_load_dwordx4 v245, s[52:55], s91 offen offset:2048 lds\n"
    "s_lshl_b32 s93, s63, 16\n"
    "v_mfma_f32_16x16x32_bf16 v[208:211], a[88:91], v[140:143], v[208:211]\n"
    "buffer_load_dwordx4 v246, s[52:55], s91 offen offset:3072 lds\n"
    "s_lshl_b32 s90, s77, 17\n"
    "s_add_u32 m0, s92, 98304\n"
    "v_mfma_f32_16x16x32_bf16 v[208:211], a[104:107], v[172:175], v[208:211]\n"
    "buffer_load_dwordx4 v245, s[52:55], s90 offen lds\n"
    "s_lshl_b32 s64, s60, 14\n"
    "v_mfma_f32_16x16x32_bf16 v[208:211], a[120:123], v[204:207], v[208:211]\n"
    "buffer_load_dwordx4 v246, s[52:55], s90 offen offset:1024 lds\n"
    "s_add_u32 s93, s93, s64\n"
    "s_add_u32 s91, s90, 6144\n"
    "v_mfma_f32_16x16x32_bf16 v[124:127], a[76:79], v[228:231], 0\n"
    "s_add_u32 s93, s93, 22827008\n"
    "buffer_load_dwordx4 v245, s[52:55], s91 offen offset:2048 lds\n"
    "v_mfma_f32_16x16x32_bf16 v[124:127], a[92:95], v[140:143], v[124:127]\n"
    "s_add_u32 s94, s93, 8192\n"
    "buffer_load_dwordx4 v246, s[52:55], s91 offen offset:3072 lds\n"
    "s_add_u32 s28, s93, 4096\n"
    "v_mfma_f32_16x16x32_bf16 v[124:127], a[108:111], v[172:175], v[124:127]\n"
    "s_add_u32 s90, s90, 16384\n"
    "s_add_u32 m0, s92, 102400\n"
    "s_add_u32 s29, s93, 12288\n"
    "v_mfma_f32_16x16x32_bf16 v[124:127], a[124:127], v[204:207], v[124:127]\n"
    "buffer_load_dwordx4 v245, s[52:55], s90 offen lds\n"
    "s_lshl_b32 s63, s60, 1\n"
    "v_mfma_f32_16x16x32_bf16 v[72:75], a[0:3], v[28:31], 0\n"
    "buffer_load_dwordx4 v246, s[52:55], s90 offen offset:1024 lds\n"
    "s_xor_b32 s71, s63, s81\n"
    "s_add_u32 s91, s90, 6144\n"
    "v_mfma_f32_16x16x32_bf16 v[72:75], a[16:19], v[128:131], v[72:75]\n"
    "buffer_load_dwordx4 v245, s[52:55], s91 offen offset:2048 lds\n"
    "s_xor_b32 s72, s71, 1\n"
    "v_mfma_f32_16x16x32_bf16 v[72:75], a[32:35], v[160:163], v[72:75]\n"
    "buffer_load_dwordx4 v246, s[52:55], s91 offen offset:3072 lds\n"
    "s_add_u32 m0, s67, 131072\n"
    "v_add_u32 v249, 0, v240\n"
    "v_mfma_f32_16x16x32_bf16 v[72:75], a[48:51], v[192:195], v[72:75]\n"
    "s_add_u32 s87, s96, s81\n"
    "buffer_load_dwordx4 v251, s[36:39], s66 offen lds\n"
    "v_mfma_f32_16x16x32_bf16 v[80:83], a[4:7], v[28:31], 0\n"
    "s_lshl_b32 s87, s87, 13\n"
    "s_add_u32 s66, s66, 7168\n"
    "buffer_load_dwordx4 v251, s[36:39], s66 offen offset:1024 lds\n"
    "v_mfma_f32_16x16x32_bf16 v[80:83], a[20:23], v[128:131], v[80:83]\n"
    "s_lshl_b32 s64, s60, 11\n"
    "s_add_u32 s66, s66, 7168\n"
    "s_add_u32 s87, s87, s64\n"
    "v_mfma_f32_16x16x32_bf16 v[80:83], a[36:39], v[160:163], v[80:83]\n"
    "buffer_load_dwordx4 v251, s[36:39], s66 offen offset:2048 lds\n"
    "s_add_u32 s66, s66, 7168\n"
    "v_mfma_f32_16x16x32_bf16 v[80:83], a[52:55], v[192:195], v[80:83]\n"
    "s_add_u32 s87, s87, 39604224\n"
    "buffer_load_dwordx4 v251, s[36:39], s66 offen offset:3072 lds\n"
    "s_lshl_b32 s88, s96, 13\n"
    "v_mfma_f32_16x16x32_bf16 v[88:91], a[8:11], v[28:31], 0\n"
    "s_add_u32 s66, s66, 109568\n"
    "s_add_u32 m0, s67, 147456\n"
    "s_lshl_b32 s64, s81, 10\n"
    "v_mfma_f32_16x16x32_bf16 v[88:91], a[24:27], v[128:131], v[88:91]\n"
    "buffer_load_dwordx4 v251, s[36:39], s66 offen lds\n"
    "s_add_u32 s88, s88, s64\n"
    "s_add_u32 s66, s66, 7168\n"
    "v_mfma_f32_16x16x32_bf16 v[88:91], a[40:43], v[160:163], v[88:91]\n"
    "buffer_load_dwordx4 v251, s[36:39], s66 offen offset:1024 lds\n"
    "s_lshl_b32 s64, s60, 2\n"
    "v_mfma_f32_16x16x32_bf16 v[88:91], a[56:59], v[192:195], v[88:91]\n"
    "s_add_u32 s66, s66, 7168\n"
    "s_add_u32 s88, s88, s64\n"
    "buffer_load_dwordx4 v251, s[36:39], s66 offen offset:2048 lds\n"
    "v_mfma_f32_16x16x32_bf16 v[96:99], a[12:15], v[28:31], 0\n"
    "s_add_u32 s88, s88, 39604224\n"
    "s_add_u32 s66, s66, 7168\n"
    "s_lshl_b32 s63, s86, 14\n"
    "v_mfma_f32_16x16x32_bf16 v[96:99], a[28:31], v[128:131], v[96:99]\n"
    "buffer_load_dwordx4 v251, s[36:39], s66 offen offset:3072 lds\n"
    "s_add_u32 s66, s66, 109568\n"
    "v_mfma_f32_16x16x32_bf16 v[96:99], a[44:47], v[160:163], v[96:99]\n"
    "s_add_u32 m0, s67, 8192\n"
    "s_nop 0\n"
    "buffer_load_dwordx4 v251, s[36:39], s66 offen lds\n"
    "s_lshl_b32 s64, s80, 8\n"
    "v_mfma_f32_16x16x32_bf16 v[96:99], a[60:63], v[192:195], v[96:99]\n"
    "s_add_u32 s66, s66, 7168\n"
    "buffer_load_dwordx4 v251, s[36:39], s66 offen offset:1024 lds\n"
    "v_mfma_f32_16x16x32_bf16 v[12:15], a[0:3], v[60:63], 0\n"
    "s_add_u32 s66, s66, 7168\n"
    "buffer_load_dwordx4 v251, s[36:39], s66 offen offset:2048 lds\n"
    "s_add_u32 s63, s63, s64\n"
    "v_mfma_f32_16x16x32_bf16 v[12:15], a[16:19], v[136:139], v[12:15]\n"
    "s_add_u32 s66, s66, 7168\n"
    "buffer_load_dwordx4 v251, s[36:39], s66 offen offset:3072 lds\n"
    "s_add_u32 s66, s66, 109568\n"
    "buffer_load_dwordx2 v[252:253], v241, s[56:59], 0 offen sc1\n"
    "v_mfma_f32_16x16x32_bf16 v[12:15], a[32:35], v[168:171], v[12:15]\n"
    "s_and_b32 s64, s81, 1\n"
    "s_lshl_b32 s64, s64, 7\n"
    "s_add_u32 s63, s63, s64\n"
    "v_mfma_f32_16x16x32_bf16 v[12:15], a[48:51], v[200:203], v[12:15]\n"
    "s_add_u32 s48, s8, s63\n"
    "s_addc_u32 s49, s9, 0\n"
    "s_and_b32 s49, s49, 0xffff\n"
    "v_mfma_f32_16x16x32_bf16 v[36:39], a[4:7], v[60:63], 0\n"
    "s_sub_u32 s50, 2097152, s63\n"
    "s_mov_b32 s51, 0x20000\n"
    "v_mfma_f32_16x16x32_bf16 v[36:39], a[20:23], v[136:139], v[36:39]\n"
    "s_add_u32 s12, s24, s63\n"
    "s_addc_u32 s13, s25, 0\n"
    "s_and_b32 s13, s13, 0xffff\n"
    "s_sub_u32 s14, 2097152, s63\n"
    "v_mfma_f32_16x16x32_bf16 v[36:39], a[36:39], v[168:171], v[36:39]\n"
    "s_mov_b32 s15, 0x20000\n"
    "v_mfma_f32_16x16x32_bf16 v[36:39], a[52:55], v[200:203], v[36:39]\n"
    "s_waitcnt vmcnt(29)\n"
    "s_barrier\n"
    "s_waitcnt vmcnt(0)\n"
    "v_cmp_ne_u64 vcc, s[68:69], v[252:253]\n"
    "s_nop 0\n"
    "s_cbranch_vccnz LSPO\n"
    "LSPO_r:\n"
    "v_mfma_f32_16x16x32_bf16 v[44:47], a[8:11], v[60:63], 0\n"
    "s_lshl_b32 s84, s99, 12\n"
    "s_add_u32 s85, s84, 131072\n"
    "v_mfma_f32_16x16x32_bf16 v[44:47], a[24:27], v[136:139], v[44:47]\n"
    "s_add_u32 s73, s84, 262144\n"
    "s_add_u32 s98, s84, 393216\n"
    "v_mfma_f32_16x16x32_bf16 v[44:47], a[40:43], v[168:171], v[44:47]\n"
    "buffer_load_dwordx4 a[128:131], v240, s[44:47], s84 offen offset:0 sc1\n"
    "v_mfma_f32_16x16x32_bf16 v[44:47], a[56:59], v[200:203], v[44:47]\n"
    "buffer_load_dwordx4 a[132:135], v240, s[44:47], s84 offen offset:1024 sc1\n"
    "v_mfma_f32_16x16x32_bf16 v[68:71], a[12:15], v[60:63], 0\n"
    "buffer_load_dwordx4 a[136:139], v240, s[44:47], s84 offen offset:2048 sc1\n"
    "v_mfma_f32_16x16x32_bf16 v[68:71], a[28:31], v[136:139], v[68:71]\n"
    "buffer_load_dwordx4 a[140:143], v240, s[44:47], s84 offen offset:3072 sc1\n"
    "v_mfma_f32_16x16x32_bf16 v[68:71], a[44:47], v[168:171], v[68:71]\n"
    "buffer_load_dwordx4 a[144:147], v240, s[44:47], s85 offen offset:0 sc1\n"
    "v_mfma_f32_16x16x32_bf16 v[68:71], a[60:63], v[200:203], v[68:71]\n"
    "buffer_load_dwordx4 a[148:151], v240, s[44:47], s85 offen offset:1024 sc1\n"
    "v_mfma_f32_16x16x32_bf16 v[52:55], a[0:3], v[224:227], 0\n"
    "buffer_load_dwordx4 a[152:155], v240, s[44:47], s85 offen offset:2048 sc1\n"
    "v_mfma_f32_16x16x32_bf16 v[52:55], a[16:19], v[132:135], v[52:55]\n"
    "buffer_load_dwordx4 a[156:159], v240, s[44:47], s85 offen offset:3072 sc1\n"
    "v_mfma_f32_16x16x32_bf16 v[52:55], a[32:35], v[164:167], v[52:55]\n"
    "buffer_load_dwordx4 a[160:163], v240, s[44:47], s73 offen offset:0 sc1\n"
    "v_mfma_f32_16x16x32_bf16 v[52:55], a[48:51], v[196:199], v[52:55]\n"
    "buffer_load_dwordx4 a[164:167], v240, s[44:47], s73 offen offset:1024 sc1\n"
    "v_mfma_f32_16x16x32_bf16 v[84:87], a[4:7], v[224:227], 0\n"
    "buffer_load_dwordx4 a[168:171], v240, s[44:47], s73 offen offset:2048 sc1\n"
    "v_mfma_f32_16x16x32_bf16 v[84:87], a[20:23], v[132:135], v[84:87]\n"
    "buffer_load_dwordx4 a[172:175], v240, s[44:47], s73 offen offset:3072 sc1\n"
    "v_mfma_f32_16x16x32_bf16 v[84:87], a[36:39], v[164:167], v[84:87]\n"
    "buffer_load_dwordx4 a[176:179], v240, s[44:47], s98 offen offset:0 sc1\n"
    "v_mfma_f32_16x16x32_bf16 v[84:87], a[52:55], v[196:199], v[84:87]\n"
    "buffer_load_dwordx4 a[180:183], v240, s[44:47], s98 offen offset:1024 sc1\n"
    "v_mfma_f32_16x16x32_bf16 v[116:119], a[8:11], v[224:227], 0\n"
    "buffer_load_dwordx4 a[184:187], v240, s[44:47], s98 offen offset:2048 sc1\n"
    "v_mfma_f32_16x16x32_bf16 v[116:119], a[24:27], v[132:135], v[116:119]\n"
    "buffer_load_dwordx4 a[188:191], v240, s[44:47], s98 offen offset:3072 sc1\n"
    "v_mfma_f32_16x16x32_bf16 v[116:119], a[40:43], v[164:167], v[116:119]\n"
    "ds_read_b64_tr_b16 a[64:65], v242 offset:32768\n"
    "ds_read_b64_tr_b16 a[66:67], v242 offset:36864\n"
    "v_mfma_f32_16x16x32_bf16 v[116:119], a[56:59], v[196:199], v[116:119]\n"
    "ds_read_b64_tr_b16 a[80:81], v242 offset:40960\n"
    "ds_read_b64_tr_b16 a[82:83], v242 offset:45056\n"
    "v_mfma_f32_16x16x32_bf16 v[92:95], a[12:15], v[224:227], 0\n"
    "ds_read_b64_tr_b16 a[96:97], v242 offset:49152\n"
    "ds_read_b64_tr_b16 a[98:99], v242 offset:53248\n"
    "v_mfma_f32_16x16x32_bf16 v[92:95], a[28:31], v[132:135], v[92:95]\n"
    "ds_read_b64_tr_b16 a[112:113], v242 offset:57344\n"
    "ds_read_b64_tr_b16 a[114:115], v242 offset:61440\n"
    "v_mfma_f32_16x16x32_bf16 v[92:95], a[44:47], v[164:167], v[92:95]\n"
    "ds_read_b64_tr_b16 a[68:69], v243 offset:32768\n"
    "ds_read_b64_tr_b16 a[70:71], v243 offset:36864\n"
    "v_mfma_f32_16x16x32_bf16 v[92:95], a[60:63], v[196:199], v[92:95]\n"
    "ds_read_b64_tr_b16 a[84:85], v243 offset:40960\n"
    "ds_read_b64_tr_b16 a[86:87], v243 offset:45056\n"
    "v_mfma_f32_16x16x32_bf16 v[152:155], a[0:3], v[228:231], 0\n"
    "ds_read_b64_tr_b16 a[100:101], v243 offset:49152\n"
    "ds_read_b64_tr_b16 a[102:103], v243 offset:53248\n"
    "v_mfma_f32_16x16x32_bf16 v[152:155], a[16:19], v[140:143], v[152:155]\n"
    "ds_read_b64_tr_b16 a[116:117], v243 offset:57344\n"
    "ds_read_b64_tr_b16 a[118:119], v243 offset:61440\n"
    "v_mfma_f32_16x16x32_bf16 v[152:155], a[32:35], v[172:175], v[152:155]\n"
    "ds_read_b64_tr_b16 a[72:73], v242 offset:33280\n"
    "ds_read_b64_tr_b16 a[74:75], v242 offset:37376\n"
    "v_mfma_f32_16x16x32_bf16 v[152:155], a[48:51], v[204:207], v[152:155]\n"
    "ds_read_b64_tr_b16 a[88:89], v242 offset:41472\n"
    "ds_read_b64_tr_b16 a[90:91], v242 offset:45568\n"
    "v_mfma_f32_16x16x32_bf16 v[184:187], a[4:7], v[228:231], 0\n"
    "ds_read_b64_tr_b16 a[104:105], v242 offset:49664\n"
    "ds_read_b64_tr_b16 a[106:107], v242 offset:53760\n"
    "v_mfma_f32_16x16x32_bf16 v[184:187], a[20:23], v[140:143], v[184:187]\n"
    "ds_read_b64_tr_b16 a[120:121], v242 offset:57856\n"
    "ds_read_b64_tr_b16 a[122:123], v242 offset:61952\n"
    "v_mfma_f32_16x16x32_bf16 v[184:187], a[36:39], v[172:175], v[184:187]\n"
    "ds_read_b64_tr_b16 a[76:77], v243 offset:33280\n"
    "ds_read_b64_tr_b16 a[78:79], v243 offset:37376\n"
    "v_mfma_f32_16x16x32_bf16 v[184:187], a[52:55], v[204:207], v[184:187]\n"
    "ds_read_b64_tr_b16 a[92:93], v243 offset:41472\n"
    "ds_read_b64_tr_b16 a[94:95], v243 offset:45568\n"
    "v_mfma_f32_16x16x32_bf16 v[216:219], a[8:11], v[228:231], 0\n"
    "ds_read_b64_tr_b16 a[108:109], v243 offset:49664\n"
    "v_mfma_f32_16x16x32_bf16 v[216:219], a[24:27], v[140:143], v[216:219]\n"
    "v_mfma_f32_16x16x32_bf16 v[216:219], a[40:43], v[172:175], v[216:219]\n"
    "ds_read_b64_tr_b16 a[110:111], v243 offset:53760\n"
    "v_mfma_f32_16x16x32_bf16 v[216:219], a[56:59], v[204:207], v[216:219]\n"
    "ds_read_b64_tr_b16 a[124:125], v243 offset:57856\n"
    "v_mfma_f32_16x16x32_bf16 v[232:235], a[12:15], v[228:231], 0\n"
    "ds_read_b64_tr_b16 a[126:127], v243 offset:61952\n"
    "v_mfma_f32_16x16x32_bf16 v[232:235], a[28:31], v[140:143], v[232:235]\n"
    "v_mfma_f32_16x16x32_bf16 v[232:235], a[44:47], v[172:175], v[232:235]\n"
    "v_mfma_f32_16x16x32_bf16 v[232:235], a[60:63], v[204:207], v[232:235]\n"
    "s_waitcnt vmcnt(15) lgkmcnt(15)\n"
    "v_mfma_f32_16x16x32_bf16 v[40:43], a[64:67], a[128:131], v[40:43]\n"
    "ds_read_b64_tr_b16 a[0:1], v242 offset:33792\n"
    "s_lshl_b32 s84, s76, 12\n"
    "s_waitcnt vmcnt(14)\n"
    "v_mfma_f32_16x16x32_bf16 v[40:43], a[80:83], a[132:135], v[40:43]\n"
    "s_add_u32 s85, s84, 131072\n"
    "v_and_b32 v241, 0xf0, v240\n"
    "s_waitcnt vmcnt(13)\n"
    "v_mfma_f32_16x16x32_bf16 v[40:43], a[96:99], a[136:139], v[40:43]\n"
    "ds_read_b64_tr_b16 a[2:3], v242 offset:37888\n"
    "s_add_u32 s73, s84, 262144\n"
    "s_waitcnt vmcnt(12)\n"
    "v_mfma_f32_16x16x32_bf16 v[40:43], a[112:115], a[140:143], v[40:43]\n"
    "ds_read_b64_tr_b16 a[16:17], v242 offset:41984\n"
    "s_add_u32 s98, s84, 393216\n"
    "v_mfma_f32_16x16x32_bf16 v[48:51], a[68:71], a[128:131], v[48:51]\n"
    "ds_read_b64_tr_b16 a[18:19], v242 offset:46080\n"
    "v_mfma_f32_16x16x32_bf16 v[48:51], a[84:87], a[132:135], v[48:51]\n"
    "buffer_load_dwordx4 a[192:195], v240, s[44:47], s84 offen offset:0 sc1\n"
    "v_mfma_f32_16x16x32_bf16 v[48:51], a[100:103], a[136:139], v[48:51]\n"
    "ds_read_b64_tr_b16 a[32:33], v242 offset:50176\n"
    "v_mfma_f32_16x16x32_bf16 v[48:51], a[116:119], a[140:143], v[48:51]\n"
    "ds_read_b64_tr_b16 a[34:35], v242 offset:54272\n"
    "s_waitcnt lgkmcnt(15)\n"
    "v_mfma_f32_16x16x32_bf16 v[56:59], a[72:75], a[128:131], v[56:59]\n"
    "buffer_load_dwordx4 a[196:199], v240, s[44:47], s84 offen offset:1024 sc1\n"
    "v_mfma_f32_16x16x32_bf16 v[56:59], a[88:91], a[132:135], v[56:59]\n"
    "ds_read_b64_tr_b16 a[48:49], v242 offset:58368\n"
    "v_mfma_f32_16x16x32_bf16 v[56:59], a[104:107], a[136:139], v[56:59]\n"
    "s_waitcnt lgkmcnt(15)\n"
    "v_mfma_f32_16x16x32_bf16 v[56:59], a[120:123], a[140:143], v[56:59]\n"
    "ds_read_b64_tr_b16 a[50:51], v242 offset:62464\n"
    "s_waitcnt lgkmcnt(14)\n"
    "v_mfma_f32_16x16x32_bf16 v[64:67], a[76:79], a[128:131], v[64:67]\n"
    "buffer_load_dwordx4 a[200:203], v240, s[44:47], s84 offen offset:2048 sc1\n"
    "s_waitcnt lgkmcnt(12)\n"
    "v_mfma_f32_16x16x32_bf16 v[64:67], a[92:95], a[132:135], v[64:67]\n"
    "ds_read_b64_tr_b16 a[4:5], v243 offset:33792\n"
    "v_lshlrev_b32 v241, 6, v241\n"
    "s_waitcnt lgkmcnt(11)\n"
    "v_mfma_f32_16x16x32_bf16 v[64:67], a[108:111], a[136:139], v[64:67]\n"
    "ds_read_b64_tr_b16 a[6:7], v243 offset:37888\n"
    "s_waitcnt lgkmcnt(10)\n"
    "v_mfma_f32_16x16x32_bf16 v[64:67], a[124:127], a[140:143], v[64:67]\n"
    "buffer_load_dwordx4 a[204:207], v240, s[44:47], s84 offen offset:3072 sc1\n"
    "s_waitcnt vmcnt(15)\n"
    "v_mfma_f32_16x16x32_bf16 v[104:107], a[64:67], a[144:147], v[104:107]\n"
    "ds_read_b64_tr_b16 a[20:21], v243 offset:41984\n"
    "s_waitcnt vmcnt(14)\n"
    "v_mfma_f32_16x16x32_bf16 v[104:107], a[80:83], a[148:151], v[104:107]\n"
    "s_waitcnt vmcnt(13)\n"
    "v_mfma_f32_16x16x32_bf16 v[104:107], a[96:99], a[152:155], v[104:107]\n"
    "ds_read_b64_tr_b16 a[22:23], v243 offset:46080\n"
    "s_waitcnt vmcnt(12)\n"
    "v_mfma_f32_16x16x32_bf16 v[104:107], a[112:115], a[156:159], v[104:107]\n"
    "buffer_load_dwordx4 a[208:211], v240, s[44:47], s85 offen offset:0 sc1\n"
    "v_mfma_f32_16x16x32_bf16 v[112:115], a[68:71], a[144:147], v[112:115]\n"
    "ds_read_b64_tr_b16 a[36:37], v243 offset:50176\n"
    "v_mfma_f32_16x16x32_bf16 v[112:115], a[84:87], a[148:151], v[112:115]\n"
    "v_mfma_f32_16x16x32_bf16 v[112:115], a[100:103], a[152:155], v[112:115]\n"
    "ds_read_b64_tr_b16 a[38:39], v243 offset:54272\n"
    "v_mfma_f32_16x16x32_bf16 v[112:115], a[116:119], a[156:159], v[112:115]\n"
    "buffer_load_dwordx4 a[212:215], v240, s[44:47], s85 offen offset:1024 sc1\n"
    "v_mfma_f32_16x16x32_bf16 v[120:123], a[72:75], a[144:147], v[120:123]\n"
    "ds_read_b64_tr_b16 a[52:53], v243 offset:58368\n"
    "v_mfma_f32_16x16x32_bf16 v[120:123], a[88:91], a[148:151], v[120:123]\n"
    "v_mfma_f32_16x16x32_bf16 v[120:123], a[104:107], a[152:155], v[120:123]\n"
    "ds_read_b64_tr_b16 a[54:55], v243 offset:62464\n"
    "v_mfma_f32_16x16x32_bf16 v[120:123], a[120:123], a[156:159], v[120:123]\n"
    "buffer_load_dwordx4 a[216:219], v240, s[44:47], s85 offen offset:2048 sc1\n"
    "v_mfma_f32_16x16x32_bf16 v[4:7], a[76:79], a[144:147], v[4:7]\n"
    "ds_read_b64_tr_b16 a[8:9], v242 offset:34304\n"
    "v_mfma_f32_16x16x32_bf16 v[4:7], a[92:95], a[148:151], v[4:7]\n"
    "ds_read_b64_tr_b16 a[10:11], v242 offset:38400\n"
    "v_lshrrev_b32 v254, 8, v240\n"
    "v_mfma_f32_16x16x32_bf16 v[4:7], a[108:111], a[152:155], v[4:7]\n"
    "buffer_load_dwordx4 a[220:223], v240, s[44:47], s85 offen offset:3072 sc1\n"
    "v_mfma_f32_16x16x32_bf16 v[4:7], a[124:127], a[156:159], v[4:7]\n"
    "ds_read_b64_tr_b16 a[24:25], v242 offset:42496\n"
    "s_waitcnt vmcnt(15)\n"
    "v_mfma_f32_16x16x32_bf16 v[76:79], a[64:67], a[160:163], v[76:79]\n"
    "s_waitcnt vmcnt(14)\n"
    "v_mfma_f32_16x16x32_bf16 v[76:79], a[80:83], a[164:167], v[76:79]\n"
    "buffer_load_dwordx4 v[148:151], v240, s[44:47], s73 offen offset:0 sc1\n"
    "s_waitcnt vmcnt(14)\n"
    "v_mfma_f32_16x16x32_bf16 v[76:79], a[96:99], a[168:171], v[76:79]\n"
    "ds_read_b64_tr_b16 a[26:27], v242 offset:46592\n"
    "s_waitcnt vmcnt(13)\n"
    "v_mfma_f32_16x16x32_bf16 v[76:79], a[112:115], a[172:175], v[76:79]\n"
    "ds_read_b64_tr_b16 a[40:41], v242 offset:50688\n"
    "v_mfma_f32_16x16x32_bf16 v[100:103], a[68:71], a[160:163], v[100:103]\n"
    "v_mfma_f32_16x16x32_bf16 v[100:103], a[84:87], a[164:167], v[100:103]\n"
    "buffer_load_dwordx4 v[180:183], v240, s[44:47], s73 offen offset:1024 sc1\n"
    "v_mfma_f32_16x16x32_bf16 v[100:103], a[100:103], a[168:171], v[100:103]\n"
    "ds_read_b64_tr_b16 a[42:43], v242 offset:54784\n"
    "v_mfma_f32_16x16x32_bf16 v[100:103], a[116:119], a[172:175], v[100:103]\n"
    "ds_read_b64_tr_b16 a[56:57], v242 offset:58880\n"
    "v_mfma_f32_16x16x32_bf16 v[108:111], a[72:75], a[160:163], v[108:111]\n"
    "buffer_load_dwordx4 v[212:215], v240, s[44:47], s73 offen offset:2048 sc1\n"
    "v_mfma_f32_16x16x32_bf16 v[108:111], a[88:91], a[164:167], v[108:111]\n"
    "ds_read_b64_tr_b16 a[58:59], v242 offset:62976\n"
    "v_mfma_f32_16x16x32_bf16 v[108:111], a[104:107], a[168:171], v[108:111]\n"
    "ds_read_b64_tr_b16 a[12:13], v243 offset:34304\n"
    "v_mfma_f32_16x16x32_bf16 v[108:111], a[120:123], a[172:175], v[108:111]\n"
    "v_mfma_f32_16x16x32_bf16 v[20:23], a[76:79], a[160:163], v[20:23]\n"
    "buffer_load_dwordx4 v[236:239], v240, s[44:47], s73 offen offset:3072 sc1\n"
    "v_mfma_f32_16x16x32_bf16 v[20:23], a[92:95], a[164:167], v[20:23]\n"
    "ds_read_b64_tr_b16 a[14:15], v243 offset:38400\n"
    "v_lshl_add_u32 v241, v254, 3, v241\n"
    "v_mfma_f32_16x16x32_bf16 v[20:23], a[108:111], a[168:171], v[20:23]\n"
    "ds_read_b64_tr_b16 a[28:29], v243 offset:42496\n"
    "v_mfma_f32_16x16x32_bf16 v[20:23], a[124:127], a[172:175], v[20:23]\n"
    "buffer_load_dwordx4 v[156:159], v240, s[44:47], s98 offen offset:0 sc1\n"
    "s_waitcnt vmcnt(16)\n"
    "v_mfma_f32_16x16x32_bf16 v[144:147], a[64:67], a[176:179], v[144:147]\n"
    "ds_read_b64_tr_b16 a[30:31], v243 offset:46592\n"
    "s_waitcnt vmcnt(15)\n"
    "v_mfma_f32_16x16x32_bf16 v[144:147], a[80:83], a[180:183], v[144:147]\n"
    "s_waitcnt vmcnt(14)\n"
    "v_mfma_f32_16x16x32_bf16 v[144:147], a[96:99], a[184:187], v[144:147]\n"
    "ds_read_b64_tr_b16 a[44:45], v243 offset:50688\n"
    "s_waitcnt vmcnt(13)\n"
    "v_mfma_f32_16x16x32_bf16 v[144:147], a[112:115], a[188:191], v[144:147]\n"
    "buffer_load_dwordx4 v[188:191], v240, s[44:47], s98 offen offset:1024 sc1\n"
    "v_mfma_f32_16x16x32_bf16 v[176:179], a[68:71], a[176:179], v[176:179]\n"
    "ds_read_b64_tr_b16 a[46:47], v243 offset:54784\n"
    "v_mfma_f32_16x16x32_bf16 v[176:179], a[84:87], a[180:183], v[176:179]\n"
    "v_mfma_f32_16x16x32_bf16 v[176:179], a[100:103], a[184:187], v[176:179]\n"
    "ds_read_b64_tr_b16 a[60:61], v243 offset:58880\n"
    "v_mfma_f32_16x16x32_bf16 v[176:179], a[116:119], a[188:191], v[176:179]\n"
    "buffer_load_dwordx4 v[220:223], v240, s[44:47], s98 offen offset:2048 sc1\n"
    "v_mfma_f32_16x16x32_bf16 v[208:211], a[72:75], a[176:179], v[208:211]\n"
    "ds_read_b64_tr_b16 a[62:63], v243 offset:62976\n"
    "v_mfma_f32_16x16x32_bf16 v[208:211], a[88:91], a[180:183], v[208:211]\n"
    "v_mfma_f32_16x16x32_bf16 v[208:211], a[104:107], a[184:187], v[208:211]\n"
    "buffer_load_dwordx4 v[32:35], v240, s[44:47], s98 offen offset:3072 sc1\n"
    "v_mfma_f32_16x16x32_bf16 v[208:211], a[120:123], a[188:191], v[208:211]\n"
    "v_mfma_f32_16x16x32_bf16 v[124:127], a[76:79], a[176:179], v[124:127]\n"
    "v_mfma_f32_16x16x32_bf16 v[124:127], a[92:95], a[180:183], v[124:127]\n"
    "v_mfma_f32_16x16x32_bf16 v[124:127], a[108:111], a[184:187], v[124:127]\n"
    "v_mfma_f32_16x16x32_bf16 v[124:127], a[124:127], a[188:191], v[124:127]\n"
    "s_waitcnt lgkmcnt(0)\n"
    "s_barrier\n"
    "v_mfma_f32_16x16x32_bf16 v[72:75], a[0:3], a[128:131], v[72:75]\n"
    "ds_read_b64_tr_b16 a[64:65], v247 offset:0\n"
    "s_lshl_b32 s84, s77, 12\n"
    "v_mfma_f32_16x16x32_bf16 v[72:75], a[16:19], a[132:135], v[72:75]\n"
    "s_add_u32 s85, s84, 131072\n"
    "ds_read_b64_tr_b16 a[66:67], v247 offset:4096\n"
    "v_mfma_f32_16x16x32_bf16 v[72:75], a[32:35], a[136:139], v[72:75]\n"
    "s_add_u32 s73, s84, 262144\n"
    "s_add_u32 s98, s84, 393216\n"
    "v_mfma_f32_16x16x32_bf16 v[72:75], a[48:51], a[140:143], v[72:75]\n"
    "ds_read_b64_tr_b16 a[80:81], v247 offset:8192\n"
    "ds_read_b64_tr_b16 a[82:83], v247 offset:12288\n"
    "v_mfma_f32_16x16x32_bf16 v[80:83], a[4:7], a[128:131], v[80:83]\n"
    "buffer_load_dwordx4 v[28:31], v240, s[44:47], s84 offen offset:0 sc1\n"
    "v_mfma_f32_16x16x32_bf16 v[80:83], a[20:23], a[132:135], v[80:83]\n"
    "buffer_load_dwordx4 v[128:131], v240, s[44:47], s84 offen offset:1024 sc1\n"
    "v_mfma_f32_16x16x32_bf16 v[80:83], a[36:39], a[136:139], v[80:83]\n"
    "ds_read_b64_tr_b16 a[96:97], v247 offset:16384\n"
    "v_mfma_f32_16x16x32_bf16 v[80:83], a[52:55], a[140:143], v[80:83]\n"
    "buffer_load_dwordx4 v[160:163], v240, s[44:47], s84 offen offset:2048 sc1\n"
    "v_mfma_f32_16x16x32_bf16 v[88:91], a[8:11], a[128:131], v[88:91]\n"
    "ds_read_b64_tr_b16 a[98:99], v247 offset:20480\n"
    "v_mfma_f32_16x16x32_bf16 v[88:91], a[24:27], a[132:135], v[88:91]\n"
    "buffer_load_dwordx4 v[192:195], v240, s[44:47], s84 offen offset:3072 sc1\n"
    "v_mfma_f32_16x16x32_bf16 v[88:91], a[40:43], a[136:139], v[88:91]\n"
    "ds_read_b64_tr_b16 a[112:113], v247 offset:24576\n"
    "v_mfma_f32_16x16x32_bf16 v[88:91], a[56:59], a[140:143], v[88:91]\n"
    "ds_read_b64_tr_b16 a[114:115], v247 offset:28672\n"
    "v_mfma_f32_16x16x32_bf16 v[96:99], a[12:15], a[128:131], v[96:99]\n"
    "buffer_load_dwordx4 v[60:63], v240, s[44:47], s85 offen offset:0 sc1\n"
    "v_mfma_f32_16x16x32_bf16 v[96:99], a[28:31], a[132:135], v[96:99]\n"
    "ds_read_b64_tr_b16 a[68:69], v248 offset:0\n"
    "v_mfma_f32_16x16x32_bf16 v[96:99], a[44:47], a[136:139], v[96:99]\n"
    "buffer_load_dwordx4 v[136:139], v240, s[44:47], s85 offen offset:1024 sc1\n"
    "v_mfma_f32_16x16x32_bf16 v[96:99], a[60:63], a[140:143], v[96:99]\n"
    "ds_read_b64_tr_b16 a[70:71], v248 offset:4096\n"
    "v_mfma_f32_16x16x32_bf16 v[12:15], a[0:3], a[144:147], v[12:15]\n"
    "buffer_load_dwordx4 v[168:171], v240, s[44:47], s85 offen offset:2048 sc1\n"
    "v_mfma_f32_16x16x32_bf16 v[12:15], a[16:19], a[148:151], v[12:15]\n"
    "ds_read_b64_tr_b16 a[84:85], v248 offset:8192\n"
    "v_mfma_f32_16x16x32_bf16 v[12:15], a[32:35], a[152:155], v[12:15]\n"
    "buffer_load_dwordx4 v[200:203], v240, s[44:47], s85 offen offset:3072 sc1\n"
    "v_mfma_f32_16x16x32_bf16 v[12:15], a[48:51], a[156:159], v[12:15]\n"
    "ds_read_b64_tr_b16 a[86:87], v248 offset:12288\n"
    "v_mfma_f32_16x16x32_bf16 v[36:39], a[4:7], a[144:147], v[36:39]\n"
    "buffer_load_dwordx4 v[224:227], v240, s[44:47], s73 offen offset:0 sc1\n"
    "v_mfma_f32_16x16x32_bf16 v[36:39], a[20:23], a[148:151], v[36:39]\n"
    "ds_read_b64_tr_b16 a[100:101], v248 offset:16384\n"
    "v_mfma_f32_16x16x32_bf16 v[36:39], a[36:39], a[152:155], v[36:39]\n"
    "ds_read_b64_tr_b16 a[102:103], v248 offset:20480\n"
    "v_mfma_f32_16x16x32_bf16 v[36:39], a[52:55], a[156:159], v[36:39]\n"
    "buffer_load_dwordx4 v[132:135], v240, s[44:47], s73 offen offset:1024 sc1\n"
    "v_mfma_f32_16x16x32_bf16 v[44:47], a[8:11], a[144:147], v[44:47]\n"
    "ds_read_b64_tr_b16 a[116:117], v248 offset:24576\n"
    "v_mfma_f32_16x16x32_bf16 v[44:47], a[24:27], a[148:151], v[44:47]\n"
    "buffer_load_dwordx4 v[164:167], v240, s[44:47], s73 offen offset:2048 sc1\n"
    "v_mfma_f32_16x16x32_bf16 v[44:47], a[40:43], a[152:155], v[44:47]\n"
    "ds_read_b64_tr_b16 a[118:119], v248 offset:28672\n"
    "v_mfma_f32_16x16x32_bf16 v[44:47], a[56:59], a[156:159], v[44:47]\n"
    "buffer_load_dwordx4 v[196:199], v240, s[44:47], s73 offen offset:3072 sc1\n"
    "v_mfma_f32_16x16x32_bf16 v[68:71], a[12:15], a[144:147], v[68:71]\n"
    "ds_read_b64_tr_b16 a[72:73], v247 offset:512\n"
    "v_mfma_f32_16x16x32_bf16 v[68:71], a[28:31], a[148:151], v[68:71]\n"
    "buffer_load_dwordx4 v[228:231], v240, s[44:47], s98 offen offset:0 sc1\n"
    "v_mfma_f32_16x16x32_bf16 v[68:71], a[44:47], a[152:155], v[68:71]\n"
    "ds_read_b64_tr_b16 a[74:75], v247 offset:4608\n"
    "v_mfma_f32_16x16x32_bf16 v[68:71], a[60:63], a[156:159], v[68:71]\n"
    "buffer_load_dwordx4 v[140:143], v240, s[44:47], s98 offen offset:1024 sc1\n"
    "v_mfma_f32_16x16x32_bf16 v[52:55], a[0:3], a[160:163], v[52:55]\n"
    "ds_read_b64_tr_b16 a[88:89], v247 offset:8704\n"
    "v_mfma_f32_16x16x32_bf16 v[52:55], a[16:19], a[164:167], v[52:55]\n"
    "ds_read_b64_tr_b16 a[90:91], v247 offset:12800\n"
    "v_mfma_f32_16x16x32_bf16 v[52:55], a[32:35], a[168:171], v[52:55]\n"
    "buffer_load_dwordx4 v[172:175], v240, s[44:47], s98 offen offset:2048 sc1\n"
    "v_mfma_f32_16x16x32_bf16 v[52:55], a[48:51], a[172:175], v[52:55]\n"
    "ds_read_b64_tr_b16 a[104:105], v247 offset:16896\n"
    "v_mfma_f32_16x16x32_bf16 v[84:87], a[4:7], a[160:163], v[84:87]\n"
    "buffer_load_dwordx4 v[204:207], v240, s[44:47], s98 offen offset:3072 sc1\n"
    "v_mfma_f32_16x16x32_bf16 v[84:87], a[20:23], a[164:167], v[84:87]\n"
    "ds_read_b64_tr_b16 a[106:107], v247 offset:20992\n"
    "v_mfma_f32_16x16x32_bf16 v[84:87], a[36:39], a[168:171], v[84:87]\n"
    "v_lshrrev_b32 v254, 7, v240\n"
    "v_mfma_f32_16x16x32_bf16 v[84:87], a[52:55], a[172:175], v[84:87]\n"
    "ds_read_b64_tr_b16 a[120:121], v247 offset:25088\n"
    "v_lshlrev_b32 v252, 4, v254\n"
    "v_mfma_f32_16x16x32_bf16 v[116:119], a[8:11], a[160:163], v[116:119]\n"
    "v_xor_b32 v252, v252, v240\n"
    "v_mfma_f32_16x16x32_bf16 v[116:119], a[24:27], a[164:167], v[116:119]\n"
    "ds_read_b64_tr_b16 a[122:123], v247 offset:29184\n"
    "v_and_b32 v252, 0x70, v252\n"
    "v_mfma_f32_16x16x32_bf16 v[116:119], a[40:43], a[168:171], v[116:119]\n"
    "ds_read_b64_tr_b16 a[76:77], v248 offset:512\n"
    "v_lshl_add_u32 v254, v254, 10, v252\n"
    "v_mfma_f32_16x16x32_bf16 v[116:119], a[56:59], a[172:175], v[116:119]\n"
    "v_mfma_f32_16x16x32_bf16 v[92:95], a[12:15], a[160:163], v[92:95]\n"
    "s_lshl_b32 s63, s60, 11\n"
    "ds_read_b64_tr_b16 a[78:79], v248 offset:4608\n"
    "v_mfma_f32_16x16x32_bf16 v[92:95], a[28:31], a[164:167], v[92:95]\n"
    "s_add_u32 s63, s63, 24576\n"
    "v_mfma_f32_16x16x32_bf16 v[92:95], a[44:47], a[168:171], v[92:95]\n"
    "s_mov_b32 m0, s63\n"
    "ds_read_b64_tr_b16 a[92:93], v248 offset:8704\n"
    "v_mfma_f32_16x16x32_bf16 v[92:95], a[60:63], a[172:175], v[92:95]\n"
    "s_mov_b32 s64, 0\n"
    "v_mfma_f32_16x16x32_bf16 v[152:155], a[0:3], a[176:179], v[152:155]\n"
    "buffer_load_dwordx4 v254, s[48:51], s64 offen lds\n"
    "v_mfma_f32_16x16x32_bf16 v[152:155], a[16:19], a[180:183], v[152:155]\n"
    "ds_read_b64_tr_b16 a[94:95], v248 offset:12800\n"
    "v_mfma_f32_16x16x32_bf16 v[152:155], a[32:35], a[184:187], v[152:155]\n"
    "ds_read_b64_tr_b16 a[108:109], v248 offset:16896\n"
    "s_mov_b32 s64, 7168\n"
    "v_mfma_f32_16x16x32_bf16 v[152:155], a[48:51], a[188:191], v[152:155]\n"
    "buffer_load_dwordx4 v254, s[48:51], s64 offen offset:1024 lds\n"
    "v_mfma_f32_16x16x32_bf16 v[184:187], a[4:7], a[176:179], v[184:187]\n"
    "ds_read_b64_tr_b16 a[110:111], v248 offset:20992\n"
    "v_mfma_f32_16x16x32_bf16 v[184:187], a[20:23], a[180:183], v[184:187]\n"
    "v_mfma_f32_16x16x32_bf16 v[184:187], a[36:39], a[184:187], v[184:187]\n"
    "ds_read_b64_tr_b16 a[124:125], v248 offset:25088\n"
    "v_and_b32 v252, 0xf0, v240\n"
    "v_mfma_f32_16x16x32_bf16 v[184:187], a[52:55], a[188:191], v[184:187]\n"
    "v_lshlrev_b32 v253, 3, v252\n"
    "v_mfma_f32_16x16x32_bf16 v[216:219], a[8:11], a[176:179], v[216:219]\n"
    "ds_read_b64_tr_b16 a[126:127], v248 offset:29184\n"
    "v_and_b32 v252, 0x70, v252\n"
    "v_mfma_f32_16x16x32_bf16 v[216:219], a[24:27], a[180:183], v[216:219]\n"
    "v_lshrrev_b32 v254, 5, v240\n"
    "v_mfma_f32_16x16x32_bf16 v[216:219], a[40:43], a[184:187], v[216:219]\n"
    "v_and_b32 v254, 0x18, v254\n"
    "v_mfma_f32_16x16x32_bf16 v[216:219], a[56:59], a[188:191], v[216:219]\n"
    "v_xor_b32 v252, v252, v254\n"
    "v_mfma_f32_16x16x32_bf16 v[232:235], a[12:15], a[176:179], v[232:235]\n"
    "v_add3_u32 v252, v252, v253, s63\n"
    "v_mfma_f32_16x16x32_bf16 v[232:235], a[28:31], a[180:183], v[232:235]\n"
    "v_mfma_f32_16x16x32_bf16 v[232:235], a[44:47], a[184:187], v[232:235]\n"
    "v_mfma_f32_16x16x32_bf16 v[232:235], a[60:63], a[188:191], v[232:235]\n"
    "s_waitcnt vmcnt(33) lgkmcnt(15)\n"
    "v_mfma_f32_16x16x32_bf16 v[40:43], a[64:67], a[192:195], v[40:43]\n"
    "ds_read_b64_tr_b16 a[0:1], v247 offset:32768\n"
    "s_add_u32 m0, s67, 32768\n"
    "s_waitcnt vmcnt(32)\n"
    "v_mfma_f32_16x16x32_bf16 v[40:43], a[80:83], a[196:199], v[40:43]\n"
    "ds_read_b64_tr_b16 a[2:3], v247 offset:36864\n"
    "ds_read_b64_tr_b16 a[16:17], v247 offset:40960\n"
    "s_waitcnt vmcnt(31)\n"
    "v_mfma_f32_16x16x32_bf16 v[40:43], a[96:99], a[200:203], v[40:43]\n"
    "buffer_load_dwordx4 v251, s[36:39], s66 offen lds\n"
    "s_waitcnt vmcnt(31)\n"
    "v_mfma_f32_16x16x32_bf16 v[40:43], a[112:115], a[204:207], v[40:43]\n"
    "ds_read_b64_tr_b16 a[18:19], v247 offset:45056\n"
    "v_mfma_f32_16x16x32_bf16 v[48:51], a[68:71], a[192:195], v[48:51]\n"
    "s_add_u32 s66, s66, 7168\n"
    "v_mfma_f32_16x16x32_bf16 v[48:51], a[84:87], a[196:199], v[48:51]\n"
    "ds_read_b64_tr_b16 a[32:33], v247 offset:49152\n"
    "v_mfma_f32_16x16x32_bf16 v[48:51], a[100:103], a[200:203], v[48:51]\n"
    "buffer_load_dwordx4 v251, s[36:39], s66 offen offset:1024 lds\n"
    "v_mfma_f32_16x16x32_bf16 v[48:51], a[116:119], a[204:207], v[48:51]\n"
    "ds_read_b64_tr_b16 a[34:35], v247 offset:53248\n"
    "s_waitcnt lgkmcnt(15)\n"
    "v_mfma_f32_16x16x32_bf16 v[56:59], a[72:75], a[192:195], v[56:59]\n"
    "v_mfma_f32_16x16x32_bf16 v[56:59], a[88:91], a[196:199], v[56:59]\n"
    "ds_read_b64_tr_b16 a[48:49], v247 offset:57344\n"
    "v_mfma_f32_16x16x32_bf16 v[56:59], a[104:107], a[200:203], v[56:59]\n"
    "s_add_u32 s66, s66, 7168\n"
    "s_waitcnt lgkmcnt(15)\n"
    "v_mfma_f32_16x16x32_bf16 v[56:59], a[120:123], a[204:207], v[56:59]\n"
    "ds_read_b64_tr_b16 a[50:51], v247 offset:61440\n"
    "s_waitcnt lgkmcnt(14)\n"
    "v_mfma_f32_16x16x32_bf16 v[64:67], a[76:79], a[192:195], v[64:67]\n"
    "buffer_load_dwordx4 v251, s[36:39], s66 offen offset:2048 lds\n"
    "s_waitcnt lgkmcnt(12)\n"
    "v_mfma_f32_16x16x32_bf16 v[64:67], a[92:95], a[196:199], v[64:67]\n"
    "ds_read_b64_tr_b16 a[4:5], v248 offset:32768\n"
    "s_waitcnt lgkmcnt(11)\n"
    "v_mfma_f32_16x16x32_bf16 v[64:67], a[108:111], a[200:203], v[64:67]\n"
    "ds_read_b64_tr_b16 a[6:7], v248 offset:36864\n"
    "s_waitcnt lgkmcnt(10)\n"
    "v_mfma_f32_16x16x32_bf16 v[64:67], a[124:127], a[204:207], v[64:67]\n"
    "s_waitcnt vmcnt(32)\n"
    "v_mfma_f32_16x16x32_bf16 v[104:107], a[64:67], a[208:211], v[104:107]\n"
    "s_add_u32 s66, s66, 7168\n"
    "ds_read_b64_tr_b16 a[20:21], v248 offset:40960\n"
    "s_waitcnt vmcnt(31)\n"
    "v_mfma_f32_16x16x32_bf16 v[104:107], a[80:83], a[212:215], v[104:107]\n"
    "s_waitcnt vmcnt(30)\n"
    "v_mfma_f32_16x16x32_bf16 v[104:107], a[96:99], a[216:219], v[104:107]\n"
    "ds_read_b64_tr_b16 a[22:23], v248 offset:45056\n"
    "s_waitcnt vmcnt(29)\n"
    "v_mfma_f32_16x16x32_bf16 v[104:107], a[112:115], a[220:223], v[104:107]\n"
    "buffer_load_dwordx4 v251, s[36:39], s66 offen offset:3072 lds\n"
    "v_mfma_f32_16x16x32_bf16 v[112:115], a[68:71], a[208:211], v[112:115]\n"
    "ds_read_b64_tr_b16 a[36:37], v248 offset:49152\n"
    "v_mfma_f32_16x16x32_bf16 v[112:115], a[84:87], a[212:215], v[112:115]\n"
    "v_mfma_f32_16x16x32_bf16 v[112:115], a[100:103], a[216:219], v[112:115]\n"
    "ds_read_b64_tr_b16 a[38:39], v248 offset:53248\n"
    "s_add_u32 s66, s66, 109568\n"
    "v_mfma_f32_16x16x32_bf16 v[112:115], a[116:119], a[220:223], v[112:115]\n"
    "v_mfma_f32_16x16x32_bf16 v[120:123], a[72:75], a[208:211], v[120:123]\n"
    "ds_read_b64_tr_b16 a[52:53], v248 offset:57344\n"
    "v_mfma_f32_16x16x32_bf16 v[120:123], a[88:91], a[212:215], v[120:123]\n"
    "buffer_load_dwordx4 a[128:131], v240, s[40:43], s95 offen offset:3072 sc1\n"
    "v_mfma_f32_16x16x32_bf16 v[120:123], a[104:107], a[216:219], v[120:123]\n"
    "ds_read_b64_tr_b16 a[54:55], v248 offset:61440\n"
    "v_mfma_f32_16x16x32_bf16 v[120:123], a[120:123], a[220:223], v[120:123]\n"
    "ds_read_b64_tr_b16 a[8:9], v247 offset:33280\n"
    "v_mfma_f32_16x16x32_bf16 v[4:7], a[76:79], a[208:211], v[4:7]\n"
    "s_add_u32 s95, s95, 4096\n"
    "v_mfma_f32_16x16x32_bf16 v[4:7], a[92:95], a[212:215], v[4:7]\n"
    "ds_read_b64_tr_b16 a[10:11], v247 offset:37376\n"
    "v_mfma_f32_16x16x32_bf16 v[4:7], a[108:111], a[216:219], v[4:7]\n"
    "v_mfma_f32_16x16x32_bf16 v[4:7], a[124:127], a[220:223], v[4:7]\n"
    "buffer_load_dwordx4 a[132:135], v240, s[40:43], s95 offen offset:0 sc1\n"
    "s_waitcnt vmcnt(31)\n"
    "v_mfma_f32_16x16x32_bf16 v[76:79], a[64:67], v[148:151], v[76:79]\n"
    "ds_read_b64_tr_b16 a[24:25], v247 offset:41472\n"
    "s_waitcnt vmcnt(30)\n"
    "v_mfma_f32_16x16x32_bf16 v[76:79], a[80:83], v[180:183], v[76:79]\n"
    "ds_read_b64_tr_b16 a[26:27], v247 offset:45568\n"
    "s_waitcnt vmcnt(29)\n"
    "v_mfma_f32_16x16x32_bf16 v[76:79], a[96:99], v[212:215], v[76:79]\n"
    "s_waitcnt vmcnt(28)\n"
    "v_mfma_f32_16x16x32_bf16 v[76:79], a[112:115], v[236:239], v[76:79]\n"
    "buffer_load_dwordx4 a[136:139], v240, s[40:43], s95 offen offset:1024 sc1\n"
    "v_mfma_f32_16x16x32_bf16 v[100:103], a[68:71], v[148:151], v[100:103]\n"
    "ds_read_b64_tr_b16 a[40:41], v247 offset:49664\n"
    "v_mfma_f32_16x16x32_bf16 v[100:103], a[84:87], v[180:183], v[100:103]\n"
    "ds_read_b64_tr_b16 a[42:43], v247 offset:53760\n"
    "v_mfma_f32_16x16x32_bf16 v[100:103], a[100:103], v[212:215], v[100:103]\n"
    "v_mfma_f32_16x16x32_bf16 v[100:103], a[116:119], v[236:239], v[100:103]\n"
    "ds_read_b64_tr_b16 a[56:57], v247 offset:57856\n"
    "v_mfma_f32_16x16x32_bf16 v[108:111], a[72:75], v[148:151], v[108:111]\n"
    "buffer_load_dwordx4 a[140:143], v240, s[40:43], s95 offen offset:2048 sc1\n"
    "v_mfma_f32_16x16x32_bf16 v[108:111], a[88:91], v[180:183], v[108:111]\n"
    "ds_read_b64_tr_b16 a[58:59], v247 offset:61952\n"
    "v_mfma_f32_16x16x32_bf16 v[108:111], a[104:107], v[212:215], v[108:111]\n"
    "ds_read_b64_tr_b16 a[12:13], v248 offset:33280\n"
    "v_mfma_f32_16x16x32_bf16 v[108:111], a[120:123], v[236:239], v[108:111]\n"
    "buffer_load_dwordx4 a[144:147], v240, s[40:43], s95 offen offset:3072 sc1\n"
    "v_mfma_f32_16x16x32_bf16 v[20:23], a[76:79], v[148:151], v[20:23]\n"
    "ds_read_b64_tr_b16 a[14:15], v248 offset:37376\n"
    "v_mfma_f32_16x16x32_bf16 v[20:23], a[92:95], v[180:183], v[20:23]\n"
    "v_mfma_f32_16x16x32_bf16 v[20:23], a[108:111], v[212:215], v[20:23]\n"
    "ds_read_b64_tr_b16 a[28:29], v248 offset:41472\n"
    "v_mfma_f32_16x16x32_bf16 v[20:23], a[124:127], v[236:239], v[20:23]\n"
    "s_add_u32 s95, s95, 4096\n"
    "s_waitcnt vmcnt(30)\n"
    "v_mfma_f32_16x16x32_bf16 v[144:147], a[64:67], v[156:159], v[144:147]\n"
    "ds_read_b64_tr_b16 a[30:31], v248 offset:45568\n"
    "s_waitcnt vmcnt(29)\n"
    "v_mfma_f32_16x16x32_bf16 v[144:147], a[80:83], v[188:191], v[144:147]\n"
    "buffer_load_dwordx4 a[148:151], v240, s[40:43], s95 offen offset:0 sc1\n"
    "s_waitcnt vmcnt(29)\n"
    "v_mfma_f32_16x16x32_bf16 v[144:147], a[96:99], v[220:223], v[144:147]\n"
    "ds_read_b64_tr_b16 a[44:45], v248 offset:49664\n"
    "s_waitcnt vmcnt(28)\n"
    "v_mfma_f32_16x16x32_bf16 v[144:147], a[112:115], v[32:35], v[144:147]\n"
    "v_mfma_f32_16x16x32_bf16 v[176:179], a[68:71], v[156:159], v[176:179]\n"
    "ds_read_b64_tr_b16 a[46:47], v248 offset:53760\n"
    "v_mfma_f32_16x16x32_bf16 v[176:179], a[84:87], v[188:191], v[176:179]\n"
    "buffer_load_dwordx4 a[152:155], v240, s[40:43], s95 offen offset:1024 sc1\n"
    "v_mfma_f32_16x16x32_bf16 v[176:179], a[100:103], v[220:223], v[176:179]\n"
    "ds_read_b64_tr_b16 a[60:61], v248 offset:57856\n"
    "v_mfma_f32_16x16x32_bf16 v[176:179], a[116:119], v[32:35], v[176:179]\n"
    "v_mfma_f32_16x16x32_bf16 v[208:211], a[72:75], v[156:159], v[208:211]\n"
    "ds_read_b64_tr_b16 a[62:63], v248 offset:61952\n"
    "v_mfma_f32_16x16x32_bf16 v[208:211], a[88:91], v[188:191], v[208:211]\n"
    "buffer_load_dwordx4 a[156:159], v240, s[40:43], s95 offen offset:2048 sc1\n"
    "v_mfma_f32_16x16x32_bf16 v[208:211], a[104:107], v[220:223], v[208:211]\n"
    "v_mfma_f32_16x16x32_bf16 v[208:211], a[120:123], v[32:35], v[208:211]\n"
    "v_mfma_f32_16x16x32_bf16 v[124:127], a[76:79], v[156:159], v[124:127]\n"
    "v_mfma_f32_16x16x32_bf16 v[124:127], a[92:95], v[188:191], v[124:127]\n"
    "v_mfma_f32_16x16x32_bf16 v[124:127], a[108:111], v[220:223], v[124:127]\n"
    "v_mfma_f32_16x16x32_bf16 v[124:127], a[124:127], v[32:35], v[124:127]\n"
    "s_waitcnt vmcnt(29) lgkmcnt(15)\n"
    "v_mfma_f32_16x16x32_bf16 v[40:43], a[0:3], v[28:31], v[40:43]\n"
    "ds_read_b64_tr_b16 a[64:65], v247 offset:1024\n"
    "s_add_u32 m0, s67, 49152\n"
    "s_waitcnt vmcnt(28)\n"
    "v_mfma_f32_16x16x32_bf16 v[40:43], a[16:19], v[128:131], v[40:43]\n"
    "buffer_load_dwordx4 v251, s[36:39], s66 offen lds\n"
    "s_waitcnt vmcnt(28)\n"
    "v_mfma_f32_16x16x32_bf16 v[40:43], a[32:35], v[160:163], v[40:43]\n"
    "s_add_u32 s66, s66, 7168\n"
    "ds_read_b64_tr_b16 a[66:67], v247 offset:5120\n"
    "s_waitcnt vmcnt(27)\n"
    "v_mfma_f32_16x16x32_bf16 v[40:43], a[48:51], v[192:195], v[40:43]\n"
    "buffer_load_dwordx4 v251, s[36:39], s66 offen offset:1024 lds\n"
    "v_mfma_f32_16x16x32_bf16 v[48:51], a[4:7], v[28:31], v[48:51]\n"
    "s_add_u32 s66, s66, 7168\n"
    "ds_read_b64_tr_b16 a[80:81], v247 offset:9216\n"
    "v_mfma_f32_16x16x32_bf16 v[48:51], a[20:23], v[128:131], v[48:51]\n"
    "buffer_load_dwordx4 v251, s[36:39], s66 offen offset:2048 lds\n"
    "v_mfma_f32_16x16x32_bf16 v[48:51], a[36:39], v[160:163], v[48:51]\n"
    "s_add_u32 s66, s66, 7168\n"
    "ds_read_b64_tr_b16 a[82:83], v247 offset:13312\n"
    "v_mfma_f32_16x16x32_bf16 v[48:51], a[52:55], v[192:195], v[48:51]\n"
    "buffer_load_dwordx4 v251, s[36:39], s66 offen offset:3072 lds\n"
    "s_add_u32 s66, s66, 109568\n"
    "s_waitcnt vmcnt(29)\n"
    "v_mfma_f32_16x16x32_bf16 v[104:107], a[0:3], v[60:63], v[104:107]\n"
    "buffer_load_dwordx4 a[160:163], v240, s[40:43], s95 offen offset:3072 sc1\n"
    "s_waitcnt vmcnt(29)\n"
    "v_mfma_f32_16x16x32_bf16 v[104:107], a[16:19], v[136:139], v[104:107]\n"
    "s_add_u32 s95, s95, 4096\n"
    "ds_read_b64_tr_b16 a[96:97], v247 offset:17408\n"
    "s_waitcnt vmcnt(28)\n"
    "v_mfma_f32_16x16x32_bf16 v[104:107], a[32:35], v[168:171], v[104:107]\n"
    "buffer_load_dwordx4 a[164:167], v240, s[40:43], s95 offen offset:0 sc1\n"
    "s_waitcnt vmcnt(28)\n"
    "v_mfma_f32_16x16x32_bf16 v[104:107], a[48:51], v[200:203], v[104:107]\n"
    "buffer_load_dwordx4 a[168:171], v240, s[40:43], s95 offen offset:1024 sc1\n"
    "v_mfma_f32_16x16x32_bf16 v[112:115], a[4:7], v[60:63], v[112:115]\n"
    "buffer_load_dwordx4 a[172:175], v240, s[40:43], s95 offen offset:2048 sc1\n"
    "v_mfma_f32_16x16x32_bf16 v[112:115], a[20:23], v[136:139], v[112:115]\n"
    "buffer_load_dwordx4 a[176:179], v240, s[40:43], s95 offen offset:3072 sc1\n"
    "v_mfma_f32_16x16x32_bf16 v[112:115], a[36:39], v[168:171], v[112:115]\n"
    "s_add_u32 s95, s95, 4096\n"
    "v_cvt_pk_bf16_f32 v40, v40, v41\n"
    "v_mfma_f32_16x16x32_bf16 v[112:115], a[52:55], v[200:203], v[112:115]\n"
    "buffer_load_dwordx4 a[180:183], v240, s[40:43], s95 offen offset:0 sc1\n"
    "v_cvt_pk_bf16_f32 v41, v42, v43\n"
    "s_waitcnt vmcnt(31)\n"
    "v_mfma_f32_16x16x32_bf16 v[76:79], a[0:3], v[224:227], v[76:79]\n"
    "buffer_load_dwordx4 a[184:187], v240, s[40:43], s95 offen offset:1024 sc1\n"
    "s_waitcnt vmcnt(31)\n"
    "v_mfma_f32_16x16x32_bf16 v[76:79], a[16:19], v[132:135], v[76:79]\n"
    "buffer_load_dwordx4 a[188:191], v240, s[40:43], s95 offen offset:2048 sc1\n"
    "s_waitcnt vmcnt(31)\n"
    "v_mfma_f32_16x16x32_bf16 v[76:79], a[32:35], v[164:167], v[76:79]\n"
    "ds_read_b64_tr_b16 a[98:99], v247 offset:21504\n"
    "v_cvt_pk_bf16_f32 v42, v48, v49\n"
    "s_waitcnt vmcnt(30)\n"
    "v_mfma_f32_16x16x32_bf16 v[76:79], a[48:51], v[196:199], v[76:79]\n"
    "v_cvt_pk_bf16_f32 v43, v50, v51\n"
    "v_xad_u32 v48, v40, v41, s68\n"
    "v_mfma_f32_16x16x32_bf16 v[100:103], a[4:7], v[224:227], v[100:103]\n"
    "ds_read_b64_tr_b16 a[112:113], v247 offset:25600\n"
    "v_xad_u32 v48, v48, v42, v43\n"
    "v_mfma_f32_16x16x32_bf16 v[100:103], a[20:23], v[132:135], v[100:103]\n"
    "s_cmp_eq_u32 s71, 0\n"
    "s_cbranch_scc1 LPS000\n"
    "buffer_store_dwordx4 v[40:43], v240, s[56:59], s93 offen offset:0 sc1\n"
    "LPS000_r:\n"
    "v_mfma_f32_16x16x32_bf16 v[100:103], a[36:39], v[164:167], v[100:103]\n"
    "ds_read_b64_tr_b16 a[114:115], v247 offset:29696\n"
    "ds_read_b64_tr_b16 a[68:69], v248 offset:1024\n"
    "v_mfma_f32_16x16x32_bf16 v[100:103], a[52:55], v[196:199], v[100:103]\n"
    "ds_read_b64_tr_b16 a[70:71], v248 offset:5120\n"
    "v_cvt_pk_bf16_f32 v104, v104, v105\n"
    "v_cvt_pk_bf16_f32 v105, v106, v107\n"
    "s_waitcnt vmcnt(29)\n"
    "v_mfma_f32_16x16x32_bf16 v[144:147], a[0:3], v[228:231], v[144:147]\n"
    "v_cvt_pk_bf16_f32 v106, v112, v113\n"
    "v_cvt_pk_bf16_f32 v107, v114, v115\n"
    "s_waitcnt vmcnt(28)\n"
    "v_mfma_f32_16x16x32_bf16 v[144:147], a[16:19], v[140:143], v[144:147]\n"
    "ds_read_b64_tr_b16 a[84:85], v248 offset:9216\n"
    "v_xad_u32 v49, v104, v105, s68\n"
    "s_waitcnt vmcnt(27)\n"
    "v_mfma_f32_16x16x32_bf16 v[144:147], a[32:35], v[172:175], v[144:147]\n"
    "ds_read_b64_tr_b16 a[86:87], v248 offset:13312\n"
    "v_xad_u32 v49, v49, v106, v107\n"
    "s_waitcnt vmcnt(26)\n"
    "v_mfma_f32_16x16x32_bf16 v[144:147], a[48:51], v[204:207], v[144:147]\n"
    "s_cmp_eq_u32 s71, 0\n"
    "s_cbranch_scc1 LPS001\n"
    "buffer_store_dwordx4 v[104:107], v240, s[56:59], s93 offen offset:2048 sc1\n"
    "LPS001_r:\n"
    "v_mfma_f32_16x16x32_bf16 v[176:179], a[4:7], v[228:231], v[176:179]\n"
    "ds_read_b64_tr_b16 a[100:101], v248 offset:17408\n"
    "v_cvt_pk_bf16_f32 v76, v76, v77\n"
    "v_mfma_f32_16x16x32_bf16 v[176:179], a[20:23], v[140:143], v[176:179]\n"
    "ds_read_b64_tr_b16 a[102:103], v248 offset:21504\n"
    "v_cvt_pk_bf16_f32 v77, v78, v79\n"
    "v_mfma_f32_16x16x32_bf16 v[176:179], a[36:39], v[172:175], v[176:179]\n"
    "ds_read_b64_tr_b16 a[116:117], v248 offset:25600\n"
    "v_cvt_pk_bf16_f32 v78, v100, v101\n"
    "v_mfma_f32_16x16x32_bf16 v[176:179], a[52:55], v[204:207], v[176:179]\n"
    "v_cvt_pk_bf16_f32 v79, v102, v103\n"
    "v_xad_u32 v50, v76, v77, s68\n"
    "v_xad_u32 v50, v50, v78, v79\n"
    "s_waitcnt lgkmcnt(15)\n"
    "v_mfma_f32_16x16x32_bf16 v[56:59], a[8:11], v[28:31], v[56:59]\n"
    "ds_read_b64_tr_b16 a[118:119], v248 offset:29696\n"
    "ds_read_b64_tr_b16 a[72:73], v247 offset:1536\n"
    "v_mfma_f32_16x16x32_bf16 v[56:59], a[24:27], v[128:131], v[56:59]\n"
    "s_cmp_eq_u32 s71, 0\n"
    "s_cbranch_scc1 LPS002\n"
    "buffer_store_dwordx4 v[76:79], v240, s[56:59], s28 offen offset:0 sc1\n"
    "LPS002_r:\n"
    "v_mfma_f32_16x16x32_bf16 v[56:59], a[40:43], v[160:163], v[56:59]\n"
    "ds_read_b64_tr_b16 a[74:75], v247 offset:5632\n"
    "v_cvt_pk_bf16_f32 v144, v144, v145\n"
    "v_mfma_f32_16x16x32_bf16 v[56:59], a[56:59], v[192:195], v[56:59]\n"
    "v_cvt_pk_bf16_f32 v145, v146, v147\n"
    "v_cvt_pk_bf16_f32 v146, v176, v177\n"
    "v_mfma_f32_16x16x32_bf16 v[64:67], a[12:15], v[28:31], v[64:67]\n"
    "ds_read_b64_tr_b16 a[88:89], v247 offset:9728\n"
    "v_cvt_pk_bf16_f32 v147, v178, v179\n"
    "v_mfma_f32_16x16x32_bf16 v[64:67], a[28:31], v[128:131], v[64:67]\n"
    "ds_read_b64_tr_b16 a[90:91], v247 offset:13824\n"
    "v_xad_u32 v51, v144, v145, s68\n"
    "v_mfma_f32_16x16x32_bf16 v[64:67], a[44:47], v[160:163], v[64:67]\n"
    "ds_read_b64_tr_b16 a[104:105], v247 offset:17920\n"
    "ds_read_b64_tr_b16 a[106:107], v247 offset:22016\n"
    "v_xad_u32 v51, v51, v146, v147\n"
    "v_mfma_f32_16x16x32_bf16 v[64:67], a[60:63], v[192:195], v[64:67]\n"
    "s_cmp_eq_u32 s71, 0\n"
    "s_cbranch_scc1 LPS003\n"
    "buffer_store_dwordx4 v[144:147], v240, s[56:59], s28 offen offset:2048 sc1\n"
    "LPS003_r:\n"
    "v_mfma_f32_16x16x32_bf16 v[120:123], a[8:11], v[60:63], v[120:123]\n"
    "v_cvt_pk_bf16_f32 v56, v56, v57\n"
    "v_cvt_pk_bf16_f32 v57, v58, v59\n"
    "v_mfma_f32_16x16x32_bf16 v[120:123], a[24:27], v[136:139], v[120:123]\n"
    "ds_read_b64_tr_b16 a[120:121], v247 offset:26112\n"
    "v_cvt_pk_bf16_f32 v58, v64, v65\n"
    "v_mfma_f32_16x16x32_bf16 v[120:123], a[40:43], v[168:171], v[120:123]\n"
    "ds_read_b64_tr_b16 a[122:123], v247 offset:30208\n"
    "v_cvt_pk_bf16_f32 v59, v66, v67\n"
    "v_mfma_f32_16x16x32_bf16 v[120:123], a[56:59], v[200:203], v[120:123]\n"
    "v_xad_u32 v48, v48, v56, v57\n"
    "v_xad_u32 v48, v48, v58, v59\n"
    "v_mfma_f32_16x16x32_bf16 v[4:7], a[12:15], v[60:63], v[4:7]\n"
    "s_cmp_eq_u32 s71, 0\n"
    "s_cbranch_scc1 LPS010\n"
    "buffer_store_dwordx4 v[56:59], v240, s[56:59], s93 offen offset:1024 sc1\n"
    "LPS010_r:\n"
    "v_mfma_f32_16x16x32_bf16 v[4:7], a[28:31], v[136:139], v[4:7]\n"
    "ds_read_b64_tr_b16 a[76:77], v248 offset:1536\n"
    "ds_read_b64_tr_b16 a[78:79], v248 offset:5632\n"
    "v_mfma_f32_16x16x32_bf16 v[4:7], a[44:47], v[168:171], v[4:7]\n"
    "ds_read_b64_tr_b16 a[92:93], v248 offset:9728\n"
    "v_cvt_pk_bf16_f32 v120, v120, v121\n"
    "v_cvt_pk_bf16_f32 v121, v122, v123\n"
    "v_mfma_f32_16x16x32_bf16 v[4:7], a[60:63], v[200:203], v[4:7]\n"
    "v_mfma_f32_16x16x32_bf16 v[108:111], a[8:11], v[224:227], v[108:111]\n"
    "ds_read_b64_tr_b16 a[94:95], v248 offset:13824\n"
    "v_mfma_f32_16x16x32_bf16 v[108:111], a[24:27], v[132:135], v[108:111]\n"
    "s_nop 4\n"
    "v_cvt_pk_bf16_f32 v122, v4, v5\n"
    "v_cvt_pk_bf16_f32 v123, v6, v7\n"
    "v_mfma_f32_16x16x32_bf16 v[108:111], a[40:43], v[164:167], v[108:111]\n"
    "v_xad_u32 v49, v49, v120, v121\n"
    "v_xad_u32 v49, v49, v122, v123\n"
    "v_mfma_f32_16x16x32_bf16 v[108:111], a[56:59], v[196:199], v[108:111]\n"
    "ds_read_b64_tr_b16 a[108:109], v248 offset:17920\n"
    "v_mfma_f32_16x16x32_bf16 v[20:23], a[12:15], v[224:227], v[20:23]\n"
    "s_cmp_eq_u32 s71, 0\n"
    "s_cbranch_scc1 LPS011\n"
    "buffer_store_dwordx4 v[120:123], v240, s[56:59], s93 offen offset:3072 sc1\n"
    "LPS011_r:\n"
    "v_mfma_f32_16x16x32_bf16 v[20:23], a[28:31], v[132:135], v[20:23]\n"
    "ds_read_b64_tr_b16 a[110:111], v248 offset:22016\n"
    "s_nop 0\n"
    "v_cvt_pk_bf16_f32 v108, v108, v109\n"
    "v_mfma_f32_16x16x32_bf16 v[20:23], a[44:47], v[164:167], v[20:23]\n"
    "ds_read_b64_tr_b16 a[124:125], v248 offset:26112\n"
    "v_cvt_pk_bf16_f32 v109, v110, v111\n"
    "v_mfma_f32_16x16x32_bf16 v[20:23], a[60:63], v[196:199], v[20:23]\n"
    "v_mfma_f32_16x16x32_bf16 v[208:211], a[8:11], v[228:231], v[208:211]\n"
    "ds_read_b64_tr_b16 a[126:127], v248 offset:30208\n"
    "v_mfma_f32_16x16x32_bf16 v[208:211], a[24:27], v[140:143], v[208:211]\n"
    "s_nop 4\n"
    "v_cvt_pk_bf16_f32 v110, v20, v21\n"
    "v_cvt_pk_bf16_f32 v111, v22, v23\n"
    "v_mfma_f32_16x16x32_bf16 v[208:211], a[40:43], v[172:175], v[208:211]\n"
    "v_xad_u32 v50, v50, v108, v109\n"
    "v_xad_u32 v50, v50, v110, v111\n"
    "v_mfma_f32_16x16x32_bf16 v[208:211], a[56:59], v[204:207], v[208:211]\n"
    "s_cmp_eq_u32 s71, 0\n"
    "s_cbranch_scc1 LPS012\n"
    "buffer_store_dwordx4 v[108:111], v240, s[56:59], s28 offen offset:1024 sc1\n"
    "LPS012_r:\n"
    "v_mfma_f32_16x16x32_bf16 v[124:127], a[12:15], v[228:231], v[124:127]\n"
    "v_mfma_f32_16x16x32_bf16 v[124:127], a[28:31], v[140:143], v[124:127]\n"
    "v_mfma_f32_16x16x32_bf16 v[124:127], a[44:47], v[172:175], v[124:127]\n"
    "v_mfma_f32_16x16x32_bf16 v[124:127], a[60:63], v[204:207], v[124:127]\n"
    "s_waitcnt lgkmcnt(15)\n"
    "v_mfma_f32_16x16x32_bf16 v[72:75], a[64:67], a[192:195], v[72:75]\n"
    "ds_read_b64_tr_b16 a[0:1], v247 offset:33792\n"
    "v_cvt_pk_bf16_f32 v208, v208, v209\n"
    "v_mfma_f32_16x16x32_bf16 v[72:75], a[80:83], a[196:199], v[72:75]\n"
    "ds_read_b64_tr_b16 a[2:3], v247 offset:37888\n"
    "ds_read_b64_tr_b16 a[16:17], v247 offset:41984\n"
    "v_mfma_f32_16x16x32_bf16 v[72:75], a[96:99], a[200:203], v[72:75]\n"
    "v_mfma_f32_16x16x32_bf16 v[72:75], a[112:115], a[204:207], v[72:75]\n"
    "ds_read_b64_tr_b16 a[18:19], v247 offset:46080\n"
    "v_cvt_pk_bf16_f32 v209, v210, v211\n"
    "v_mfma_f32_16x16x32_bf16 v[80:83], a[68:71], a[192:195], v[80:83]\n"
    "v_mfma_f32_16x16x32_bf16 v[80:83], a[84:87], a[196:199], v[80:83]\n"
    "ds_read_b64_tr_b16 a[32:33], v247 offset:50176\n"
    "v_mfma_f32_16x16x32_bf16 v[80:83], a[100:103], a[200:203], v[80:83]\n"
    "v_mfma_f32_16x16x32_bf16 v[80:83], a[116:119], a[204:207], v[80:83]\n"
    "ds_read_b64_tr_b16 a[34:35], v247 offset:54272\n"
    "s_waitcnt lgkmcnt(15)\n"
    "v_mfma_f32_16x16x32_bf16 v[88:91], a[72:75], a[192:195], v[88:91]\n"
    "v_cvt_pk_bf16_f32 v210, v124, v125\n"
    "v_mfma_f32_16x16x32_bf16 v[88:91], a[88:91], a[196:199], v[88:91]\n"
    "ds_read_b64_tr_b16 a[48:49], v247 offset:58368\n"
    "v_mfma_f32_16x16x32_bf16 v[88:91], a[104:107], a[200:203], v[88:91]\n"
    "s_waitcnt lgkmcnt(15)\n"
    "v_mfma_f32_16x16x32_bf16 v[88:91], a[120:123], a[204:207], v[88:91]\n"
    "ds_read_b64_tr_b16 a[50:51], v247 offset:62464\n"
    "s_waitcnt lgkmcnt(14)\n"
    "v_mfma_f32_16x16x32_bf16 v[96:99], a[76:79], a[192:195], v[96:99]\n"
    "ds_read_b64_tr_b16 a[4:5], v248 offset:33792\n"
    "s_waitcnt lgkmcnt(13)\n"
    "v_mfma_f32_16x16x32_bf16 v[96:99], a[92:95], a[196:199], v[96:99]\n"
    "v_cvt_pk_bf16_f32 v211, v126, v127\n"
    "s_waitcnt lgkmcnt(11)\n"
    "v_mfma_f32_16x16x32_bf16 v[96:99], a[108:111], a[200:203], v[96:99]\n"
    "ds_read_b64_tr_b16 a[6:7], v248 offset:37888\n"
    "s_waitcnt lgkmcnt(10)\n"
    "v_mfma_f32_16x16x32_bf16 v[96:99], a[124:127], a[204:207], v[96:99]\n"
    "v_mfma_f32_16x16x32_bf16 v[12:15], a[64:67], a[208:211], v[12:15]\n"
    "ds_read_b64_tr_b16 a[20:21], v248 offset:41984\n"
    "v_mfma_f32_16x16x32_bf16 v[12:15], a[80:83], a[212:215], v[12:15]\n"
    "v_mfma_f32_16x16x32_bf16 v[12:15], a[96:99], a[216:219], v[12:15]\n"
    "ds_read_b64_tr_b16 a[22:23], v248 offset:46080\n"
    "v_mfma_f32_16x16x32_bf16 v[12:15], a[112:115], a[220:223], v[12:15]\n"
    "v_xad_u32 v51, v51, v208, v209\n"
    "v_mfma_f32_16x16x32_bf16 v[36:39], a[68:71], a[208:211], v[36:39]\n"
    "ds_read_b64_tr_b16 a[36:37], v248 offset:50176\n"
    "v_mfma_f32_16x16x32_bf16 v[36:39], a[84:87], a[212:215], v[36:39]\n"
    "v_mfma_f32_16x16x32_bf16 v[36:39], a[100:103], a[216:219], v[36:39]\n"
    "ds_read_b64_tr_b16 a[38:39], v248 offset:54272\n"
    "v_mfma_f32_16x16x32_bf16 v[36:39], a[116:119], a[220:223], v[36:39]\n"
    "v_mfma_f32_16x16x32_bf16 v[44:47], a[72:75], a[208:211], v[44:47]\n"
    "ds_read_b64_tr_b16 a[52:53], v248 offset:58368\n"
    "v_xad_u32 v51, v51, v210, v211\n"
    "v_mfma_f32_16x16x32_bf16 v[44:47], a[88:91], a[212:215], v[44:47]\n"
    "v_mfma_f32_16x16x32_bf16 v[44:47], a[104:107], a[216:219], v[44:47]\n"
    "ds_read_b64_tr_b16 a[54:55], v248 offset:62464\n"
    "v_mfma_f32_16x16x32_bf16 v[44:47], a[120:123], a[220:223], v[44:47]\n"
    "ds_read_b64_tr_b16 a[8:9], v247 offset:34304\n"
    "v_mfma_f32_16x16x32_bf16 v[68:71], a[76:79], a[208:211], v[68:71]\n"
    "v_mfma_f32_16x16x32_bf16 v[68:71], a[92:95], a[212:215], v[68:71]\n"
    "ds_read_b64_tr_b16 a[10:11], v247 offset:38400\n"
    "v_mfma_f32_16x16x32_bf16 v[68:71], a[108:111], a[216:219], v[68:71]\n"
    "s_cmp_eq_u32 s71, 0\n"
    "s_cbranch_scc1 LPS013\n"
    "buffer_store_dwordx4 v[208:211], v240, s[56:59], s28 offen offset:3072 sc1\n"
    "LPS013_r:\n"
    "v_mfma_f32_16x16x32_bf16 v[68:71], a[124:127], a[220:223], v[68:71]\n"
    "ds_read_b64_tr_b16 a[24:25], v247 offset:42496\n"
    "v_mfma_f32_16x16x32_bf16 v[52:55], a[64:67], v[148:151], v[52:55]\n"
    "v_mfma_f32_16x16x32_bf16 v[52:55], a[80:83], v[180:183], v[52:55]\n"
    "ds_read_b64_tr_b16 a[26:27], v247 offset:46592\n"
    "v_mfma_f32_16x16x32_bf16 v[52:55], a[96:99], v[212:215], v[52:55]\n"
    "v_mfma_f32_16x16x32_bf16 v[52:55], a[112:115], v[236:239], v[52:55]\n"
    "ds_read_b64_tr_b16 a[40:41], v247 offset:50688\n"
    "v_mfma_f32_16x16x32_bf16 v[84:87], a[68:71], v[148:151], v[84:87]\n"
    "v_mfma_f32_16x16x32_bf16 v[84:87], a[84:87], v[180:183], v[84:87]\n"
    "ds_read_b64_tr_b16 a[42:43], v247 offset:54784\n"
    "v_mfma_f32_16x16x32_bf16 v[84:87], a[100:103], v[212:215], v[84:87]\n"
    "v_mfma_f32_16x16x32_bf16 v[84:87], a[116:119], v[236:239], v[84:87]\n"
    "ds_read_b64_tr_b16 a[56:57], v247 offset:58880\n"
    "v_mfma_f32_16x16x32_bf16 v[116:119], a[72:75], v[148:151], v[116:119]\n"
    "v_mfma_f32_16x16x32_bf16 v[116:119], a[88:91], v[180:183], v[116:119]\n"
    "ds_read_b64_tr_b16 a[58:59], v247 offset:62976\n"
    "v_mfma_f32_16x16x32_bf16 v[116:119], a[104:107], v[212:215], v[116:119]\n"
    "ds_read_b64_tr_b16 a[12:13], v248 offset:34304\n"
    "v_mfma_f32_16x16x32_bf16 v[116:119], a[120:123], v[236:239], v[116:119]\n"
    "v_mfma_f32_16x16x32_bf16 v[92:95], a[76:79], v[148:151], v[92:95]\n"
    "ds_read_b64_tr_b16 a[14:15], v248 offset:38400\n"
    "v_mfma_f32_16x16x32_bf16 v[92:95], a[92:95], v[180:183], v[92:95]\n"
    "v_mfma_f32_16x16x32_bf16 v[92:95], a[108:111], v[212:215], v[92:95]\n"
    "ds_read_b64_tr_b16 a[28:29], v248 offset:42496\n"
    "v_mfma_f32_16x16x32_bf16 v[92:95], a[124:127], v[236:239], v[92:95]\n"
    "v_mfma_f32_16x16x32_bf16 v[152:155], a[64:67], v[156:159], v[152:155]\n"
    "ds_read_b64_tr_b16 a[30:31], v248 offset:46592\n"
    "v_mfma_f32_16x16x32_bf16 v[152:155], a[80:83], v[188:191], v[152:155]\n"
    "v_mfma_f32_16x16x32_bf16 v[152:155], a[96:99], v[220:223], v[152:155]\n"
    "ds_read_b64_tr_b16 a[44:45], v248 offset:50688\n"
    "v_mfma_f32_16x16x32_bf16 v[152:155], a[112:115], v[32:35], v[152:155]\n"
    "buffer_store_dwordx4 v[48:51], v240, s[56:59], s87 offen offset:0 sc1\n"
    "v_mfma_f32_16x16x32_bf16 v[184:187], a[68:71], v[156:159], v[184:187]\n"
    "ds_read_b64_tr_b16 a[46:47], v248 offset:54784\n"
    "v_mfma_f32_16x16x32_bf16 v[184:187], a[84:87], v[188:191], v[184:187]\n"
    "v_mfma_f32_16x16x32_bf16 v[184:187], a[100:103], v[220:223], v[184:187]\n"
    "ds_read_b64_tr_b16 a[60:61], v248 offset:58880\n"
    "v_mfma_f32_16x16x32_bf16 v[184:187], a[116:119], v[32:35], v[184:187]\n"
    "v_mfma_f32_16x16x32_bf16 v[216:219], a[72:75], v[156:159], v[216:219]\n"
    "ds_read_b64_tr_b16 a[62:63], v248 offset:62976\n"
    "v_mfma_f32_16x16x32_bf16 v[216:219], a[88:91], v[188:191], v[216:219]\n"
    "v_mfma_f32_16x16x32_bf16 v[216:219], a[104:107], v[220:223], v[216:219]\n"
    "v_mfma_f32_16x16x32_bf16 v[216:219], a[120:123], v[32:35], v[216:219]\n"
    "v_mfma_f32_16x16x32_bf16 v[232:235], a[76:79], v[156:159], v[232:235]\n"
    "v_mfma_f32_16x16x32_bf16 v[232:235], a[92:95], v[188:191], v[232:235]\n"
    "v_mfma_f32_16x16x32_bf16 v[232:235], a[108:111], v[220:223], v[232:235]\n"
    "v_mfma_f32_16x16x32_bf16 v[232:235], a[124:127], v[32:35], v[232:235]\n"
    "s_waitcnt lgkmcnt(0)\n"
    "s_barrier\n"
    "v_mfma_f32_16x16x32_bf16 v[72:75], a[0:3], v[28:31], v[72:75]\n"
    "ds_read_b64_tr_b16 a[64:65], v244 offset:0\n"
    "s_add_u32 s47, s81, 1\n"
    "v_mfma_f32_16x16x32_bf16 v[72:75], a[16:19], v[128:131], v[72:75]\n"
    "s_and_b32 s47, s47, 7\n"
    "s_lshl_b32 s40, s47, 16\n"
    "v_mfma_f32_16x16x32_bf16 v[72:75], a[32:35], v[160:163], v[72:75]\n"
    "ds_read_b64_tr_b16 a[66:67], v244 offset:1024\n"
    "s_add_u32 s40, s40, s83\n"
    "v_mfma_f32_16x16x32_bf16 v[72:75], a[48:51], v[192:195], v[72:75]\n"
    "v_mfma_f32_16x16x32_bf16 v[80:83], a[4:7], v[28:31], v[80:83]\n"
    "s_lshl_b32 s47, s47, 13\n"
    "v_mfma_f32_16x16x32_bf16 v[80:83], a[20:23], v[128:131], v[80:83]\n"
    "ds_read_b64_tr_b16 a[68:69], v250 offset:0\n"
    "s_add_u32 s47, s47, s88\n"
    "v_mfma_f32_16x16x32_bf16 v[80:83], a[36:39], v[160:163], v[80:83]\n"
    "v_mfma_f32_16x16x32_bf16 v[80:83], a[52:55], v[192:195], v[80:83]\n"
    "s_add_u32 s52, s81, 2\n"
    "v_mfma_f32_16x16x32_bf16 v[12:15], a[0:3], v[60:63], v[12:15]\n"
    "v_cvt_pk_bf16_f32 v72, v72, v73\n"
    "v_cvt_pk_bf16_f32 v73, v74, v75\n"
    "v_mfma_f32_16x16x32_bf16 v[12:15], a[16:19], v[136:139], v[12:15]\n"
    "s_nop 2\n"
    "v_cvt_pk_bf16_f32 v74, v80, v81\n"
    "v_cvt_pk_bf16_f32 v75, v82, v83\n"
    "v_mfma_f32_16x16x32_bf16 v[12:15], a[32:35], v[168:171], v[12:15]\n"
    "s_and_b32 s52, s52, 7\n"
    "v_xad_u32 v80, v72, v73, s68\n"
    "v_mfma_f32_16x16x32_bf16 v[12:15], a[48:51], v[200:203], v[12:15]\n"
    "ds_read_b64_tr_b16 a[70:71], v250 offset:1024\n"
    "v_xad_u32 v80, v80, v74, v75\n"
    "v_mfma_f32_16x16x32_bf16 v[36:39], a[4:7], v[60:63], v[36:39]\n"
    "s_lshl_b32 s41, s52, 16\n"
    "s_add_u32 s41, s41, s83\n"
    "v_mfma_f32_16x16x32_bf16 v[36:39], a[20:23], v[136:139], v[36:39]\n"
    "s_cmp_eq_u32 s72, 0\n"
    "s_cbranch_scc1 LPS100\n"
    "buffer_store_dwordx4 v[72:75], v240, s[56:59], s94 offen offset:0 sc1\n"
    "LPS100_r:\n"
    "v_mfma_f32_16x16x32_bf16 v[36:39], a[36:39], v[168:171], v[36:39]\n"
    "ds_read_b64_tr_b16 a[72:73], v244 offset:512\n"
    "s_lshl_b32 s52, s52, 13\n"
    "v_mfma_f32_16x16x32_bf16 v[36:39], a[52:55], v[200:203], v[36:39]\n"
    "s_add_u32 s52, s52, s88\n"
    "v_cvt_pk_bf16_f32 v12, v12, v13\n"
    "v_mfma_f32_16x16x32_bf16 v[52:55], a[0:3], v[224:227], v[52:55]\n"
    "s_add_u32 s53, s81, 3\n"
    "v_cvt_pk_bf16_f32 v13, v14, v15\n"
    "v_mfma_f32_16x16x32_bf16 v[52:55], a[16:19], v[132:135], v[52:55]\n"
    "s_nop 1\n"
    "v_cvt_pk_bf16_f32 v14, v36, v37\n"
    "v_cvt_pk_bf16_f32 v15, v38, v39\n"
    "v_mfma_f32_16x16x32_bf16 v[52:55], a[32:35], v[164:167], v[52:55]\n"
    "ds_read_b64_tr_b16 a[74:75], v244 offset:1536\n"
    "v_xad_u32 v81, v12, v13, s68\n"
    "v_mfma_f32_16x16x32_bf16 v[52:55], a[48:51], v[196:199], v[52:55]\n"
    "s_and_b32 s53, s53, 7\n"
    "v_xad_u32 v81, v81, v14, v15\n"
    "v_mfma_f32_16x16x32_bf16 v[84:87], a[4:7], v[224:227], v[84:87]\n"
    "s_cmp_eq_u32 s72, 0\n"
    "s_cbranch_scc1 LPS101\n"
    "buffer_store_dwordx4 v[12:15], v240, s[56:59], s94 offen offset:2048 sc1\n"
    "LPS101_r:\n"
    "v_mfma_f32_16x16x32_bf16 v[84:87], a[20:23], v[132:135], v[84:87]\n"
    "s_lshl_b32 s42, s53, 16\n"
    "ds_read_b64_tr_b16 a[76:77], v250 offset:512\n"
    "s_add_u32 s42, s42, s83\n"
    "v_mfma_f32_16x16x32_bf16 v[84:87], a[36:39], v[164:167], v[84:87]\n"
    "s_lshl_b32 s53, s53, 13\n"
    "v_cvt_pk_bf16_f32 v52, v52, v53\n"
    "v_mfma_f32_16x16x32_bf16 v[84:87], a[52:55], v[196:199], v[84:87]\n"
    "s_add_u32 s53, s53, s88\n"
    "v_cvt_pk_bf16_f32 v53, v54, v55\n"
    "v_mfma_f32_16x16x32_bf16 v[152:155], a[0:3], v[228:231], v[152:155]\n"
    "ds_read_b64_tr_b16 a[78:79], v250 offset:1536\n"
    "v_mfma_f32_16x16x32_bf16 v[152:155], a[16:19], v[140:143], v[152:155]\n"
    "s_nop 2\n"
    "v_cvt_pk_bf16_f32 v54, v84, v85\n"
    "v_cvt_pk_bf16_f32 v55, v86, v87\n"
    "v_mfma_f32_16x16x32_bf16 v[152:155], a[32:35], v[172:175], v[152:155]\n"
    "s_add_u32 s54, s81, 4\n"
    "v_xad_u32 v82, v52, v53, s68\n"
    "v_mfma_f32_16x16x32_bf16 v[152:155], a[48:51], v[204:207], v[152:155]\n"
    "s_and_b32 s54, s54, 7\n"
    "v_xad_u32 v82, v82, v54, v55\n"
    "v_mfma_f32_16x16x32_bf16 v[184:187], a[4:7], v[228:231], v[184:187]\n"
    "s_cmp_eq_u32 s72, 0\n"
    "s_cbranch_scc1 LPS102\n"
    "buffer_store_dwordx4 v[52:55], v240, s[56:59], s29 offen offset:0 sc1\n"
    "LPS102_r:\n"
    "v_mfma_f32_16x16x32_bf16 v[184:187], a[20:23], v[140:143], v[184:187]\n"
    "ds_read_b64_tr_b16 a[80:81], v244 offset:4096\n"
    "s_lshl_b32 s43, s54, 16\n"
    "v_mfma_f32_16x16x32_bf16 v[184:187], a[36:39], v[172:175], v[184:187]\n"
    "s_add_u32 s43, s43, s83\n"
    "v_cvt_pk_bf16_f32 v152, v152, v153\n"
    "v_mfma_f32_16x16x32_bf16 v[184:187], a[52:55], v[204:207], v[184:187]\n"
    "s_lshl_b32 s54, s54, 13\n"
    "v_cvt_pk_bf16_f32 v153, v154, v155\n"
    "v_mfma_f32_16x16x32_bf16 v[88:91], a[8:11], v[28:31], v[88:91]\n"
    "ds_read_b64_tr_b16 a[82:83], v244 offset:5120\n"
    "v_mfma_f32_16x16x32_bf16 v[88:91], a[24:27], v[128:131], v[88:91]\n"
    "s_nop 2\n"
    "v_cvt_pk_bf16_f32 v154, v184, v185\n"
    "v_cvt_pk_bf16_f32 v155, v186, v187\n"
    "v_mfma_f32_16x16x32_bf16 v[88:91], a[40:43], v[160:163], v[88:91]\n"
    "s_add_u32 s54, s54, s88\n"
    "v_xad_u32 v83, v152, v153, s68\n"
    "v_mfma_f32_16x16x32_bf16 v[88:91], a[56:59], v[192:195], v[88:91]\n"
    "s_add_u32 s55, s81, 5\n"
    "v_xad_u32 v83, v83, v154, v155\n"
    "v_mfma_f32_16x16x32_bf16 v[96:99], a[12:15], v[28:31], v[96:99]\n"
    "s_cmp_eq_u32 s72, 0\n"
    "s_cbranch_scc1 LPS103\n"
    "buffer_store_dwordx4 v[152:155], v240, s[56:59], s29 offen offset:2048 sc1\n"
    "LPS103_r:\n"
    "v_mfma_f32_16x16x32_bf16 v[96:99], a[28:31], v[128:131], v[96:99]\n"
    "ds_read_b64_tr_b16 a[84:85], v250 offset:4096\n"
    "s_and_b32 s55, s55, 7\n"
    "v_mfma_f32_16x16x32_bf16 v[96:99], a[44:47], v[160:163], v[96:99]\n"
    "s_lshl_b32 s44, s55, 16\n"
    "v_cvt_pk_bf16_f32 v88, v88, v89\n"
    "v_mfma_f32_16x16x32_bf16 v[96:99], a[60:63], v[192:195], v[96:99]\n"
    "s_add_u32 s44, s44, s83\n"
    "v_cvt_pk_bf16_f32 v89, v90, v91\n"
    "v_mfma_f32_16x16x32_bf16 v[44:47], a[8:11], v[60:63], v[44:47]\n"
    "ds_read_b64_tr_b16 a[86:87], v250 offset:5120\n"
    "s_lshl_b32 s55, s55, 13\n"
    "v_mfma_f32_16x16x32_bf16 v[44:47], a[24:27], v[136:139], v[44:47]\n"
    "s_nop 1\n"
    "v_cvt_pk_bf16_f32 v90, v96, v97\n"
    "v_cvt_pk_bf16_f32 v91, v98, v99\n"
    "v_mfma_f32_16x16x32_bf16 v[44:47], a[40:43], v[168:171], v[44:47]\n"
    "s_add_u32 s55, s55, s88\n"
    "v_xad_u32 v80, v80, v88, v89\n"
    "v_xad_u32 v80, v80, v90, v91\n"
    "v_mfma_f32_16x16x32_bf16 v[44:47], a[56:59], v[200:203], v[44:47]\n"
    "s_cmp_eq_u32 s72, 0\n"
    "s_cbranch_scc1 LPS110\n"
    "buffer_store_dwordx4 v[88:91], v240, s[56:59], s94 offen offset:1024 sc1\n"
    "LPS110_r:\n"
    "v_mfma_f32_16x16x32_bf16 v[68:71], a[12:15], v[60:63], v[68:71]\n"
    "ds_read_b64_tr_b16 a[88:89], v244 offset:4608\n"
    "s_add_u32 s84, s81, 6\n"
    "v_mfma_f32_16x16x32_bf16 v[68:71], a[28:31], v[136:139], v[68:71]\n"
    "s_and_b32 s84, s84, 7\n"
    "v_cvt_pk_bf16_f32 v44, v44, v45\n"
    "v_mfma_f32_16x16x32_bf16 v[68:71], a[44:47], v[168:171], v[68:71]\n"
    "ds_read_b64_tr_b16 a[90:91], v244 offset:5632\n"
    "s_lshl_b32 s45, s84, 16\n"
    "v_mfma_f32_16x16x32_bf16 v[68:71], a[60:63], v[200:203], v[68:71]\n"
    "v_cvt_pk_bf16_f32 v45, v46, v47\n"
    "v_mfma_f32_16x16x32_bf16 v[116:119], a[8:11], v[224:227], v[116:119]\n"
    "s_add_u32 s45, s45, s83\n"
    "v_mfma_f32_16x16x32_bf16 v[116:119], a[24:27], v[132:135], v[116:119]\n"
    "s_nop 3\n"
    "v_cvt_pk_bf16_f32 v46, v68, v69\n"
    "v_cvt_pk_bf16_f32 v47, v70, v71\n"
    "v_mfma_f32_16x16x32_bf16 v[116:119], a[40:43], v[164:167], v[116:119]\n"
    "s_lshl_b32 s84, s84, 13\n"
    "v_xad_u32 v81, v81, v44, v45\n"
    "v_mfma_f32_16x16x32_bf16 v[116:119], a[56:59], v[196:199], v[116:119]\n"
    "ds_read_b64_tr_b16 a[92:93], v250 offset:4608\n"
    "v_xad_u32 v81, v81, v46, v47\n"
    "v_mfma_f32_16x16x32_bf16 v[92:95], a[12:15], v[224:227], v[92:95]\n"
    "s_cmp_eq_u32 s72, 0\n"
    "s_cbranch_scc1 LPS111\n"
    "buffer_store_dwordx4 v[44:47], v240, s[56:59], s94 offen offset:3072 sc1\n"
    "LPS111_r:\n"
    "v_mfma_f32_16x16x32_bf16 v[92:95], a[28:31], v[132:135], v[92:95]\n"
    "s_add_u32 s84, s84, s88\n"
    "s_add_u32 s85, s81, 7\n"
    "v_mfma_f32_16x16x32_bf16 v[92:95], a[44:47], v[164:167], v[92:95]\n"
    "ds_read_b64_tr_b16 a[94:95], v250 offset:5632\n"
    "v_cvt_pk_bf16_f32 v116, v116, v117\n"
    "v_mfma_f32_16x16x32_bf16 v[92:95], a[60:63], v[196:199], v[92:95]\n"
    "s_and_b32 s85, s85, 7\n"
    "v_cvt_pk_bf16_f32 v117, v118, v119\n"
    "v_mfma_f32_16x16x32_bf16 v[216:219], a[8:11], v[228:231], v[216:219]\n"
    "s_lshl_b32 s46, s85, 16\n"
    "v_mfma_f32_16x16x32_bf16 v[216:219], a[24:27], v[140:143], v[216:219]\n"
    "s_nop 2\n"
    "v_cvt_pk_bf16_f32 v118, v92, v93\n"
    "v_cvt_pk_bf16_f32 v119, v94, v95\n"
    "v_mfma_f32_16x16x32_bf16 v[216:219], a[40:43], v[172:175], v[216:219]\n"
    "s_add_u32 s46, s46, s83\n"
    "v_xad_u32 v82, v82, v116, v117\n"
    "v_mfma_f32_16x16x32_bf16 v[216:219], a[56:59], v[204:207], v[216:219]\n"
    "s_lshl_b32 s85, s85, 13\n"
    "v_xad_u32 v82, v82, v118, v119\n"
    "v_mfma_f32_16x16x32_bf16 v[232:235], a[12:15], v[228:231], v[232:235]\n"
    "s_cmp_eq_u32 s72, 0\n"
    "s_cbranch_scc1 LPS112\n"
    "buffer_store_dwordx4 v[116:119], v240, s[56:59], s29 offen offset:1024 sc1\n"
    "LPS112_r:\n"
    "v_mfma_f32_16x16x32_bf16 v[232:235], a[28:31], v[140:143], v[232:235]\n"
    "s_add_u32 s85, s85, s88\n"
    "v_mfma_f32_16x16x32_bf16 v[232:235], a[44:47], v[172:175], v[232:235]\n"
    "v_mfma_f32_16x16x32_bf16 v[232:235], a[60:63], v[204:207], v[232:235]\n"
    "s_waitcnt lgkmcnt(14)\n"
    "v_mfma_f32_16x16x32_bf16 v[0:3], a[64:67], a[244:247], v[0:3]\n"
    "ds_read_b64_tr_b16 a[96:97], v244 offset:8192\n"
    "v_cvt_pk_bf16_f32 v216, v216, v217\n"
    "s_waitcnt lgkmcnt(7)\n"
    "v_mfma_f32_16x16x32_bf16 v[0:3], a[80:83], a[248:251], v[0:3]\n"
    "v_cvt_pk_bf16_f32 v217, v218, v219\n"
    "s_nop 0\n"
    "v_cvt_pk_bf16_f32 v218, v232, v233\n"
    "v_cvt_pk_bf16_f32 v219, v234, v235\n"
    "v_mfma_f32_16x16x32_bf16 v[8:11], a[68:71], a[244:247], v[8:11]\n"
    "v_xad_u32 v83, v83, v216, v217\n"
    "v_xad_u32 v83, v83, v218, v219\n"
    "s_waitcnt lgkmcnt(5)\n"
    "v_mfma_f32_16x16x32_bf16 v[8:11], a[84:87], a[248:251], v[8:11]\n"
    "ds_read_b64_tr_b16 a[98:99], v244 offset:9216\n"
    "ds_read_b64_tr_b16 a[100:101], v250 offset:8192\n"
    "ds_read_b64_tr_b16 a[102:103], v250 offset:9216\n"
    "v_mfma_f32_16x16x32_bf16 v[16:19], a[72:75], a[244:247], v[16:19]\n"
    "ds_read_b64_tr_b16 a[104:105], v244 offset:8704\n"
    "ds_read_b64_tr_b16 a[106:107], v244 offset:9728\n"
    "s_waitcnt lgkmcnt(8)\n"
    "v_mfma_f32_16x16x32_bf16 v[16:19], a[88:91], a[248:251], v[16:19]\n"
    "ds_read_b64_tr_b16 a[108:109], v250 offset:8704\n"
    "ds_read_b64_tr_b16 a[110:111], v250 offset:9728\n"
    "ds_read_b64_tr_b16 a[112:113], v244 offset:12288\n"
    "v_mfma_f32_16x16x32_bf16 v[24:27], a[76:79], a[244:247], v[24:27]\n"
    "ds_read_b64_tr_b16 a[114:115], v244 offset:13312\n"
    "ds_read_b64_tr_b16 a[116:117], v250 offset:12288\n"
    "s_waitcnt lgkmcnt(11)\n"
    "v_mfma_f32_16x16x32_bf16 v[24:27], a[92:95], a[248:251], v[24:27]\n"
    "ds_read_b64_tr_b16 a[118:119], v250 offset:13312\n"
    "ds_read_b64_tr_b16 a[120:121], v244 offset:12800\n"
    "ds_read_b64_tr_b16 a[122:123], v244 offset:13824\n"
    "ds_read_b64_tr_b16 a[124:125], v250 offset:12800\n"
    "ds_read_b64_tr_b16 a[126:127], v250 offset:13824\n"
    "s_waitcnt lgkmcnt(14)\n"
    "v_mfma_f32_16x16x32_bf16 v[0:3], a[96:99], a[252:255], v[0:3]\n"
    "s_cmp_eq_u32 s72, 0\n"
    "s_cbranch_scc1 LPS113\n"
    "buffer_store_dwordx4 v[216:219], v240, s[56:59], s29 offen offset:3072 sc1\n"
    "LPS113_r:\n"
    "s_waitcnt vmcnt(20) lgkmcnt(6)\n"
    "v_mfma_f32_16x16x32_bf16 v[0:3], a[112:115], a[128:131], v[0:3]\n"
    "buffer_store_dwordx4 v[80:83], v240, s[56:59], s87 offen offset:1024 sc1\n"
    "v_mfma_f32_16x16x32_bf16 v[8:11], a[100:103], a[252:255], v[8:11]\n"
    "ds_read_b64_tr_b16 a[64:65], v244 offset:16384\n"
    "ds_read_b64_tr_b16 a[66:67], v244 offset:17408\n"
    "ds_read_b64_tr_b16 a[68:69], v250 offset:16384\n"
    "s_waitcnt lgkmcnt(7)\n"
    "v_mfma_f32_16x16x32_bf16 v[8:11], a[116:119], a[128:131], v[8:11]\n"
    "ds_read_b64_tr_b16 a[70:71], v250 offset:17408\n"
    "ds_read_b64_tr_b16 a[72:73], v244 offset:16896\n"
    "v_mfma_f32_16x16x32_bf16 v[16:19], a[104:107], a[252:255], v[16:19]\n"
    "ds_read_b64_tr_b16 a[74:75], v244 offset:17920\n"
    "ds_read_b64_tr_b16 a[76:77], v250 offset:16896\n"
    "ds_read_b64_tr_b16 a[78:79], v250 offset:17920\n"
    "s_waitcnt lgkmcnt(10)\n"
    "v_mfma_f32_16x16x32_bf16 v[16:19], a[120:123], a[128:131], v[16:19]\n"
    "ds_read_b64_tr_b16 a[80:81], v244 offset:20480\n"
    "ds_read_b64_tr_b16 a[82:83], v244 offset:21504\n"
    "v_mfma_f32_16x16x32_bf16 v[24:27], a[108:111], a[252:255], v[24:27]\n"
    "ds_read_b64_tr_b16 a[84:85], v250 offset:20480\n"
    "ds_read_b64_tr_b16 a[86:87], v250 offset:21504\n"
    "ds_read_b64_tr_b16 a[88:89], v244 offset:20992\n"
    "s_waitcnt lgkmcnt(13)\n"
    "v_mfma_f32_16x16x32_bf16 v[24:27], a[124:127], a[128:131], v[24:27]\n"
    "ds_read_b64_tr_b16 a[90:91], v244 offset:22016\n"
    "ds_read_b64_tr_b16 a[92:93], v250 offset:20992\n"
    "ds_read_b64_tr_b16 a[94:95], v250 offset:22016\n"
    "s_waitcnt vmcnt(20) lgkmcnt(14)\n"
    "v_mfma_f32_16x16x32_bf16 v[0:3], a[64:67], a[132:135], v[0:3]\n"
    "ds_read_b64_tr_b16 a[96:97], v244 offset:24576\n"
    "ds_read_b64_tr_b16 a[98:99], v244 offset:25600\n"
    "s_waitcnt vmcnt(19) lgkmcnt(8)\n"
    "v_mfma_f32_16x16x32_bf16 v[0:3], a[80:83], a[136:139], v[0:3]\n"
    "ds_read_b64_tr_b16 a[100:101], v250 offset:24576\n"
    "ds_read_b64_tr_b16 a[102:103], v250 offset:25600\n"
    "ds_read_b64_tr_b16 a[104:105], v244 offset:25088\n"
    "v_mfma_f32_16x16x32_bf16 v[8:11], a[68:71], a[132:135], v[8:11]\n"
    "ds_read_b64_tr_b16 a[106:107], v244 offset:26112\n"
    "ds_read_b64_tr_b16 a[108:109], v250 offset:25088\n"
    "s_waitcnt lgkmcnt(11)\n"
    "v_mfma_f32_16x16x32_bf16 v[8:11], a[84:87], a[136:139], v[8:11]\n"
    "ds_read_b64_tr_b16 a[110:111], v250 offset:26112\n"
    "ds_read_b64_tr_b16 a[112:113], v244 offset:28672\n"
    "ds_read_b64_tr_b16 a[114:115], v244 offset:29696\n"
    "v_mfma_f32_16x16x32_bf16 v[16:19], a[72:75], a[132:135], v[16:19]\n"
    "ds_read_b64_tr_b16 a[116:117], v250 offset:28672\n"
    "ds_read_b64_tr_b16 a[118:119], v250 offset:29696\n"
    "s_waitcnt lgkmcnt(14)\n"
    "v_mfma_f32_16x16x32_bf16 v[16:19], a[88:91], a[136:139], v[16:19]\n"
    "ds_read_b64_tr_b16 a[120:121], v244 offset:29184\n"
    "ds_read_b64_tr_b16 a[122:123], v244 offset:30208\n"
    "ds_read_b64_tr_b16 a[124:125], v250 offset:29184\n"
    "v_mfma_f32_16x16x32_bf16 v[24:27], a[76:79], a[132:135], v[24:27]\n"
    "ds_read_b64_tr_b16 a[126:127], v250 offset:30208\n"
    "s_waitcnt lgkmcnt(15)\n"
    "v_mfma_f32_16x16x32_bf16 v[24:27], a[92:95], a[136:139], v[24:27]\n"
    "s_waitcnt vmcnt(10)\n"
    "s_waitcnt lgkmcnt(0)\n"
    "s_barrier\n"
    "v_mfma_f32_16x16x32_bf16 v[0:3], a[96:99], a[140:143], v[0:3]\n"
    "ds_read_b64_tr_b16 a[64:65], v242 offset:8192\n"
    "ds_read_b64_tr_b16 a[66:67], v242 offset:9216\n"
    "v_mfma_f32_16x16x32_bf16 v[0:3], a[112:115], a[144:147], v[0:3]\n"
    "ds_read_b64_tr_b16 a[68:69], v243 offset:8192\n"
    "ds_read_b64_tr_b16 a[70:71], v243 offset:9216\n"
    "ds_read_b64 v[80:81], v252\n"
    "v_mfma_f32_16x16x32_bf16 v[8:11], a[100:103], a[140:143], v[8:11]\n"
    "ds_read_b64_tr_b16 a[72:73], v242 offset:8704\n"
    "ds_read_b64_tr_b16 a[74:75], v242 offset:9728\n"
    "ds_read_b64_tr_b16 a[76:77], v243 offset:8704\n"
    "v_mfma_f32_16x16x32_bf16 v[8:11], a[116:119], a[144:147], v[8:11]\n"
    "ds_read_b64_tr_b16 a[78:79], v243 offset:9728\n"
    "ds_read_b64_tr_b16 a[80:81], v242 offset:12288\n"
    "v_xor_b32 v252, 32, v252\n"
    "v_mfma_f32_16x16x32_bf16 v[16:19], a[104:107], a[140:143], v[16:19]\n"
    "ds_read_b64_tr_b16 a[82:83], v242 offset:13312\n"
    "ds_read_b64_tr_b16 a[84:85], v243 offset:12288\n"
    "ds_read_b64_tr_b16 a[86:87], v243 offset:13312\n"
    "v_mfma_f32_16x16x32_bf16 v[16:19], a[120:123], a[144:147], v[16:19]\n"
    "ds_read_b64 v[82:83], v252\n"
    "ds_read_b64_tr_b16 a[88:89], v242 offset:12800\n"
    "ds_read_b64_tr_b16 a[90:91], v242 offset:13824\n"
    "v_mfma_f32_16x16x32_bf16 v[24:27], a[108:111], a[140:143], v[24:27]\n"
    "ds_read_b64_tr_b16 a[92:93], v243 offset:12800\n"
    "ds_read_b64_tr_b16 a[94:95], v243 offset:13824\n"
    "v_xor_b32 v252, 96, v252\n"
    "v_mfma_f32_16x16x32_bf16 v[24:27], a[124:127], a[144:147], v[24:27]\n"
    "ds_read_b64 v[96:97], v252\n"
    "s_waitcnt lgkmcnt(15)\n"
    "v_mfma_f32_16x16x32_bf16 v[0:3], a[64:67], a[148:151], v[0:3]\n"
    "ds_read_b64_tr_b16 a[96:97], v242 offset:16384\n"
    "ds_read_b64_tr_b16 a[98:99], v242 offset:17408\n"
    "ds_read_b64_tr_b16 a[100:101], v243 offset:16384\n"
    "s_waitcnt lgkmcnt(11)\n"
    "v_mfma_f32_16x16x32_bf16 v[0:3], a[80:83], a[152:155], v[0:3]\n"
    "ds_read_b64_tr_b16 a[102:103], v243 offset:17408\n"
    "ds_read_b64_tr_b16 a[104:105], v242 offset:16896\n"
    "ds_read_b64_tr_b16 a[106:107], v242 offset:17920\n"
    "ds_read_b64_tr_b16 a[108:109], v243 offset:16896\n"
    "v_mfma_f32_16x16x32_bf16 v[8:11], a[68:71], a[148:151], v[8:11]\n"
    "ds_read_b64_tr_b16 a[110:111], v243 offset:17920\n"
    "ds_read_b64_tr_b16 a[112:113], v242 offset:20480\n"
    "v_xor_b32 v252, 32, v252\n"
    "s_waitcnt lgkmcnt(15)\n"
    "v_mfma_f32_16x16x32_bf16 v[8:11], a[84:87], a[152:155], v[8:11]\n"
    "ds_read_b64_tr_b16 a[114:115], v242 offset:21504\n"
    "ds_read_b64_tr_b16 a[116:117], v243 offset:20480\n"
    "ds_read_b64 v[98:99], v252\n"
    "v_mfma_f32_16x16x32_bf16 v[16:19], a[72:75], a[148:151], v[16:19]\n"
    "ds_read_b64_tr_b16 a[118:119], v243 offset:21504\n"
    "ds_read_b64_tr_b16 a[120:121], v242 offset:20992\n"
    "ds_read_b64_tr_b16 a[122:123], v242 offset:22016\n"
    "s_waitcnt lgkmcnt(15)\n"
    "v_mfma_f32_16x16x32_bf16 v[16:19], a[88:91], a[152:155], v[16:19]\n"
    "s_lshl_b32 s63, s60, 11\n"
    "ds_read_b64_tr_b16 a[124:125], v243 offset:20992\n"
    "ds_read_b64_tr_b16 a[126:127], v243 offset:22016\n"
    "v_mfma_f32_16x16x32_bf16 v[24:27], a[76:79], a[148:151], v[24:27]\n"
    "v_add_u32 v249, s63, v249\n"
    "v_mfma_f32_16x16x32_bf16 v[24:27], a[92:95], a[152:155], v[24:27]\n"
    "s_waitcnt lgkmcnt(15)\n"
    "v_mfma_f32_16x16x32_bf16 v[0:3], a[96:99], a[156:159], v[0:3]\n"
    "ds_read_b64_tr_b16 a[64:65], v242 offset:32768\n"
    "ds_read_b64_tr_b16 a[66:67], v242 offset:33792\n"
    "ds_read_b64_tr_b16 a[68:69], v243 offset:32768\n"
    "s_waitcnt vmcnt(9) lgkmcnt(10)\n"
    "v_mfma_f32_16x16x32_bf16 v[0:3], a[112:115], a[160:163], v[0:3]\n"
    "ds_read_b64_tr_b16 a[70:71], v243 offset:33792\n"
    "ds_read_b64_tr_b16 a[72:73], v242 offset:33280\n"
    "ds_read_b64_tr_b16 a[74:75], v242 offset:34304\n"
    "v_mfma_f32_16x16x32_bf16 v[8:11], a[100:103], a[156:159], v[8:11]\n"
    "ds_read_b128 v[28:31], v249 offset:0\n"
    "ds_read_b64_tr_b16 a[76:77], v243 offset:33280\n"
    "ds_read_b64_tr_b16 a[78:79], v243 offset:34304\n"
    "ds_read_b64_tr_b16 a[80:81], v242 offset:36864\n"
    "s_waitcnt lgkmcnt(14)\n"
    "v_mfma_f32_16x16x32_bf16 v[8:11], a[116:119], a[160:163], v[8:11]\n"
    "ds_read_b128 v[128:131], v249 offset:1024\n"
    "ds_read_b64_tr_b16 a[82:83], v242 offset:37888\n"
    "ds_read_b64_tr_b16 a[84:85], v243 offset:36864\n"
    "v_mfma_f32_16x16x32_bf16 v[16:19], a[104:107], a[156:159], v[16:19]\n"
    "ds_read_b64_tr_b16 a[86:87], v243 offset:37888\n"
    "ds_read_b64_tr_b16 a[88:89], v242 offset:37376\n"
    "v_lshlrev_b32 v156, 16, v80\n"
    "s_waitcnt lgkmcnt(15)\n"
    "v_mfma_f32_16x16x32_bf16 v[16:19], a[120:123], a[160:163], v[16:19]\n"
    "ds_read_b64_tr_b16 a[90:91], v242 offset:38400\n"
    "ds_read_b64_tr_b16 a[92:93], v243 offset:37376\n"
    "ds_read_b64_tr_b16 a[94:95], v243 offset:38400\n"
    "v_mfma_f32_16x16x32_bf16 v[24:27], a[108:111], a[156:159], v[24:27]\n"
    "v_and_b32 v157, 0xffff0000, v80\n"
    "v_mfma_f32_16x16x32_bf16 v[24:27], a[124:127], a[160:163], v[24:27]\n"
    "v_lshlrev_b32 v158, 16, v81\n"
    "s_waitcnt vmcnt(8) lgkmcnt(15)\n"
    "v_mfma_f32_16x16x32_bf16 v[0:3], a[64:67], a[164:167], v[0:3]\n"
    "ds_read_b64_tr_b16 a[96:97], v242 offset:40960\n"
    "ds_read_b64_tr_b16 a[98:99], v242 offset:41984\n"
    "ds_read_b64_tr_b16 a[100:101], v243 offset:40960\n"
    "s_waitcnt vmcnt(7) lgkmcnt(9)\n"
    "v_mfma_f32_16x16x32_bf16 v[0:3], a[80:83], a[168:171], v[0:3]\n"
    "ds_read_b64_tr_b16 a[102:103], v243 offset:41984\n"
    "ds_read_b64_tr_b16 a[104:105], v242 offset:41472\n"
    "ds_read_b64_tr_b16 a[106:107], v242 offset:42496\n"
    "v_mfma_f32_16x16x32_bf16 v[8:11], a[68:71], a[164:167], v[8:11]\n"
    "ds_read_b64_tr_b16 a[108:109], v243 offset:41472\n"
    "ds_read_b64_tr_b16 a[110:111], v243 offset:42496\n"
    "v_and_b32 v159, 0xffff0000, v81\n"
    "s_waitcnt lgkmcnt(12)\n"
    "v_mfma_f32_16x16x32_bf16 v[8:11], a[84:87], a[168:171], v[8:11]\n"
    "ds_read_b64_tr_b16 a[112:113], v242 offset:45056\n"
    "ds_read_b64_tr_b16 a[114:115], v242 offset:46080\n"
    "ds_read_b64_tr_b16 a[116:117], v243 offset:45056\n"
    "ds_read_b64_tr_b16 a[118:119], v243 offset:46080\n"
    "v_mfma_f32_16x16x32_bf16 v[16:19], a[72:75], a[164:167], v[16:19]\n"
    "ds_read_b64_tr_b16 a[120:121], v242 offset:45568\n"
    "ds_read_b64_tr_b16 a[122:123], v242 offset:46592\n"
    "v_lshlrev_b32 v188, 16, v82\n"
    "s_waitcnt lgkmcnt(15)\n"
    "v_mfma_f32_16x16x32_bf16 v[16:19], a[88:91], a[168:171], v[16:19]\n"
    "ds_read_b64_tr_b16 a[124:125], v243 offset:45568\n"
    "ds_read_b64_tr_b16 a[126:127], v243 offset:46592\n"
    "v_and_b32 v189, 0xffff0000, v82\n"
    "v_mfma_f32_16x16x32_bf16 v[24:27], a[76:79], a[164:167], v[24:27]\n"
    "s_waitcnt lgkmcnt(15)\n"
    "v_mfma_f32_16x16x32_bf16 v[24:27], a[92:95], a[168:171], v[24:27]\n"
    "v_lshlrev_b32 v190, 16, v83\n"
    "s_waitcnt vmcnt(6) lgkmcnt(14)\n"
    "v_mfma_f32_16x16x32_bf16 v[0:3], a[96:99], a[172:175], v[0:3]\n"
    "ds_read_b64_tr_b16 a[64:65], v242 offset:49152\n"
    "ds_read_b64_tr_b16 a[66:67], v242 offset:50176\n"
    "ds_read_b64_tr_b16 a[68:69], v243 offset:49152\n"
    "s_waitcnt vmcnt(5) lgkmcnt(9)\n"
    "v_mfma_f32_16x16x32_bf16 v[0:3], a[112:115], a[176:179], v[0:3]\n"
    "ds_read_b64_tr_b16 a[70:71], v243 offset:50176\n"
    "ds_read_b64_tr_b16 a[72:73], v242 offset:49664\n"
    "ds_read_b64_tr_b16 a[74:75], v242 offset:50688\n"
    "v_mfma_f32_16x16x32_bf16 v[8:11], a[100:103], a[172:175], v[8:11]\n"
    "ds_read_b64_tr_b16 a[76:77], v243 offset:49664\n"
    "ds_read_b64_tr_b16 a[78:79], v243 offset:50688\n"
    "v_and_b32 v191, 0xffff0000, v83\n"
    "s_waitcnt lgkmcnt(12)\n"
    "v_mfma_f32_16x16x32_bf16 v[8:11], a[116:119], a[176:179], v[8:11]\n"
    "ds_read_b64_tr_b16 a[80:81], v242 offset:53248\n"
    "ds_read_b64_tr_b16 a[82:83], v242 offset:54272\n"
    "ds_read_b64_tr_b16 a[84:85], v243 offset:53248\n"
    "v_mfma_f32_16x16x32_bf16 v[16:19], a[104:107], a[172:175], v[16:19]\n"
    "ds_read_b64_tr_b16 a[86:87], v243 offset:54272\n"
    "ds_read_b64_tr_b16 a[88:89], v242 offset:53760\n"
    "ds_read_b64_tr_b16 a[90:91], v242 offset:54784\n"
    "v_lshlrev_b32 v220, 16, v96\n"
    "s_waitcnt lgkmcnt(15)\n"
    "v_mfma_f32_16x16x32_bf16 v[16:19], a[120:123], a[176:179], v[16:19]\n"
    "ds_read_b64_tr_b16 a[92:93], v243 offset:53760\n"
    "ds_read_b64_tr_b16 a[94:95], v243 offset:54784\n"
    "v_and_b32 v221, 0xffff0000, v96\n"
    "v_mfma_f32_16x16x32_bf16 v[24:27], a[108:111], a[172:175], v[24:27]\n"
    "v_lshlrev_b32 v222, 16, v97\n"
    "s_waitcnt lgkmcnt(15)\n"
    "v_mfma_f32_16x16x32_bf16 v[24:27], a[124:127], a[176:179], v[24:27]\n"
    "s_waitcnt vmcnt(4) lgkmcnt(14)\n"
    "v_mfma_f32_16x16x32_bf16 v[0:3], a[64:67], a[180:183], v[0:3]\n"
    "ds_read_b64_tr_b16 a[96:97], v242 offset:57344\n"
    "ds_read_b64_tr_b16 a[98:99], v242 offset:58368\n"
    "ds_read_b64_tr_b16 a[100:101], v243 offset:57344\n"
    "s_waitcnt vmcnt(3) lgkmcnt(9)\n"
    "v_mfma_f32_16x16x32_bf16 v[0:3], a[80:83], a[184:187], v[0:3]\n"
    "ds_read_b64_tr_b16 a[102:103], v243 offset:58368\n"
    "ds_read_b64_tr_b16 a[104:105], v242 offset:57856\n"
    "v_and_b32 v223, 0xffff0000, v97\n"
    "v_mfma_f32_16x16x32_bf16 v[8:11], a[68:71], a[180:183], v[8:11]\n"
    "ds_read_b64_tr_b16 a[106:107], v242 offset:58880\n"
    "v_lshlrev_b32 v32, 16, v98\n"
    "s_waitcnt lgkmcnt(10)\n"
    "v_mfma_f32_16x16x32_bf16 v[8:11], a[84:87], a[184:187], v[8:11]\n"
    "ds_read_b64_tr_b16 a[108:109], v243 offset:57856\n"
    "v_mfma_f32_16x16x32_bf16 v[16:19], a[72:75], a[180:183], v[16:19]\n"
    "ds_read_b64_tr_b16 a[110:111], v243 offset:58880\n"
    "v_and_b32 v33, 0xffff0000, v98\n"
    "s_waitcnt lgkmcnt(10)\n"
    "v_mfma_f32_16x16x32_bf16 v[16:19], a[88:91], a[184:187], v[16:19]\n"
    "v_mfma_f32_16x16x32_bf16 v[24:27], a[76:79], a[180:183], v[24:27]\n"
    "v_lshlrev_b32 v34, 16, v99\n"
    "s_waitcnt lgkmcnt(8)\n"
    "v_mfma_f32_16x16x32_bf16 v[24:27], a[92:95], a[184:187], v[24:27]\n"
    "s_waitcnt vmcnt(2) lgkmcnt(6)\n"
    "v_mfma_f32_16x16x32_bf16 v[0:3], a[96:99], a[188:191], v[0:3]\n"
    "v_and_b32 v35, 0xffff0000, v99\n"
    "s_waitcnt lgkmcnt(4)\n"
    "v_mfma_f32_16x16x32_bf16 v[8:11], a[100:103], a[188:191], v[8:11]\n"
    "s_waitcnt lgkmcnt(2)\n"
    "v_mfma_f32_16x16x32_bf16 v[16:19], a[104:107], a[188:191], v[16:19]\n"
    "s_waitcnt lgkmcnt(0)\n"
    "v_mfma_f32_16x16x32_bf16 v[24:27], a[108:111], a[188:191], v[24:27]\n"
    "buffer_load_dwordx4 v[160:163], v240, s[56:59], s40 offen offset:0 sc1\n"
    "buffer_load_dwordx4 v[192:195], v240, s[56:59], s40 offen offset:1024 sc1\n"
    "buffer_load_dword v72, v240, s[56:59], s47 offen sc1\n"
    "buffer_load_dwordx4 v[60:63], v240, s[56:59], s41 offen offset:0 sc1\n"
    "buffer_load_dwordx4 v[136:139], v240, s[56:59], s41 offen offset:1024 sc1\n"
    "buffer_load_dword v73, v240, s[56:59], s52 offen sc1\n"
    "buffer_load_dwordx4 v[168:171], v240, s[56:59], s42 offen offset:0 sc1\n"
    "buffer_load_dwordx4 v[200:203], v240, s[56:59], s42 offen offset:1024 sc1\n"
    "buffer_load_dword v74, v240, s[56:59], s53 offen sc1\n"
    "buffer_load_dwordx4 v[224:227], v240, s[56:59], s43 offen offset:0 sc1\n"
    "buffer_load_dwordx4 v[132:135], v240, s[56:59], s43 offen offset:1024 sc1\n"
    "buffer_load_dword v75, v240, s[56:59], s54 offen sc1\n"
    "buffer_load_dwordx4 v[164:167], v240, s[56:59], s44 offen offset:0 sc1\n"
    "buffer_load_dwordx4 v[196:199], v240, s[56:59], s44 offen offset:1024 sc1\n"
    "buffer_load_dword v88, v240, s[56:59], s55 offen sc1\n"
    "buffer_load_dwordx4 v[228:231], v240, s[56:59], s45 offen offset:0 sc1\n"
    "buffer_load_dwordx4 v[140:143], v240, s[56:59], s45 offen offset:1024 sc1\n"
    "buffer_load_dword v89, v240, s[56:59], s84 offen sc1\n"
    "buffer_load_dwordx4 v[172:175], v240, s[56:59], s46 offen offset:0 sc1\n"
    "buffer_load_dwordx4 v[204:207], v240, s[56:59], s46 offen offset:1024 sc1\n"
    "buffer_load_dword v90, v240, s[56:59], s85 offen sc1\n"
    "v_mul_f32 v36, 0xbfb8aa3b, v0\n"
    "v_mul_f32 v37, 0xbfb8aa3b, v1\n"
    "v_mul_f32 v38, 0xbfb8aa3b, v2\n"
    "v_mul_f32 v39, 0xbfb8aa3b, v3\n"
    "v_exp_f32 v36, v36\n"
    "v_exp_f32 v37, v37\n"
    "v_exp_f32 v38, v38\n"
    "v_exp_f32 v39, v39\n"
    "v_add_f32 v36, 1.0, v36\n"
    "v_add_f32 v37, 1.0, v37\n"
    "v_add_f32 v38, 1.0, v38\n"
    "v_add_f32 v39, 1.0, v39\n"
    "v_rcp_f32 v36, v36\n"
    "v_rcp_f32 v37, v37\n"
    "v_rcp_f32 v38, v38\n"
    "v_rcp_f32 v39, v39\n"
    "v_mul_f32 v0, v0, v36\n"
    "v_mul_f32 v1, v1, v37\n"
    "v_mul_f32 v2, v2, v38\n"
    "v_mul_f32 v3, v3, v39\n"
    "v_mul_f32 v68, 0xbfb8aa3b, v8\n"
    "v_mul_f32 v69, 0xbfb8aa3b, v9\n"
    "v_mul_f32 v70, 0xbfb8aa3b, v10\n"
    "v_mul_f32 v71, 0xbfb8aa3b, v11\n"
    "v_exp_f32 v68, v68\n"
    "v_exp_f32 v69, v69\n"
    "v_exp_f32 v70, v70\n"
    "v_exp_f32 v71, v71\n"
    "v_add_f32 v68, 1.0, v68\n"
    "v_add_f32 v69, 1.0, v69\n"
    "v_add_f32 v70, 1.0, v70\n"
    "v_add_f32 v71, 1.0, v71\n"
    "v_rcp_f32 v68, v68\n"
    "v_rcp_f32 v69, v69\n"
    "v_rcp_f32 v70, v70\n"
    "v_rcp_f32 v71, v71\n"
    "v_mul_f32 v8, v8, v68\n"
    "v_mul_f32 v9, v9, v69\n"
    "v_mul_f32 v10, v10, v70\n"
    "v_mul_f32 v11, v11, v71\n"
    "v_mul_f32 v84, 0xbfb8aa3b, v16\n"
    "v_mul_f32 v85, 0xbfb8aa3b, v17\n"
    "v_mul_f32 v86, 0xbfb8aa3b, v18\n"
    "v_mul_f32 v87, 0xbfb8aa3b, v19\n"
    "v_exp_f32 v84, v84\n"
    "v_exp_f32 v85, v85\n"
    "v_exp_f32 v86, v86\n"
    "v_exp_f32 v87, v87\n"
    "v_add_f32 v84, 1.0, v84\n"
    "v_add_f32 v85, 1.0, v85\n"
    "v_add_f32 v86, 1.0, v86\n"
    "v_add_f32 v87, 1.0, v87\n"
    "v_rcp_f32 v84, v84\n"
    "v_rcp_f32 v85, v85\n"
    "v_rcp_f32 v86, v86\n"
    "v_rcp_f32 v87, v87\n"
    "v_mul_f32 v16, v16, v84\n"
    "v_mul_f32 v17, v17, v85\n"
    "v_mul_f32 v18, v18, v86\n"
    "v_mul_f32 v19, v19, v87\n"
    "v_mul_f32 v92, 0xbfb8aa3b, v24\n"
    "v_mul_f32 v93, 0xbfb8aa3b, v25\n"
    "v_mul_f32 v94, 0xbfb8aa3b, v26\n"
    "v_mul_f32 v95, 0xbfb8aa3b, v27\n"
    "v_exp_f32 v92, v92\n"
    "v_exp_f32 v93, v93\n"
    "v_exp_f32 v94, v94\n"
    "v_exp_f32 v95, v95\n"
    "v_add_f32 v92, 1.0, v92\n"
    "v_add_f32 v93, 1.0, v93\n"
    "v_add_f32 v94, 1.0, v94\n"
    "v_add_f32 v95, 1.0, v95\n"
    "v_rcp_f32 v92, v92\n"
    "v_rcp_f32 v93, v93\n"
    "v_rcp_f32 v94, v94\n"
    "v_rcp_f32 v95, v95\n"
    "v_mul_f32 v24, v24, v92\n"
    "v_mul_f32 v25, v25, v93\n"
    "v_mul_f32 v26, v26, v94\n"
    "v_mul_f32 v27, v27, v95\n"
    "s_mov_b64 s[36:37], -1\n"
    "LACC:\n"
    "v_lshlrev_b32 v148, 16, v28\n"
    "v_and_b32 v149, 0xffff0000, v28\n"
    "v_lshlrev_b32 v150, 16, v29\n"
    "v_and_b32 v151, 0xffff0000, v29\n"
    "v_lshlrev_b32 v180, 16, v30\n"
    "v_and_b32 v181, 0xffff0000, v30\n"
    "v_lshlrev_b32 v182, 16, v31\n"
    "v_and_b32 v183, 0xffff0000, v31\n"
    "v_lshlrev_b32 v212, 16, v128\n"
    "v_and_b32 v213, 0xffff0000, v128\n"
    "v_lshlrev_b32 v214, 16, v129\n"
    "v_and_b32 v215, 0xffff0000, v129\n"
    "v_lshlrev_b32 v236, 16, v130\n"
    "v_and_b32 v237, 0xffff0000, v130\n"
    "v_lshlrev_b32 v238, 16, v131\n"
    "v_and_b32 v239, 0xffff0000, v131\n"
    "s_waitcnt vmcnt(20)\n"
    "v_lshlrev_b32 v36, 16, v160\n"
    "v_and_b32 v37, 0xffff0000, v160\n"
    "v_lshlrev_b32 v38, 16, v161\n"
    "v_and_b32 v39, 0xffff0000, v161\n"
    "v_lshlrev_b32 v68, 16, v162\n"
    "v_and_b32 v69, 0xffff0000, v162\n"
    "v_lshlrev_b32 v70, 16, v163\n"
    "v_and_b32 v71, 0xffff0000, v163\n"
    "s_waitcnt vmcnt(19)\n"
    "v_lshlrev_b32 v84, 16, v192\n"
    "v_and_b32 v85, 0xffff0000, v192\n"
    "v_lshlrev_b32 v86, 16, v193\n"
    "v_and_b32 v87, 0xffff0000, v193\n"
    "v_lshlrev_b32 v92, 16, v194\n"
    "v_and_b32 v93, 0xffff0000, v194\n"
    "v_lshlrev_b32 v94, 16, v195\n"
    "v_and_b32 v95, 0xffff0000, v195\n"
    "v_pk_add_f32 v[148:149], v[148:149], v[36:37]\n"
    "v_pk_add_f32 v[150:151], v[150:151], v[38:39]\n"
    "v_pk_add_f32 v[180:181], v[180:181], v[68:69]\n"
    "v_pk_add_f32 v[182:183], v[182:183], v[70:71]\n"
    "v_pk_add_f32 v[212:213], v[212:213], v[84:85]\n"
    "v_pk_add_f32 v[214:215], v[214:215], v[86:87]\n"
    "v_pk_add_f32 v[236:237], v[236:237], v[92:93]\n"
    "v_pk_add_f32 v[238:239], v[238:239], v[94:95]\n"
    "v_xad_u32 v185, v160, v161, s68\n"
    "v_xad_u32 v185, v185, v162, v163\n"
    "v_xad_u32 v185, v185, v192, v193\n"
    "v_xad_u32 v185, v185, v194, v195\n"
    "s_waitcnt vmcnt(18)\n"
    "v_xor_b32 v186, v72, v185\n"
    "s_waitcnt vmcnt(17)\n"
    "v_lshlrev_b32 v36, 16, v60\n"
    "v_and_b32 v37, 0xffff0000, v60\n"
    "v_lshlrev_b32 v38, 16, v61\n"
    "v_and_b32 v39, 0xffff0000, v61\n"
    "v_lshlrev_b32 v68, 16, v62\n"
    "v_and_b32 v69, 0xffff0000, v62\n"
    "v_lshlrev_b32 v70, 16, v63\n"
    "v_and_b32 v71, 0xffff0000, v63\n"
    "s_waitcnt vmcnt(16)\n"
    "v_lshlrev_b32 v84, 16, v136\n"
    "v_and_b32 v85, 0xffff0000, v136\n"
    "v_lshlrev_b32 v86, 16, v137\n"
    "v_and_b32 v87, 0xffff0000, v137\n"
    "v_lshlrev_b32 v92, 16, v138\n"
    "v_and_b32 v93, 0xffff0000, v138\n"
    "v_lshlrev_b32 v94, 16, v139\n"
    "v_and_b32 v95, 0xffff0000, v139\n"
    "v_pk_add_f32 v[148:149], v[148:149], v[36:37]\n"
    "v_pk_add_f32 v[150:151], v[150:151], v[38:39]\n"
    "v_pk_add_f32 v[180:181], v[180:181], v[68:69]\n"
    "v_pk_add_f32 v[182:183], v[182:183], v[70:71]\n"
    "v_pk_add_f32 v[212:213], v[212:213], v[84:85]\n"
    "v_pk_add_f32 v[214:215], v[214:215], v[86:87]\n"
    "v_pk_add_f32 v[236:237], v[236:237], v[92:93]\n"
    "v_pk_add_f32 v[238:239], v[238:239], v[94:95]\n"
    "v_xad_u32 v184, v60, v61, s68\n"
    "v_xad_u32 v184, v184, v62, v63\n"
    "v_xad_u32 v184, v184, v136, v137\n"
    "v_xad_u32 v184, v184, v138, v139\n"
    "s_waitcnt vmcnt(15)\n"
    "v_xor_b32 v184, v73, v184\n"
    "v_or_b32 v186, v186, v184\n"
    "s_waitcnt vmcnt(14)\n"
    "v_lshlrev_b32 v36, 16, v168\n"
    "v_and_b32 v37, 0xffff0000, v168\n"
    "v_lshlrev_b32 v38, 16, v169\n"
    "v_and_b32 v39, 0xffff0000, v169\n"
    "v_lshlrev_b32 v68, 16, v170\n"
    "v_and_b32 v69, 0xffff0000, v170\n"
    "v_lshlrev_b32 v70, 16, v171\n"
    "v_and_b32 v71, 0xffff0000, v171\n"
    "s_waitcnt vmcnt(13)\n"
    "v_lshlrev_b32 v84, 16, v200\n"
    "v_and_b32 v85, 0xffff0000, v200\n"
    "v_lshlrev_b32 v86, 16, v201\n"
    "v_and_b32 v87, 0xffff0000, v201\n"
    "v_lshlrev_b32 v92, 16, v202\n"
    "v_and_b32 v93, 0xffff0000, v202\n"
    "v_lshlrev_b32 v94, 16, v203\n"
    "v_and_b32 v95, 0xffff0000, v203\n"
    "v_pk_add_f32 v[148:149], v[148:149], v[36:37]\n"
    "v_pk_add_f32 v[150:151], v[150:151], v[38:39]\n"
    "v_pk_add_f32 v[180:181], v[180:181], v[68:69]\n"
    "v_pk_add_f32 v[182:183], v[182:183], v[70:71]\n"
    "v_pk_add_f32 v[212:213], v[212:213], v[84:85]\n"
    "v_pk_add_f32 v[214:215], v[214:215], v[86:87]\n"
    "v_pk_add_f32 v[236:237], v[236:237], v[92:93]\n"
    "v_pk_add_f32 v[238:239], v[238:239], v[94:95]\n"
    "v_xad_u32 v185, v168, v169, s68\n"
    "v_xad_u32 v185, v185, v170, v171\n"
    "v_xad_u32 v185, v185, v200, v201\n"
    "v_xad_u32 v185, v185, v202, v203\n"
    "s_waitcnt vmcnt(12)\n"
    "v_xor_b32 v185, v74, v185\n"
    "v_or_b32 v186, v186, v185\n"
    "s_waitcnt vmcnt(11)\n"
    "v_lshlrev_b32 v36, 16, v224\n"
    "v_and_b32 v37, 0xffff0000, v224\n"
    "v_lshlrev_b32 v38, 16, v225\n"
    "v_and_b32 v39, 0xffff0000, v225\n"
    "v_lshlrev_b32 v68, 16, v226\n"
    "v_and_b32 v69, 0xffff0000, v226\n"
    "v_lshlrev_b32 v70, 16, v227\n"
    "v_and_b32 v71, 0xffff0000, v227\n"
    "s_waitcnt vmcnt(10)\n"
    "v_lshlrev_b32 v84, 16, v132\n"
    "v_and_b32 v85, 0xffff0000, v132\n"
    "v_lshlrev_b32 v86, 16, v133\n"
    "v_and_b32 v87, 0xffff0000, v133\n"
    "v_lshlrev_b32 v92, 16, v134\n"
    "v_and_b32 v93, 0xffff0000, v134\n"
    "v_lshlrev_b32 v94, 16, v135\n"
    "v_and_b32 v95, 0xffff0000, v135\n"
    "v_pk_add_f32 v[148:149], v[148:149], v[36:37]\n"
    "v_pk_add_f32 v[150:151], v[150:151], v[38:39]\n"
    "v_pk_add_f32 v[180:181], v[180:181], v[68:69]\n"
    "v_pk_add_f32 v[182:183], v[182:183], v[70:71]\n"
    "v_pk_add_f32 v[212:213], v[212:213], v[84:85]\n"
    "v_pk_add_f32 v[214:215], v[214:215], v[86:87]\n"
    "v_pk_add_f32 v[236:237], v[236:237], v[92:93]\n"
    "v_pk_add_f32 v[238:239], v[238:239], v[94:95]\n"
    "v_xad_u32 v184, v224, v225, s68\n"
    "v_xad_u32 v184, v184, v226, v227\n"
    "v_xad_u32 v184, v184, v132, v133\n"
    "v_xad_u32 v184, v184, v134, v135\n"
    "s_waitcnt vmcnt(9)\n"
    "v_xor_b32 v184, v75, v184\n"
    "v_or_b32 v186, v186, v184\n"
    "s_waitcnt vmcnt(8)\n"
    "v_lshlrev_b32 v36, 16, v164\n"
    "v_and_b32 v37, 0xffff0000, v164\n"
    "v_lshlrev_b32 v38, 16, v165\n"
    "v_and_b32 v39, 0xffff0000, v165\n"
    "v_lshlrev_b32 v68, 16, v166\n"
    "v_and_b32 v69, 0xffff0000, v166\n"
    "v_lshlrev_b32 v70, 16, v167\n"
    "v_and_b32 v71, 0xffff0000, v167\n"
    "s_waitcnt vmcnt(7)\n"
    "v_lshlrev_b32 v84, 16, v196\n"
    "v_and_b32 v85, 0xffff0000, v196\n"
    "v_lshlrev_b32 v86, 16, v197\n"
    "v_and_b32 v87, 0xffff0000, v197\n"
    "v_lshlrev_b32 v92, 16, v198\n"
    "v_and_b32 v93, 0xffff0000, v198\n"
    "v_lshlrev_b32 v94, 16, v199\n"
    "v_and_b32 v95, 0xffff0000, v199\n"
    "v_pk_add_f32 v[148:149], v[148:149], v[36:37]\n"
    "v_pk_add_f32 v[150:151], v[150:151], v[38:39]\n"
    "v_pk_add_f32 v[180:181], v[180:181], v[68:69]\n"
    "v_pk_add_f32 v[182:183], v[182:183], v[70:71]\n"
    "v_pk_add_f32 v[212:213], v[212:213], v[84:85]\n"
    "v_pk_add_f32 v[214:215], v[214:215], v[86:87]\n"
    "v_pk_add_f32 v[236:237], v[236:237], v[92:93]\n"
    "v_pk_add_f32 v[238:239], v[238:239], v[94:95]\n"
    "v_xad_u32 v185, v164, v165, s68\n"
    "v_xad_u32 v185, v185, v166, v167\n"
    "v_xad_u32 v185, v185, v196, v197\n"
    "v_xad_u32 v185, v185, v198, v199\n"
    "s_waitcnt vmcnt(6)\n"
    "v_xor_b32 v185, v88, v185\n"
    "v_or_b32 v186, v186, v185\n"
    "s_waitcnt vmcnt(5)\n"
    "v_lshlrev_b32 v36, 16, v228\n"
    "v_and_b32 v37, 0xffff0000, v228\n"
    "v_lshlrev_b32 v38, 16, v229\n"
    "v_and_b32 v39, 0xffff0000, v229\n"
    "v_lshlrev_b32 v68, 16, v230\n"
    "v_and_b32 v69, 0xffff0000, v230\n"
    "v_lshlrev_b32 v70, 16, v231\n"
    "v_and_b32 v71, 0xffff0000, v231\n"
    "s_waitcnt vmcnt(4)\n"
    "v_lshlrev_b32 v84, 16, v140\n"
    "v_and_b32 v85, 0xffff0000, v140\n"
    "v_lshlrev_b32 v86, 16, v141\n"
    "v_and_b32 v87, 0xffff0000, v141\n"
    "v_lshlrev_b32 v92, 16, v142\n"
    "v_and_b32 v93, 0xffff0000, v142\n"
    "v_lshlrev_b32 v94, 16, v143\n"
    "v_and_b32 v95, 0xffff0000, v143\n"
    "v_pk_add_f32 v[148:149], v[148:149], v[36:37]\n"
    "v_pk_add_f32 v[150:151], v[150:151], v[38:39]\n"
    "v_pk_add_f32 v[180:181], v[180:181], v[68:69]\n"
    "v_pk_add_f32 v[182:183], v[182:183], v[70:71]\n"
    "v_pk_add_f32 v[212:213], v[212:213], v[84:85]\n"
    "v_pk_add_f32 v[214:215], v[214:215], v[86:87]\n"
    "v_pk_add_f32 v[236:237], v[236:237], v[92:93]\n"
    "v_pk_add_f32 v[238:239], v[238:239], v[94:95]\n"
    "v_xad_u32 v184, v228, v229, s68\n"
    "v_xad_u32 v184, v184, v230, v231\n"
    "v_xad_u32 v184, v184, v140, v141\n"
    "v_xad_u32 v184, v184, v142, v143\n"
    "s_waitcnt vmcnt(3)\n"
    "v_xor_b32 v184, v89, v184\n"
    "v_or_b32 v186, v186, v184\n"
    "s_waitcnt vmcnt(2)\n"
    "v_lshlrev_b32 v36, 16, v172\n"
    "v_and_b32 v37, 0xffff0000, v172\n"
    "v_lshlrev_b32 v38, 16, v173\n"
    "v_and_b32 v39, 0xffff0000, v173\n"
    "v_lshlrev_b32 v68, 16, v174\n"
    "v_and_b32 v69, 0xffff0000, v174\n"
    "v_lshlrev_b32 v70, 16, v175\n"
    "v_and_b32 v71, 0xffff0000, v175\n"
    "s_waitcnt vmcnt(1)\n"
    "v_lshlrev_b32 v84, 16, v204\n"
    "v_and_b32 v85, 0xffff0000, v204\n"
    "v_lshlrev_b32 v86, 16, v205\n"
    "v_and_b32 v87, 0xffff0000, v205\n"
    "v_lshlrev_b32 v92, 16, v206\n"
    "v_and_b32 v93, 0xffff0000, v206\n"
    "v_lshlrev_b32 v94, 16, v207\n"
    "v_and_b32 v95, 0xffff0000, v207\n"
    "v_pk_add_f32 v[148:149], v[148:149], v[36:37]\n"
    "v_pk_add_f32 v[150:151], v[150:151], v[38:39]\n"
    "v_pk_add_f32 v[180:181], v[180:181], v[68:69]\n"
    "v_pk_add_f32 v[182:183], v[182:183], v[70:71]\n"
    "v_pk_add_f32 v[212:213], v[212:213], v[84:85]\n"
    "v_pk_add_f32 v[214:215], v[214:215], v[86:87]\n"
    "v_pk_add_f32 v[236:237], v[236:237], v[92:93]\n"
    "v_pk_add_f32 v[238:239], v[238:239], v[94:95]\n"
    "v_xad_u32 v185, v172, v173, s68\n"
    "v_xad_u32 v185, v185, v174, v175\n"
    "v_xad_u32 v185, v185, v204, v205\n"
    "v_xad_u32 v185, v185, v206, v207\n"
    "s_waitcnt vmcnt(0)\n"
    "v_xor_b32 v185, v90, v185\n"
    "v_or_b32 v186, v186, v185\n"
    "v_cmp_ne_u32 vcc, 0, v186\n"
    "s_nop 0\n"
    "s_and_b64 vcc, vcc, s[36:37]\n"
    "s_cbranch_scc1 LSPP\n"
    "v_fmac_f32 v156, v0, v148\n"
    "v_fmac_f32 v157, v1, v149\n"
    "v_fmac_f32 v158, v2, v150\n"
    "v_fmac_f32 v159, v3, v151\n"
    "v_cvt_pk_bf16_f32 v156, v156, v157\n"
    "v_cvt_pk_bf16_f32 v157, v158, v159\n"
    "buffer_store_dwordx2 v[156:157], v241, s[12:15], 0 offen offset:0 \n"
    "v_fmac_f32 v188, v8, v180\n"
    "v_fmac_f32 v189, v9, v181\n"
    "v_fmac_f32 v190, v10, v182\n"
    "v_fmac_f32 v191, v11, v183\n"
    "v_cvt_pk_bf16_f32 v188, v188, v189\n"
    "v_cvt_pk_bf16_f32 v189, v190, v191\n"
    "buffer_store_dwordx2 v[188:189], v241, s[12:15], 0 offen offset:32 \n"
    "v_fmac_f32 v220, v16, v212\n"
    "v_fmac_f32 v221, v17, v213\n"
    "v_fmac_f32 v222, v18, v214\n"
    "v_fmac_f32 v223, v19, v215\n"
    "v_cvt_pk_bf16_f32 v220, v220, v221\n"
    "v_cvt_pk_bf16_f32 v221, v222, v223\n"
    "buffer_store_dwordx2 v[220:221], v241, s[12:15], 0 offen offset:64 \n"
    "v_fmac_f32 v32, v24, v236\n"
    "v_fmac_f32 v33, v25, v237\n"
    "v_fmac_f32 v34, v26, v238\n"
    "v_fmac_f32 v35, v27, v239\n"
    "v_cvt_pk_bf16_f32 v32, v32, v33\n"
    "v_cvt_pk_bf16_f32 v33, v34, v35\n"
    "buffer_store_dwordx2 v[32:33], v241, s[12:15], 0 offen offset:96 \n"
    "s_waitcnt vmcnt(0) lgkmcnt(0)\n"
    "s_branch LEND\n"
    "LSPA:\n"
    "s_mov_b32 s82, 0x40000\n"
    "LSPA_l:\n"
    "s_sleep 1\n"
    "buffer_load_dwordx2 v[224:225], v254, s[56:59], 0 offen sc1\n"
    "s_waitcnt vmcnt(0)\n"
    "v_cmp_ne_u64 vcc, s[68:69], v[224:225]\n"
    "s_nop 1\n"
    "s_cbranch_vccz LSPA_r\n"
    "s_sub_u32 s82, s82, 1\n"
    "s_cmp_lg_u32 s82, 0\n"
    "s_cbranch_scc1 LSPA_l\n"
    "s_branch LSPA_r\n"
    "LPH:\n"
    "s_mov_b64 exec, 1\n"
    "s_nop 0\n"
    "buffer_store_dwordx2 a[248:249], v254, s[56:59], s87 offen sc1\n"
    "s_mov_b64 exec, -1\n"
    "s_add_u32 s87, s87, 32768\n"
    "s_branch LPH_r\n"
    "LSPH:\n"
    "s_mov_b32 s82, 0x40000\n"
    "LSPH_l:\n"
    "s_sleep 1\n"
    "buffer_load_dwordx2 v[252:253], v250, s[56:59], 0 offen sc1\n"
    "s_waitcnt vmcnt(0)\n"
    "v_cmp_ne_u64 vcc, s[68:69], v[252:253]\n"
    "s_nop 1\n"
    "s_cbranch_vccz LSPH_r\n"
    "s_sub_u32 s82, s82, 1\n"
    "s_cmp_lg_u32 s82, 0\n"
    "s_cbranch_scc1 LSPH_l\n"
    "s_branch LSPH_r\n"
    "LFB:\n"
    "s_nop 15\n"
    "s_waitcnt vmcnt(0) lgkmcnt(0)\n"
    "s_lshl_b32 s73, s60, 12\n"
    "s_sub_u32 s28, s48, s73\n"
    "s_subb_u32 s29, s49, 0\n"
    "s_add_u32 s28, s28, 1024\n"
    "s_addc_u32 s29, s29, 0\n"
    "s_mov_b32 s30, 131072\n"
    "s_mov_b32 s31, 0x20000\n"
    "v_and_b32 v254, 0xf0, v240\n"
    "v_lshrrev_b32 v253, 8, v240\n"
    "v_lshlrev_b32 v252, 4, v254\n"
    "v_lshl_add_u32 v252, v253, 4, v252\n"
    "v_bfe_u32 v253, v240, 6, 3\n"
    "v_mul_u32_u24 v253, 768, v253\n"
    "v_sub_u32 v253, v251, v253\n"
    "s_lshl_b32 s82, s60, 13\n"
    "s_add_u32 s82, s82, 32768\n"
    "v_add_u32 v254, s82, v242\n"
    "v_xor_b32 v249, 32, v254\n"
    "v_mov_b32 v20, 0xff800000\n"
    "v_mov_b32 v52, 0xff800000\n"
    "v_mov_b32 v84, 0xff800000\n"
    "v_mov_b32 v116, 0xff800000\n"
    "s_mov_b32 s73, 0\n"
    "LFB1:\n"
    "s_add_u32 s98, s73, 4096\n"
    "buffer_load_dwordx4 a[32:35], v252, s[44:47], s73 offen offset:0\n"
    "buffer_load_dwordx4 a[36:39], v252, s[44:47], s73 offen offset:64\n"
    "buffer_load_dwordx4 a[40:43], v252, s[44:47], s73 offen offset:128\n"
    "buffer_load_dwordx4 a[44:47], v252, s[44:47], s73 offen offset:192\n"
    "buffer_load_dwordx4 a[48:51], v252, s[44:47], s98 offen offset:0\n"
    "buffer_load_dwordx4 a[52:55], v252, s[44:47], s98 offen offset:64\n"
    "buffer_load_dwordx4 a[56:59], v252, s[44:47], s98 offen offset:128\n"
    "buffer_load_dwordx4 a[60:63], v252, s[44:47], s98 offen offset:192\n"
    "s_waitcnt vmcnt(0)\n"
    "v_mfma_f32_16x16x32_bf16 v[4:7], a[32:35], v[0:3], 0\n"
    "v_mfma_f32_16x16x32_bf16 v[4:7], a[36:39], v[8:11], v[4:7]\n"
    "v_mfma_f32_16x16x32_bf16 v[4:7], a[40:43], v[16:19], v[4:7]\n"
    "v_mfma_f32_16x16x32_bf16 v[4:7], a[44:47], v[24:27], v[4:7]\n"
    "v_mfma_f32_16x16x32_bf16 v[12:15], a[48:51], v[0:3], 0\n"
    "v_mfma_f32_16x16x32_bf16 v[12:15], a[52:55], v[8:11], v[12:15]\n"
    "v_mfma_f32_16x16x32_bf16 v[12:15], a[56:59], v[16:19], v[12:15]\n"
    "v_mfma_f32_16x16x32_bf16 v[12:15], a[60:63], v[24:27], v[12:15]\n"
    "v_mfma_f32_16x16x32_bf16 v[36:39], a[32:35], v[32:35], 0\n"
    "v_mfma_f32_16x16x32_bf16 v[36:39], a[36:39], v[40:43], v[36:39]\n"
    "v_mfma_f32_16x16x32_bf16 v[36:39], a[40:43], v[48:51], v[36:39]\n"
    "v_mfma_f32_16x16x32_bf16 v[36:39], a[44:47], v[56:59], v[36:39]\n"
    "v_mfma_f32_16x16x32_bf16 v[44:47], a[48:51], v[32:35], 0\n"
    "v_mfma_f32_16x16x32_bf16 v[44:47], a[52:55], v[40:43], v[44:47]\n"
    "v_mfma_f32_16x16x32_bf16 v[44:47], a[56:59], v[48:51], v[44:47]\n"
    "v_mfma_f32_16x16x32_bf16 v[44:47], a[60:63], v[56:59], v[44:47]\n"
    "v_mfma_f32_16x16x32_bf16 v[68:71], a[32:35], v[64:67], 0\n"
    "v_mfma_f32_16x16x32_bf16 v[68:71], a[36:39], v[72:75], v[68:71]\n"
    "v_mfma_f32_16x16x32_bf16 v[68:71], a[40:43], v[80:83], v[68:71]\n"
    "v_mfma_f32_16x16x32_bf16 v[68:71], a[44:47], v[88:91], v[68:71]\n"
    "v_mfma_f32_16x16x32_bf16 v[76:79], a[48:51], v[64:67], 0\n"
    "v_mfma_f32_16x16x32_bf16 v[76:79], a[52:55], v[72:75], v[76:79]\n"
    "v_mfma_f32_16x16x32_bf16 v[76:79], a[56:59], v[80:83], v[76:79]\n"
    "v_mfma_f32_16x16x32_bf16 v[76:79], a[60:63], v[88:91], v[76:79]\n"
    "v_mfma_f32_16x16x32_bf16 v[100:103], a[32:35], v[96:99], 0\n"
    "v_mfma_f32_16x16x32_bf16 v[100:103], a[36:39], v[104:107], v[100:103]\n"
    "v_mfma_f32_16x16x32_bf16 v[100:103], a[40:43], v[112:115], v[100:103]\n"
    "v_mfma_f32_16x16x32_bf16 v[100:103], a[44:47], v[120:123], v[100:103]\n"
    "v_mfma_f32_16x16x32_bf16 v[108:111], a[48:51], v[96:99], 0\n"
    "v_mfma_f32_16x16x32_bf16 v[108:111], a[52:55], v[104:107], v[108:111]\n"
    "v_mfma_f32_16x16x32_bf16 v[108:111], a[56:59], v[112:115], v[108:111]\n"
    "v_mfma_f32_16x16x32_bf16 v[108:111], a[60:63], v[120:123], v[108:111]\n"
    "s_nop 15\n"
    "v_max3_f32 v20, v20, v4, v5\n"
    "v_max3_f32 v20, v20, v6, v7\n"
    "v_max3_f32 v20, v20, v12, v13\n"
    "v_max3_f32 v20, v20, v14, v15\n"
    "v_max3_f32 v52, v52, v36, v37\n"
    "v_max3_f32 v52, v52, v38, v39\n"
    "v_max3_f32 v52, v52, v44, v45\n"
    "v_max3_f32 v52, v52, v46, v47\n"
    "v_max3_f32 v84, v84, v68, v69\n"
    "v_max3_f32 v84, v84, v70, v71\n"
    "v_max3_f32 v84, v84, v76, v77\n"
    "v_max3_f32 v84, v84, v78, v79\n"
    "v_max3_f32 v116, v116, v100, v101\n"
    "v_max3_f32 v116, v116, v102, v103\n"
    "v_max3_f32 v116, v116, v108, v109\n"
    "v_max3_f32 v116, v116, v110, v111\n"
    "s_add_u32 s73, s73, 8192\n"
    "s_cmp_lt_u32 s73, 131072\n"
    "s_cbranch_scc1 LFB1\n"
    "v_mov_b32 v21, v20\n"
    "s_nop 1\n"
    "v_permlane16_swap_b32 v21, v20\n"
    "s_nop 1\n"
    "v_max_f32 v20, v20, v21\n"
    "v_mov_b32 v21, v20\n"
    "s_nop 1\n"
    "v_permlane32_swap_b32 v21, v20\n"
    "s_nop 1\n"
    "v_max_f32 v20, v20, v21\n"
    "v_sub_f32 v23, 0, v20\n"
    "v_sub_f32 v22, 0, v20\n"
    "v_sub_f32 v21, 0, v20\n"
    "v_sub_f32 v20, 0, v20\n"
    "v_mov_b32 v53, v52\n"
    "s_nop 1\n"
    "v_permlane16_swap_b32 v53, v52\n"
    "s_nop 1\n"
    "v_max_f32 v52, v52, v53\n"
    "v_mov_b32 v53, v52\n"
    "s_nop 1\n"
    "v_permlane32_swap_b32 v53, v52\n"
    "s_nop 1\n"
    "v_max_f32 v52, v52, v53\n"
    "v_sub_f32 v55, 0, v52\n"
    "v_sub_f32 v54, 0, v52\n"
    "v_sub_f32 v53, 0, v52\n"
    "v_sub_f32 v52, 0, v52\n"
    "v_mov_b32 v85, v84\n"
    "s_nop 1\n"
    "v_permlane16_swap_b32 v85, v84\n"
    "s_nop 1\n"
    "v_max_f32 v84, v84, v85\n"
    "v_mov_b32 v85, v84\n"
    "s_nop 1\n"
    "v_permlane32_swap_b32 v85, v84\n"
    "s_nop 1\n"
    "v_max_f32 v84, v84, v85\n"
    "v_sub_f32 v87, 0, v84\n"
    "v_sub_f32 v86, 0, v84\n"
    "v_sub_f32 v85, 0, v84\n"
    "v_sub_f32 v84, 0, v84\n"
    "v_mov_b32 v117, v116\n"
    "s_nop 1\n"
    "v_permlane16_swap_b32 v117, v116\n"
    "s_nop 1\n"
    "v_max_f32 v116, v116, v117\n"
    "v_mov_b32 v117, v116\n"
    "s_nop 1\n"
    "v_permlane32_swap_b32 v117, v116\n"
    "s_nop 1\n"
    "v_max_f32 v116, v116, v117\n"
    "v_sub_f32 v119, 0, v116\n"
    "v_sub_f32 v118, 0, v116\n"
    "v_sub_f32 v117, 0, v116\n"
    "v_sub_f32 v116, 0, v116\n"
    "v_mov_b32 v28, 0\n"
    "v_mov_b32 v29, 0\n"
    "v_mov_b32 v30, 0\n"
    "v_mov_b32 v31, 0\n"
    "v_mov_b32 v60, 0\n"
    "v_mov_b32 v61, 0\n"
    "v_mov_b32 v62, 0\n"
    "v_mov_b32 v63, 0\n"
    "v_mov_b32 v224, 0\n"
    "v_mov_b32 v225, 0\n"
    "v_mov_b32 v226, 0\n"
    "v_mov_b32 v227, 0\n"
    "v_mov_b32 v228, 0\n"
    "v_mov_b32 v229, 0\n"
    "v_mov_b32 v230, 0\n"
    "v_mov_b32 v231, 0\n"
    "v_mov_b32 v92, 0\n"
    "v_mov_b32 v93, 0\n"
    "v_mov_b32 v94, 0\n"
    "v_mov_b32 v95, 0\n"
    "v_mov_b32 v124, 0\n"
    "v_mov_b32 v125, 0\n"
    "v_mov_b32 v126, 0\n"
    "v_mov_b32 v127, 0\n"
    "v_mov_b32 v232, 0\n"
    "v_mov_b32 v233, 0\n"
    "v_mov_b32 v234, 0\n"
    "v_mov_b32 v235, 0\n"
    "v_mov_b32 v236, 0\n"
    "v_mov_b32 v237, 0\n"
    "v_mov_b32 v238, 0\n"
    "v_mov_b32 v239, 0\n"
    "v_mov_b32 v128, 0\n"
    "v_mov_b32 v129, 0\n"
    "v_mov_b32 v130, 0\n"
    "v_mov_b32 v131, 0\n"
    "v_mov_b32 v136, 0\n"
    "v_mov_b32 v137, 0\n"
    "v_mov_b32 v138, 0\n"
    "v_mov_b32 v139, 0\n"
    "v_mov_b32 v132, 0\n"
    "v_mov_b32 v133, 0\n"
    "v_mov_b32 v134, 0\n"
    "v_mov_b32 v135, 0\n"
    "v_mov_b32 v140, 0\n"
    "v_mov_b32 v141, 0\n"
    "v_mov_b32 v142, 0\n"
    "v_mov_b32 v143, 0\n"
    "v_mov_b32 v144, 0\n"
    "v_mov_b32 v145, 0\n"
    "v_mov_b32 v146, 0\n"
    "v_mov_b32 v147, 0\n"
    "v_mov_b32 v152, 0\n"
    "v_mov_b32 v153, 0\n"
    "v_mov_b32 v154, 0\n"
    "v_mov_b32 v155, 0\n"
    "v_mov_b32 v148, 0\n"
    "v_mov_b32 v149, 0\n"
    "v_mov_b32 v150, 0\n"
    "v_mov_b32 v151, 0\n"
    "v_mov_b32 v156, 0\n"
    "v_mov_b32 v157, 0\n"
    "v_mov_b32 v158, 0\n"
    "v_mov_b32 v159, 0\n"
    "v_mov_b32 v160, 0\n"
    "v_mov_b32 v161, 0\n"
    "v_mov_b32 v162, 0\n"
    "v_mov_b32 v163, 0\n"
    "v_mov_b32 v168, 0\n"
    "v_mov_b32 v169, 0\n"
    "v_mov_b32 v170, 0\n"
    "v_mov_b32 v171, 0\n"
    "v_mov_b32 v164, 0\n"
    "v_mov_b32 v165, 0\n"
    "v_mov_b32 v166, 0\n"
    "v_mov_b32 v167, 0\n"
    "v_mov_b32 v172, 0\n"
    "v_mov_b32 v173, 0\n"
    "v_mov_b32 v174, 0\n"
    "v_mov_b32 v175, 0\n"
    "v_mov_b32 v176, 0\n"
    "v_mov_b32 v177, 0\n"
    "v_mov_b32 v178, 0\n"
    "v_mov_b32 v179, 0\n"
    "v_mov_b32 v184, 0\n"
    "v_mov_b32 v185, 0\n"
    "v_mov_b32 v186, 0\n"
    "v_mov_b32 v187, 0\n"
    "v_mov_b32 v180, 0\n"
    "v_mov_b32 v181, 0\n"
    "v_mov_b32 v182, 0\n"
    "v_mov_b32 v183, 0\n"
    "v_mov_b32 v188, 0\n"
    "v_mov_b32 v189, 0\n"
    "v_mov_b32 v190, 0\n"
    "v_mov_b32 v191, 0\n"
    "v_mov_b32 v192, 0\n"
    "v_mov_b32 v193, 0\n"
    "v_mov_b32 v194, 0\n"
    "v_mov_b32 v195, 0\n"
    "v_mov_b32 v200, 0\n"
    "v_mov_b32 v201, 0\n"
    "v_mov_b32 v202, 0\n"
    "v_mov_b32 v203, 0\n"
    "v_mov_b32 v196, 0\n"
    "v_mov_b32 v197, 0\n"
    "v_mov_b32 v198, 0\n"
    "v_mov_b32 v199, 0\n"
    "v_mov_b32 v204, 0\n"
    "v_mov_b32 v205, 0\n"
    "v_mov_b32 v206, 0\n"
    "v_mov_b32 v207, 0\n"
    "v_mov_b32 v208, 0\n"
    "v_mov_b32 v209, 0\n"
    "v_mov_b32 v210, 0\n"
    "v_mov_b32 v211, 0\n"
    "v_mov_b32 v216, 0\n"
    "v_mov_b32 v217, 0\n"
    "v_mov_b32 v218, 0\n"
    "v_mov_b32 v219, 0\n"
    "v_mov_b32 v212, 0\n"
    "v_mov_b32 v213, 0\n"
    "v_mov_b32 v214, 0\n"
    "v_mov_b32 v215, 0\n"
    "v_mov_b32 v220, 0\n"
    "v_mov_b32 v221, 0\n"
    "v_mov_b32 v222, 0\n"
    "v_mov_b32 v223, 0\n"
    "v_accvgpr_write_b32 a192, 0\n"
    "v_accvgpr_write_b32 a193, 0\n"
    "v_accvgpr_write_b32 a194, 0\n"
    "v_accvgpr_write_b32 a195, 0\n"
    "v_accvgpr_write_b32 a196, 0\n"
    "v_accvgpr_write_b32 a197, 0\n"
    "v_accvgpr_write_b32 a198, 0\n"
    "v_accvgpr_write_b32 a199, 0\n"
    "v_accvgpr_write_b32 a200, 0\n"
    "v_accvgpr_write_b32 a201, 0\n"
    "v_accvgpr_write_b32 a202, 0\n"
    "v_accvgpr_write_b32 a203, 0\n"
    "v_accvgpr_write_b32 a204, 0\n"
    "v_accvgpr_write_b32 a205, 0\n"
    "v_accvgpr_write_b32 a206, 0\n"
    "v_accvgpr_write_b32 a207, 0\n"
    "s_mov_b32 s73, 0\n"
    "LFB2:\n"
    "s_add_u32 s98, s73, 4096\n"
    "buffer_load_dwordx4 a[32:35], v252, s[44:47], s73 offen offset:0\n"
    "buffer_load_dwordx4 a[36:39], v252, s[44:47], s73 offen offset:64\n"
    "buffer_load_dwordx4 a[40:43], v252, s[44:47], s73 offen offset:128\n"
    "buffer_load_dwordx4 a[44:47], v252, s[44:47], s73 offen offset:192\n"
    "buffer_load_dwordx4 a[48:51], v252, s[44:47], s98 offen offset:0\n"
    "buffer_load_dwordx4 a[52:55], v252, s[44:47], s98 offen offset:64\n"
    "buffer_load_dwordx4 a[56:59], v252, s[44:47], s98 offen offset:128\n"
    "buffer_load_dwordx4 a[60:63], v252, s[44:47], s98 offen offset:192\n"
    "s_add_u32 m0, s82, 0\n"
    "s_add_u32 s70, s73, 0\n"
    "buffer_load_dwordx4 v253, s[28:31], s70 offen lds\n"
    "s_add_u32 m0, s82, 1024\n"
    "s_add_u32 s70, s73, 128\n"
    "buffer_load_dwordx4 v253, s[28:31], s70 offen lds\n"
    "s_add_u32 m0, s82, 2048\n"
    "s_add_u32 s70, s73, 2048\n"
    "buffer_load_dwordx4 v253, s[28:31], s70 offen lds\n"
    "s_add_u32 m0, s82, 3072\n"
    "s_add_u32 s70, s73, 2176\n"
    "buffer_load_dwordx4 v253, s[28:31], s70 offen lds\n"
    "s_add_u32 m0, s82, 4096\n"
    "s_add_u32 s70, s73, 4096\n"
    "buffer_load_dwordx4 v253, s[28:31], s70 offen lds\n"
    "s_add_u32 m0, s82, 5120\n"
    "s_add_u32 s70, s73, 4224\n"
    "buffer_load_dwordx4 v253, s[28:31], s70 offen lds\n"
    "s_add_u32 m0, s82, 6144\n"
    "s_add_u32 s70, s73, 6144\n"
    "buffer_load_dwordx4 v253, s[28:31], s70 offen lds\n"
    "s_add_u32 m0, s82, 7168\n"
    "s_add_u32 s70, s73, 6272\n"
    "buffer_load_dwordx4 v253, s[28:31], s70 offen lds\n"
    "s_waitcnt vmcnt(0)\n"
    "ds_read_b64_tr_b16 a[64:65], v254 offset:0\n"
    "ds_read_b64_tr_b16 a[66:67], v254 offset:4096\n"
    "ds_read_b64_tr_b16 a[68:69], v249 offset:0\n"
    "ds_read_b64_tr_b16 a[70:71], v249 offset:4096\n"
    "ds_read_b64_tr_b16 a[72:73], v254 offset:512\n"
    "ds_read_b64_tr_b16 a[74:75], v254 offset:4608\n"
    "ds_read_b64_tr_b16 a[76:77], v249 offset:512\n"
    "ds_read_b64_tr_b16 a[78:79], v249 offset:4608\n"
    "ds_read_b64_tr_b16 a[80:81], v254 offset:1024\n"
    "ds_read_b64_tr_b16 a[82:83], v254 offset:5120\n"
    "ds_read_b64_tr_b16 a[84:85], v249 offset:1024\n"
    "ds_read_b64_tr_b16 a[86:87], v249 offset:5120\n"
    "ds_read_b64_tr_b16 a[88:89], v254 offset:1536\n"
    "ds_read_b64_tr_b16 a[90:91], v254 offset:5632\n"
    "ds_read_b64_tr_b16 a[92:93], v249 offset:1536\n"
    "ds_read_b64_tr_b16 a[94:95], v249 offset:5632\n"
    "s_waitcnt lgkmcnt(0)\n"
    "v_mfma_f32_16x16x32_bf16 v[4:7], a[32:35], v[0:3], v[20:23]\n"
    "v_mfma_f32_16x16x32_bf16 v[4:7], a[36:39], v[8:11], v[4:7]\n"
    "v_mfma_f32_16x16x32_bf16 v[4:7], a[40:43], v[16:19], v[4:7]\n"
    "v_mfma_f32_16x16x32_bf16 v[4:7], a[44:47], v[24:27], v[4:7]\n"
    "v_mfma_f32_16x16x32_bf16 v[12:15], a[48:51], v[0:3], v[20:23]\n"
    "v_mfma_f32_16x16x32_bf16 v[12:15], a[52:55], v[8:11], v[12:15]\n"
    "v_mfma_f32_16x16x32_bf16 v[12:15], a[56:59], v[16:19], v[12:15]\n"
    "v_mfma_f32_16x16x32_bf16 v[12:15], a[60:63], v[24:27], v[12:15]\n"
    "v_mfma_f32_16x16x32_bf16 v[36:39], a[32:35], v[32:35], v[52:55]\n"
    "v_mfma_f32_16x16x32_bf16 v[36:39], a[36:39], v[40:43], v[36:39]\n"
    "v_mfma_f32_16x16x32_bf16 v[36:39], a[40:43], v[48:51], v[36:39]\n"
    "v_mfma_f32_16x16x32_bf16 v[36:39], a[44:47], v[56:59], v[36:39]\n"
    "v_mfma_f32_16x16x32_bf16 v[44:47], a[48:51], v[32:35], v[52:55]\n"
    "v_mfma_f32_16x16x32_bf16 v[44:47], a[52:55], v[40:43], v[44:47]\n"
    "v_mfma_f32_16x16x32_bf16 v[44:47], a[56:59], v[48:51], v[44:47]\n"
    "v_mfma_f32_16x16x32_bf16 v[44:47], a[60:63], v[56:59], v[44:47]\n"
    "v_mfma_f32_16x16x32_bf16 v[68:71], a[32:35], v[64:67], v[84:87]\n"
    "v_mfma_f32_16x16x32_bf16 v[68:71], a[36:39], v[72:75], v[68:71]\n"
    "v_mfma_f32_16x16x32_bf16 v[68:71], a[40:43], v[80:83], v[68:71]\n"
    "v_mfma_f32_16x16x32_bf16 v[68:71], a[44:47], v[88:91], v[68:71]\n"
    "v_mfma_f32_16x16x32_bf16 v[76:79], a[48:51], v[64:67], v[84:87]\n"
    "v_mfma_f32_16x16x32_bf16 v[76:79], a[52:55], v[72:75], v[76:79]\n"
    "v_mfma_f32_16x16x32_bf16 v[76:79], a[56:59], v[80:83], v[76:79]\n"
    "v_mfma_f32_16x16x32_bf16 v[76:79], a[60:63], v[88:91], v[76:79]\n"
    "v_mfma_f32_16x16x32_bf16 v[100:103], a[32:35], v[96:99], v[116:119]\n"
    "v_mfma_f32_16x16x32_bf16 v[100:103], a[36:39], v[104:107], v[100:103]\n"
    "v_mfma_f32_16x16x32_bf16 v[100:103], a[40:43], v[112:115], v[100:103]\n"
    "v_mfma_f32_16x16x32_bf16 v[100:103], a[44:47], v[120:123], v[100:103]\n"
    "v_mfma_f32_16x16x32_bf16 v[108:111], a[48:51], v[96:99], v[116:119]\n"
    "v_mfma_f32_16x16x32_bf16 v[108:111], a[52:55], v[104:107], v[108:111]\n"
    "v_mfma_f32_16x16x32_bf16 v[108:111], a[56:59], v[112:115], v[108:111]\n"
    "v_mfma_f32_16x16x32_bf16 v[108:111], a[60:63], v[120:123], v[108:111]\n"
    "s_nop 15\n"
    "v_exp_f32 v4, v4\n"
    "v_exp_f32 v5, v5\n"
    "v_exp_f32 v6, v6\n"
    "v_exp_f32 v7, v7\n"
    "v_exp_f32 v12, v12\n"
    "v_exp_f32 v13, v13\n"
    "v_exp_f32 v14, v14\n"
    "v_exp_f32 v15, v15\n"
    "v_exp_f32 v36, v36\n"
    "v_exp_f32 v37, v37\n"
    "v_exp_f32 v38, v38\n"
    "v_exp_f32 v39, v39\n"
    "v_exp_f32 v44, v44\n"
    "v_exp_f32 v45, v45\n"
    "v_exp_f32 v46, v46\n"
    "v_exp_f32 v47, v47\n"
    "v_exp_f32 v68, v68\n"
    "v_exp_f32 v69, v69\n"
    "v_exp_f32 v70, v70\n"
    "v_exp_f32 v71, v71\n"
    "v_exp_f32 v76, v76\n"
    "v_exp_f32 v77, v77\n"
    "v_exp_f32 v78, v78\n"
    "v_exp_f32 v79, v79\n"
    "v_exp_f32 v100, v100\n"
    "v_exp_f32 v101, v101\n"
    "v_exp_f32 v102, v102\n"
    "v_exp_f32 v103, v103\n"
    "v_exp_f32 v108, v108\n"
    "v_exp_f32 v109, v109\n"
    "v_exp_f32 v110, v110\n"
    "v_exp_f32 v111, v111\n"
    "s_nop 1\n"
    "v_cvt_pk_bf16_f32 v4, v4, v5\n"
    "v_cvt_pk_bf16_f32 v5, v6, v7\n"
    "v_cvt_pk_bf16_f32 v6, v12, v13\n"
    "v_cvt_pk_bf16_f32 v7, v14, v15\n"
    "v_cvt_pk_bf16_f32 v36, v36, v37\n"
    "v_cvt_pk_bf16_f32 v37, v38, v39\n"
    "v_cvt_pk_bf16_f32 v38, v44, v45\n"
    "v_cvt_pk_bf16_f32 v39, v46, v47\n"
    "v_cvt_pk_bf16_f32 v68, v68, v69\n"
    "v_cvt_pk_bf16_f32 v69, v70, v71\n"
    "v_cvt_pk_bf16_f32 v70, v76, v77\n"
    "v_cvt_pk_bf16_f32 v71, v78, v79\n"
    "v_cvt_pk_bf16_f32 v100, v100, v101\n"
    "v_cvt_pk_bf16_f32 v101, v102, v103\n"
    "v_cvt_pk_bf16_f32 v102, v108, v109\n"
    "v_cvt_pk_bf16_f32 v103, v110, v111\n"
    "s_nop 3\n"
    "v_mfma_f32_16x16x32_bf16 a[192:195], a[208:211], v[4:7], a[192:195]\n"
    "v_mfma_f32_16x16x32_bf16 a[196:199], a[208:211], v[36:39], a[196:199]\n"
    "v_mfma_f32_16x16x32_bf16 a[200:203], a[208:211], v[68:71], a[200:203]\n"
    "v_mfma_f32_16x16x32_bf16 a[204:207], a[208:211], v[100:103], a[204:207]\n"
    "v_mfma_f32_16x16x32_bf16 v[28:31], a[64:67], v[4:7], v[28:31]\n"
    "v_mfma_f32_16x16x32_bf16 v[60:63], a[64:67], v[36:39], v[60:63]\n"
    "v_mfma_f32_16x16x32_bf16 v[224:227], a[64:67], v[68:71], v[224:227]\n"
    "v_mfma_f32_16x16x32_bf16 v[228:231], a[64:67], v[100:103], v[228:231]\n"
    "v_mfma_f32_16x16x32_bf16 v[92:95], a[68:71], v[4:7], v[92:95]\n"
    "v_mfma_f32_16x16x32_bf16 v[124:127], a[68:71], v[36:39], v[124:127]\n"
    "v_mfma_f32_16x16x32_bf16 v[232:235], a[68:71], v[68:71], v[232:235]\n"
    "v_mfma_f32_16x16x32_bf16 v[236:239], a[68:71], v[100:103], v[236:239]\n"
    "v_mfma_f32_16x16x32_bf16 v[128:131], a[72:75], v[4:7], v[128:131]\n"
    "v_mfma_f32_16x16x32_bf16 v[136:139], a[72:75], v[36:39], v[136:139]\n"
    "v_mfma_f32_16x16x32_bf16 v[132:135], a[72:75], v[68:71], v[132:135]\n"
    "v_mfma_f32_16x16x32_bf16 v[140:143], a[72:75], v[100:103], v[140:143]\n"
    "v_mfma_f32_16x16x32_bf16 v[144:147], a[76:79], v[4:7], v[144:147]\n"
    "v_mfma_f32_16x16x32_bf16 v[152:155], a[76:79], v[36:39], v[152:155]\n"
    "v_mfma_f32_16x16x32_bf16 v[148:151], a[76:79], v[68:71], v[148:151]\n"
    "v_mfma_f32_16x16x32_bf16 v[156:159], a[76:79], v[100:103], v[156:159]\n"
    "v_mfma_f32_16x16x32_bf16 v[160:163], a[80:83], v[4:7], v[160:163]\n"
    "v_mfma_f32_16x16x32_bf16 v[168:171], a[80:83], v[36:39], v[168:171]\n"
    "v_mfma_f32_16x16x32_bf16 v[164:167], a[80:83], v[68:71], v[164:167]\n"
    "v_mfma_f32_16x16x32_bf16 v[172:175], a[80:83], v[100:103], v[172:175]\n"
    "v_mfma_f32_16x16x32_bf16 v[176:179], a[84:87], v[4:7], v[176:179]\n"
    "v_mfma_f32_16x16x32_bf16 v[184:187], a[84:87], v[36:39], v[184:187]\n"
    "v_mfma_f32_16x16x32_bf16 v[180:183], a[84:87], v[68:71], v[180:183]\n"
    "v_mfma_f32_16x16x32_bf16 v[188:191], a[84:87], v[100:103], v[188:191]\n"
    "v_mfma_f32_16x16x32_bf16 v[192:195], a[88:91], v[4:7], v[192:195]\n"
    "v_mfma_f32_16x16x32_bf16 v[200:203], a[88:91], v[36:39], v[200:203]\n"
    "v_mfma_f32_16x16x32_bf16 v[196:199], a[88:91], v[68:71], v[196:199]\n"
    "v_mfma_f32_16x16x32_bf16 v[204:207], a[88:91], v[100:103], v[204:207]\n"
    "v_mfma_f32_16x16x32_bf16 v[208:211], a[92:95], v[4:7], v[208:211]\n"
    "v_mfma_f32_16x16x32_bf16 v[216:219], a[92:95], v[36:39], v[216:219]\n"
    "v_mfma_f32_16x16x32_bf16 v[212:215], a[92:95], v[68:71], v[212:215]\n"
    "v_mfma_f32_16x16x32_bf16 v[220:223], a[92:95], v[100:103], v[220:223]\n"
    "s_nop 15\n"
    "s_add_u32 s73, s73, 8192\n"
    "s_cmp_lt_u32 s73, 131072\n"
    "s_cbranch_scc1 LFB2\n"
    "v_accvgpr_read_b32 v20, a192\n"
    "v_accvgpr_read_b32 v21, a196\n"
    "v_accvgpr_read_b32 v22, a200\n"
    "v_accvgpr_read_b32 v23, a204\n"
    "v_mov_b32 v68, 0\n"
    "v_mov_b32 v69, 0\n"
    "v_mov_b32 v70, 0\n"
    "v_mov_b32 v71, 0\n"
    "v_mov_b32 v100, 0\n"
    "v_mov_b32 v101, 0\n"
    "v_mov_b32 v102, 0\n"
    "v_mov_b32 v103, 0\n"
    "s_nop 3\n"
    "s_branch LFB_r\n"
    "LSPO:\n"
    "s_mov_b32 s82, 0x40000\n"
    "LSPO_l:\n"
    "s_sleep 1\n"
    "buffer_load_dwordx2 v[252:253], v241, s[56:59], 0 offen sc1\n"
    "s_waitcnt vmcnt(0)\n"
    "v_cmp_ne_u64 vcc, s[68:69], v[252:253]\n"
    "s_nop 1\n"
    "s_cbranch_vccz LSPO_r\n"
    "s_sub_u32 s82, s82, 1\n"
    "s_cmp_lg_u32 s82, 0\n"
    "s_cbranch_scc1 LSPO_l\n"
    "s_branch LSPO_r\n"
    "LPS000:\n"
    "ds_write_b128 v249, v[40:43] offset:0\n"
    "s_branch LPS000_r\n"
    "LPS001:\n"
    "ds_write_b128 v249, v[104:107] offset:2048\n"
    "s_branch LPS001_r\n"
    "LPS002:\n"
    "ds_write_b128 v249, v[76:79] offset:4096\n"
    "s_branch LPS002_r\n"
    "LPS003:\n"
    "ds_write_b128 v249, v[144:147] offset:6144\n"
    "s_branch LPS003_r\n"
    "LPS010:\n"
    "ds_write_b128 v249, v[56:59] offset:1024\n"
    "s_branch LPS010_r\n"
    "LPS011:\n"
    "ds_write_b128 v249, v[120:123] offset:3072\n"
    "s_branch LPS011_r\n"
    "LPS012:\n"
    "ds_write_b128 v249, v[108:111] offset:5120\n"
    "s_branch LPS012_r\n"
    "LPS013:\n"
    "ds_write_b128 v249, v[208:211] offset:7168\n"
    "s_branch LPS013_r\n"
    "LPS100:\n"
    "ds_write_b128 v249, v[72:75] offset:0\n"
    "s_branch LPS100_r\n"
    "LPS101:\n"
    "ds_write_b128 v249, v[12:15] offset:2048\n"
    "s_branch LPS101_r\n"
    "LPS102:\n"
    "ds_write_b128 v249, v[52:55] offset:4096\n"
    "s_branch LPS102_r\n"
    "LPS103:\n"
    "ds_write_b128 v249, v[152:155] offset:6144\n"
    "s_branch LPS103_r\n"
    "LPS110:\n"
    "ds_write_b128 v249, v[88:91] offset:1024\n"
    "s_branch LPS110_r\n"
    "LPS111:\n"
    "ds_write_b128 v249, v[44:47] offset:3072\n"
    "s_branch LPS111_r\n"
    "LPS112:\n"
    "ds_write_b128 v249, v[116:119] offset:5120\n"
    "s_branch LPS112_r\n"
    "LPS113:\n"
    "ds_write_b128 v249, v[216:219] offset:7168\n"
    "s_branch LPS113_r\n"
    "LSPP:\n"
    "s_mov_b32 s82, 0x40000\n"
    "s_mov_b64 s[36:37], 0\n"
    "LSPP_l:\n"
    "buffer_load_dwordx4 v[160:163], v240, s[56:59], s40 offen offset:0 sc1\n"
    "buffer_load_dwordx4 v[192:195], v240, s[56:59], s40 offen offset:1024 sc1\n"
    "buffer_load_dword v72, v240, s[56:59], s47 offen sc1\n"
    "buffer_load_dwordx4 v[60:63], v240, s[56:59], s41 offen offset:0 sc1\n"
    "buffer_load_dwordx4 v[136:139], v240, s[56:59], s41 offen offset:1024 sc1\n"
    "buffer_load_dword v73, v240, s[56:59], s52 offen sc1\n"
    "buffer_load_dwordx4 v[168:171], v240, s[56:59], s42 offen offset:0 sc1\n"
    "buffer_load_dwordx4 v[200:203], v240, s[56:59], s42 offen offset:1024 sc1\n"
    "buffer_load_dword v74, v240, s[56:59], s53 offen sc1\n"
    "buffer_load_dwordx4 v[224:227], v240, s[56:59], s43 offen offset:0 sc1\n"
    "buffer_load_dwordx4 v[132:135], v240, s[56:59], s43 offen offset:1024 sc1\n"
    "buffer_load_dword v75, v240, s[56:59], s54 offen sc1\n"
    "buffer_load_dwordx4 v[164:167], v240, s[56:59], s44 offen offset:0 sc1\n"
    "buffer_load_dwordx4 v[196:199], v240, s[56:59], s44 offen offset:1024 sc1\n"
    "buffer_load_dword v88, v240, s[56:59], s55 offen sc1\n"
    "buffer_load_dwordx4 v[228:231], v240, s[56:59], s45 offen offset:0 sc1\n"
    "buffer_load_dwordx4 v[140:143], v240, s[56:59], s45 offen offset:1024 sc1\n"
    "buffer_load_dword v89, v240, s[56:59], s84 offen sc1\n"
    "buffer_load_dwordx4 v[172:175], v240, s[56:59], s46 offen offset:0 sc1\n"
    "buffer_load_dwordx4 v[204:207], v240, s[56:59], s46 offen offset:1024 sc1\n"
    "buffer_load_dword v90, v240, s[56:59], s85 offen sc1\n"
    "s_waitcnt vmcnt(0)\n"
    "v_xad_u32 v185, v160, v161, s68\n"
    "v_xad_u32 v185, v185, v162, v163\n"
    "v_xad_u32 v185, v185, v192, v193\n"
    "v_xad_u32 v185, v185, v194, v195\n"
    "v_xor_b32 v186, v72, v185\n"
    "v_xad_u32 v184, v60, v61, s68\n"
    "v_xad_u32 v184, v184, v62, v63\n"
    "v_xad_u32 v184, v184, v136, v137\n"
    "v_xad_u32 v184, v184, v138, v139\n"
    "v_xor_b32 v184, v73, v184\n"
    "v_or_b32 v186, v186, v184\n"
    "v_xad_u32 v185, v168, v169, s68\n"
    "v_xad_u32 v185, v185, v170, v171\n"
    "v_xad_u32 v185, v185, v200, v201\n"
    "v_xad_u32 v185, v185, v202, v203\n"
    "v_xor_b32 v185, v74, v185\n"
    "v_or_b32 v186, v186, v185\n"
    "v_xad_u32 v184, v224, v225, s68\n"
    "v_xad_u32 v184, v184, v226, v227\n"
    "v_xad_u32 v184, v184, v132, v133\n"
    "v_xad_u32 v184, v184, v134, v135\n"
    "v_xor_b32 v184, v75, v184\n"
    "v_or_b32 v186, v186, v184\n"
    "v_xad_u32 v185, v164, v165, s68\n"
    "v_xad_u32 v185, v185, v166, v167\n"
    "v_xad_u32 v185, v185, v196, v197\n"
    "v_xad_u32 v185, v185, v198, v199\n"
    "v_xor_b32 v185, v88, v185\n"
    "v_or_b32 v186, v186, v185\n"
    "v_xad_u32 v184, v228, v229, s68\n"
    "v_xad_u32 v184, v184, v230, v231\n"
    "v_xad_u32 v184, v184, v140, v141\n"
    "v_xad_u32 v184, v184, v142, v143\n"
    "v_xor_b32 v184, v89, v184\n"
    "v_or_b32 v186, v186, v184\n"
    "v_xad_u32 v185, v172, v173, s68\n"
    "v_xad_u32 v185, v185, v174, v175\n"
    "v_xad_u32 v185, v185, v204, v205\n"
    "v_xad_u32 v185, v185, v206, v207\n"
    "v_xor_b32 v185, v90, v185\n"
    "v_or_b32 v186, v186, v185\n"
    "v_cmp_ne_u32 vcc, 0, v186\n"
    "s_nop 1\n"
    "s_cbranch_vccz LACC\n"
    "s_sub_u32 s82, s82, 1\n"
    "s_cmp_lg_u32 s82, 0\n"
    "s_cbranch_scc1 LSPP_l\n"
    "s_branch LACC\n"
    "LEND:\n"
    :: "s"(ka), "s"(bid), "s"(w) : "v0","v1","v2","v3","v4","v5","v6","v7","v8","v9","v10","v11","v12","v13","v14","v15","v16","v17","v18","v19","v20","v21","v22","v23","v24","v25","v26","v27","v28","v29","v30","v31","v32","v33","v34","v35","v36","v37","v38","v39","v40","v41","v42","v43","v44","v45","v46","v47","v48","v49","v50","v51","v52","v53","v54","v55","v56","v57","v58","v59","v60","v61","v62","v63","v64","v65","v66","v67","v68","v69","v70","v71","v72","v73","v74","v75","v76","v77","v78","v79","v80","v81","v82","v83","v84","v85","v86","v87","v88","v89","v90","v91","v92","v93","v94","v95","v96","v97","v98","v99","v100","v101","v102","v103","v104","v105","v106","v107","v108","v109","v110","v111","v112","v113","v114","v115","v116","v117","v118","v119","v120","v121","v122","v123","v124","v125","v126","v127","v128","v129","v130","v131","v132","v133","v134","v135","v136","v137","v138","v139","v140","v141","v142","v143","v144","v145","v146","v147","v148","v149","v150","v151","v152","v153","v154","v155","v156","v157","v158","v159","v160","v161","v162","v163","v164","v165","v166","v167","v168","v169","v170","v171","v172","v173","v174","v175","v176","v177","v178","v179","v180","v181","v182","v183","v184","v185","v186","v187","v188","v189","v190","v191","v192","v193","v194","v195","v196","v197","v198","v199","v200","v201","v202","v203","v204","v205","v206","v207","v208","v209","v210","v211","v212","v213","v214","v215","v216","v217","v218","v219","v220","v221","v222","v223","v224","v225","v226","v227","v228","v229","v230","v231","v232","v233","v234","v235","v236","v237","v238","v239","v240","v241","v242","v243","v244","v245","v246","v247","v248","v249","v250","v251","v252","v253","v254","v255","a0","a1","a2","a3","a4","a5","a6","a7","a8","a9","a10","a11","a12","a13","a14","a15","a16","a17","a18","a19","a20","a21","a22","a23","a24","a25","a26","a27","a28","a29","a30","a31","a32","a33","a34","a35","a36","a37","a38","a39","a40","a41","a42","a43","a44","a45","a46","a47","a48","a49","a50","a51","a52","a53","a54","a55","a56","a57","a58","a59","a60","a61","a62","a63","a64","a65","a66","a67","a68","a69","a70","a71","a72","a73","a74","a75","a76","a77","a78","a79","a80","a81","a82","a83","a84","a85","a86","a87","a88","a89","a90","a91","a92","a93","a94","a95","a96","a97","a98","a99","a100","a101","a102","a103","a104","a105","a106","a107","a108","a109","a110","a111","a112","a113","a114","a115","a116","a117","a118","a119","a120","a121","a122","a123","a124","a125","a126","a127","a128","a129","a130","a131","a132","a133","a134","a135","a136","a137","a138","a139","a140","a141","a142","a143","a144","a145","a146","a147","a148","a149","a150","a151","a152","a153","a154","a155","a156","a157","a158","a159","a160","a161","a162","a163","a164","a165","a166","a167","a168","a169","a170","a171","a172","a173","a174","a175","a176","a177","a178","a179","a180","a181","a182","a183","a184","a185","a186","a187","a188","a189","a190","a191","a192","a193","a194","a195","a196","a197","a198","a199","a200","a201","a202","a203","a204","a205","a206","a207","a208","a209","a210","a211","a212","a213","a214","a215","a216","a217","a218","a219","a220","a221","a222","a223","a224","a225","a226","a227","a228","a229","a230","a231","a232","a233","a234","a235","a236","a237","a238","a239","a240","a241","a242","a243","a244","a245","a246","a247","a248","a249","a250","a251","a252","a253","a254","a255","s8","s9","s10","s11","s12","s13","s14","s15","s16","s17","s18","s19","s20","s21","s22","s23","s24","s25","s26","s27","s28","s29","s30","s31","s34","s35","s36","s37","s38","s39","s40","s41","s42","s43","s44","s45","s46","s47","s48","s49","s50","s51","s52","s53","s54","s55","s56","s57","s58","s59","s60","s61","s62","s63","s64","s65","s66","s67","s68","s69","s70","s71","s72","s73","s74","s75","s76","s77","s78","s79","s80","s81","s82","s83","s84","s85","s86","s87","s88","s89","s90","s91","s92","s93","s94","s95","s96","s97","s98","s99","vcc","scc","memory");
}
extern "C" void kernel_launch(void* const* d_in, const int* in_sizes, int n_in, void* d_out, int out_size, void* d_ws, size_t ws_size, hipStream_t stream) {
  (void)in_sizes; (void)n_in; (void)out_size; (void)ws_size;
  (void)hipFuncSetAttribute((const void*)fused_kernel, hipFuncAttributeMaxDynamicSharedMemorySize, 163840);
  hipLaunchKernelGGL(fused_kernel, dim3(256), dim3(256), 163840, stream, d_in[0], d_in[1], d_in[2], d_in[3], d_in[4], d_in[6], d_in[7], d_in[8], d_out, d_ws);
}
```
